```python
import torch

def _softmax(x, axis=-1):
    return torch.softmax(x, dim=axis)
def _gelu(x):
    return torch.nn.functional.gelu(x, approximate="tanh")
def _silu(x):
    return torch.nn.functional.silu(x)
def _sigmoid(x):
    return torch.sigmoid(x)
def _layernorm(x, axis=-1, eps=1e-5):
    m = x.mean(dim=axis, keepdim=True)
    v = x.var(dim=axis, keepdim=True, unbiased=False)
    return (x - m) / torch.sqrt(v + eps)
def _scatter_add(tgt, idx, src, axis=0):
    return tgt.clone().index_add_(axis, idx, src)
def _segment_sum(src, seg, n):
    out = torch.zeros((n,) + src.shape[1:], dtype=src.dtype)
    return out.index_add_(0, seg, src)
def _pad(x, pw):
    flat = tuple(v for pair in reversed(pw) for v in pair)
    return torch.nn.functional.pad(x, flat)

kernel_name = 'sequential_preLN__grouped_kv_mqa__dense_activation_squared_relu__prompt'

def setup_inputs():

    _w = (lambda a: (lambda t: t.to(torch.bfloat16) if t.is_floating_point() else t)(torch.as_tensor(a)))
    def _A_setup():
        B, S, D, H = 1, 2048, 512, 32
        Dh = D // H
        return {
            "x": _w((lambda *s: torch.randn(*s, dtype=torch.bfloat16) * 0.05)(B, S, D)),
            "wq": _w((lambda *s: torch.randn(*s, dtype=torch.bfloat16) * 0.05)(D, H * Dh)),
            "wk": _w((lambda *s: torch.randn(*s, dtype=torch.bfloat16) * 0.05)(D, Dh)),
            "wv": _w((lambda *s: torch.randn(*s, dtype=torch.bfloat16) * 0.05)(D, Dh)),
            "wo": _w((lambda *s: torch.randn(*s, dtype=torch.bfloat16) * 0.05)(H * Dh, D)),
        }
    _A_in = _A_setup()

    def _B_setup():
        B, S, D, F = 1, 2048, 512, 1792
        return {
            "x": _w((lambda *s: torch.randn(*s, dtype=torch.bfloat16) * 0.05)(B, S, D)),
            "w1": _w((lambda *s: torch.randn(*s, dtype=torch.bfloat16) * 0.05)(D, F)),
            "b1": _w((lambda *s: torch.randn(*s, dtype=torch.bfloat16) * 0.4)(F)),
            "w2": _w((lambda *s: torch.randn(*s, dtype=torch.bfloat16) * 0.05)(F, D)),
            "b2": _w((lambda *s: torch.randn(*s, dtype=torch.bfloat16) * 1.6)(D)),
        }
    _B_in = _B_setup()

    return {
        "A_x": _A_in["x"],
        "A_wq": _A_in["wq"],
        "A_wk": _A_in["wk"],
        "A_wv": _A_in["wv"],
        "A_wo": _A_in["wo"],
        "B_x": _B_in["x"],
        "B_w1": _B_in["w1"],
        "B_b1": _B_in["b1"],
        "B_w2": _B_in["w2"],
        "B_b2": _B_in["b2"],
    }

def reference(A_x, A_wq, A_wk, A_wv, A_wo, B_x, B_w1, B_b1, B_w2, B_b2):
    def _A_ref(x, wq, wk, wv, wo):
        B, S, D = x.shape
        H = 32
        Dh = D // H
        q = (lambda x, *d: x.permute(*d))((x @ wq).reshape(B, S, H, Dh), 0, 2, 1, 3)
        k = x @ wk
        v = x @ wv
        scores = torch.einsum("bhqd,bkd->bhqk", q, k) * (Dh ** -0.5)
        p = _softmax(scores, axis=-1)
        o = torch.einsum("bhqk,bkd->bhqd", p, v)
        return (lambda x, *d: x.permute(*d))(o, 0, 2, 1, 3).reshape(B, S, H * Dh) @ wo

    def _B_ref(x, w1, b1, w2, b2):
        h = torch.einsum("bsd,df->bsf", x, w1) + b1
        zero = torch.zeros_like(h)
        r = torch.where(h > zero, h, zero)
        h = r * r
        out = torch.einsum("bsf,fd->bsd", h, w2) + b2
        return out

    x = A_x
    h = x + _A_ref(_layernorm(x, axis=-1), A_wq, A_wk, A_wv, A_wo)
    h2 = h + _B_ref(_layernorm(h, axis=-1), B_w1, B_b1, B_w2, B_b2)
    return h2
```

```cpp
#include <hip/hip_runtime.h>
#include <hip/hip_bf16.h>
typedef unsigned short u16;
__device__ inline float bf(u16 v){ return __uint_as_float(((unsigned)v)<<16); }
__device__ inline u16 tobf(float f){ unsigned u=__float_as_uint(f); return (u + 0x7FFF + ((u>>16)&1))>>16; }
constexpr int S=2048,D=512,H=32,DH=16,F=1792;
__global__ void ln_k(const u16* xb, const float* xf, float* y){
  int r=blockIdx.x, t=threadIdx.x; __shared__ float red[512];
  float v = xb? bf(xb[r*D+t]) : xf[r*D+t];
  red[t]=v; __syncthreads();
  for(int s=256;s>0;s>>=1){ if(t<s) red[t]+=red[t+s]; __syncthreads(); }
  float m=red[0]/D; __syncthreads();
  red[t]=(v-m)*(v-m); __syncthreads();
  for(int s=256;s>0;s>>=1){ if(t<s) red[t]+=red[t+s]; __syncthreads(); }
  float var=red[0]/D;
  y[r*D+t]=(v-m)*rsqrtf(var+1e-5f);
}
__global__ void gemm_k(const float* A,const u16* W,float* C,u16* Cb,int K,int N,int mode,const u16* aux,const float* auxf){
  int n=blockIdx.x*64+(threadIdx.x&63); int m0=(blockIdx.y*4+(threadIdx.x>>6))*8;
  if(n>=N) return;
  float acc[8]={};
  for(int k=0;k<K;k++){ float w=bf(W[k*N+n]);
    #pragma unroll
    for(int i=0;i<8;i++) acc[i]+=A[(m0+i)*K+k]*w; }
  for(int i=0;i<8;i++){ int m=m0+i; float v=acc[i];
    if(mode==1) v+=bf(aux[m*N+n]);
    if(mode==2){ v+=bf(aux[n]); v=v>0?v*v:0; }
    if(mode==3){ v+=bf(aux[n])+auxf[m*N+n]; Cb[m*N+n]=tobf(v);} else C[m*N+n]=v; }
}
__global__ void attn_k(const float* q,const float* k,const float* v,float* o){
  int row=blockIdx.x*64+threadIdx.x;
  float qq[16],acc[16]; for(int d=0;d<16;d++){qq[d]=q[row*16+d]*0.25f;acc[d]=0;}
  float m=-1e30f,l=0;
  for(int j=0;j<S;j++){ float s=0;
    #pragma unroll
    for(int d=0;d<16;d++) s+=qq[d]*k[j*16+d];
    float mn=fmaxf(m,s); float a=__expf(m-mn), p=__expf(s-mn); l=l*a+p;
    #pragma unroll
    for(int d=0;d<16;d++) acc[d]=acc[d]*a+p*v[j*16+d];
    m=mn; }
  for(int d=0;d<16;d++) o[row*16+d]=acc[d]/l;
}
extern "C" void kernel_launch(void* const* d_in, const int* in_sizes, int n_in, void* d_out, int out_size, void* d_ws, size_t ws_size, hipStream_t stream){
  const u16* x=(const u16*)d_in[0],*wq=(const u16*)d_in[1],*wk=(const u16*)d_in[2],*wv=(const u16*)d_in[3],*wo=(const u16*)d_in[4];
  const u16* w1=(const u16*)d_in[6],*b1=(const u16*)d_in[7],*w2=(const u16*)d_in[8],*b2=(const u16*)d_in[9];
  float* ws=(float*)d_ws; float* xn=ws; float* q=xn+S*D; float* k=q+S*D; float* v=k+S*16; float* o=v+S*16; float* h=o+S*D; float* hn=h+S*D; float* h1=hn+S*D;
  ln_k<<<S,512,0,stream>>>(x,nullptr,xn);
  gemm_k<<<dim3(8,64),256,0,stream>>>(xn,wq,q,nullptr,D,D,0,nullptr,nullptr);
  gemm_k<<<dim3(1,64),256,0,stream>>>(xn,wk,k,nullptr,D,16,0,nullptr,nullptr);
  gemm_k<<<dim3(1,64),256,0,stream>>>(xn,wv,v,nullptr,D,16,0,nullptr,nullptr);
  attn_k<<<S*H/64,64,0,stream>>>(q,k,v,o);
  gemm_k<<<dim3(8,64),256,0,stream>>>(o,wo,h,nullptr,D,D,1,x,nullptr);
  ln_k<<<S,512,0,stream>>>(nullptr,h,hn);
  gemm_k<<<dim3(28,64),256,0,stream>>>(hn,w1,h1,nullptr,D,F,2,b1,nullptr);
  gemm_k<<<dim3(8,64),256,0,stream>>>(h1,w2,nullptr,(u16*)d_out,F,D,3,b2,h);
}
```

```cpp
#include <hip/hip_runtime.h>

struct Srd { const void* p; unsigned n; unsigned f; };
struct Args { Srd d[11]; };

__global__ void __launch_bounds__(512, 2) block_kernel(Args a) {
  __shared__ __attribute__((aligned(16))) char smem[163840];
  asm volatile("" :: "s"((unsigned)(size_t)smem) : "memory");
  unsigned tid = threadIdx.x;
  asm volatile(
    "; ---- setup\n"
    "s_load_dwordx16 s[4:19], %1, 0\n"
    "s_load_dwordx4 s[44:47], %1, 160\n"
    "s_load_dwordx16 s[20:35], %1, 64\n"
    "s_load_dwordx8 s[36:43], %1, 128\n"
    "s_mov_b32 s48, %2\n"
    "v_lshrrev_b32 v1, 6, v0\n"
    "v_and_b32 v0, 63, v0\n"
    "v_readfirstlane_b32 s49, v1\n"
    "v_lshlrev_b32 v1, 4, v0\n"
    "s_and_b32 s50, s48, 7\n"
    "s_bfe_u32 s51, s48, 0x30003\n"
    "s_lshr_b32 s98, s48, 6\n"
    "s_lshl2_add_u32 s52, s50, s98\n"
    "s_lshl_b32 s66, s51, 7\n"
    "s_and_b32 s53, s49, 3\n"
    "s_lshr_b32 s54, s49, 2\n"
    "s_cmp_eq_u32 s54, 0\n"
    "s_cbranch_scc0 .LB1_%=\n"
    "; ================ waves 0..3: Q projection, tokens 0..31, then output projection + communication\n"
    "; ---- first batch: four rows of the K/V unit and a quarter of Wk or Wv per wave\n"
    "s_setprio 3\n"
    "s_and_b32 s61, s51, 3\n"
    "s_lshl2_add_u32 s61, s61, s53\n"
    "s_xor_b32 s59, s61, 8\n"
    "s_mul_i32 s59, s59, 4160\n"
    "s_add_u32 m0, s59, 16640\n"
    "s_lshl4_add_u32 s58, s52, s61\n"
    "s_lshl_b32 s58, s58, 12\n"
    "s_lshl_b32 s59, s53, 12\n"
    "s_and_b32 s60, s53, 1\n"
    "s_lshl2_add_u32 s60, s60, s53\n"
    "s_lshr_b32 s60, s60, 1\n"
    "s_mul_i32 s60, s60, 4160\n"
    "s_lshl_b32 s61, s61, 2\n"
    "s_cmp_ge_u32 s51, 4\n"
    "s_waitcnt lgkmcnt(0)\n"
    "s_cselect_b64 s[12:13], s[16:17], s[12:13]\n"
    "buffer_load_dwordx4 v1, s[4:7], s58 offen lds\n"
    "s_add_u32 m0, m0, 16\n"
    "s_nop 0\n"
    "buffer_load_dwordx4 v1, s[4:7], s58 offen offset:1024 lds\n"
    "s_add_u32 m0, m0, 16\n"
    "s_nop 0\n"
    "buffer_load_dwordx4 v1, s[4:7], s58 offen offset:2048 lds\n"
    "s_add_u32 m0, m0, 16\n"
    "s_nop 0\n"
    "buffer_load_dwordx4 v1, s[4:7], s58 offen offset:3072 lds\n"
    "s_mov_b32 m0, s60\n"
    "s_nop 0\n"
    "buffer_load_dwordx4 v1, s[12:15], s59 offen lds\n"
    "s_add_u32 m0, m0, 16\n"
    "s_nop 0\n"
    "buffer_load_dwordx4 v1, s[12:15], s59 offen offset:1024 lds\n"
    "s_add_u32 m0, m0, 16\n"
    "s_nop 0\n"
    "buffer_load_dwordx4 v1, s[12:15], s59 offen offset:2048 lds\n"
    "s_add_u32 m0, m0, 16\n"
    "s_nop 0\n"
    "buffer_load_dwordx4 v1, s[12:15], s59 offen offset:3072 lds\n"
    "s_barrier\n"
    "s_branch .LAmain5_%=\n"
    ".LB1_%=:\n"
    "; ================ waves 4..7: K/V unit, loading, tokens 32..63\n"
    "s_setprio 2\n"
    "v_mov_b32 v20, 0x3f803f80\n"
    "v_mov_b32 v21, 0x3f803f80\n"
    "v_mov_b32 v22, 0x3f803f80\n"
    "v_mov_b32 v23, 0x3f803f80\n"
    "v_mov_b32 v24, 0\n"
    "v_lshlrev_b32 v16, 2, v0\n"
    "v_add_u32 v16, 150336, v16\n"
    "ds_write_b32 v16, v24\n"
    "v_mul_u32_u24 v28, 2080, v0\n"
    "v_mov_b32 v25, 0\n"
    "v_mov_b32 v26, 0\n"
    "v_mov_b32 v27, 0\n"
    "ds_write_b128 v28, v[20:23] offset:1024\n"
    "ds_write_b128 v28, v[24:27] offset:2064\n"
    "s_cmp_eq_u32 s49, 7\n"
    "s_cselect_b32 s59, 0, 0x40000000\n"
    "v_mov_b32 v29, s59\n"
    "s_lshr_b32 s58, s48, 3\n"
    "s_lshl_b32 s58, s58, 11\n"
    "s_add_u32 s58, s58, s59\n"
    "v_and_b32 v30, 15, v0\n"
    "v_lshl_add_u32 v30, v30, 7, s58\n"
    "v_min_u32 v31, 19, v0\n"
    "v_lshl_add_u32 v31, v31, 7, s59\n"
    "s_lshl3_add_u32 s60, s52, s51\n"
    "s_mul_i32 s60, s60, 2560\n"
    "s_add_u32 s60, s60, 1048576\n"
    "s_waitcnt lgkmcnt(0)\n"
    "; ---- wave 7: the epoch word, and one sixteen-line share of this kernel's code towards the L2\n"
    "buffer_load_dwordx2 v[2:3], v29, s[44:47], 0 offen sc1\n"
    "buffer_load_dword v6, v31, s[44:47], s60 offen sc1\n"
    "s_getpc_b64 s[12:13]\n"
    ".Lpc_%=:\n"
    "s_mov_b32 s14, .Lend_%=-.Lpc_%=\n"
    "s_mov_b32 s15, 0x20000\n"
    "buffer_load_dword v6, v30, s[12:15], 0 offen\n"
    "s_barrier\n"
    "s_lshl3_add_u32 s55, s52, s51\n"
    "s_lshl_b32 s67, s52, 1\n"
    "s_cmp_eq_u32 s49, 7\n"
    "s_cselect_b32 s63, 0x40000000, 0\n"
    "s_lshl1_add_u32 s99, s98, s49\n"
    "s_and_b32 s99, s99, 7\n"
    "s_mov_b32 s3, 0\n"
    "s_add_u32 s58, s53, 150336\n"
    "v_mov_b32 v7, s58\n"
    "s_cmp_eq_u32 s49, 7\n"
    "s_cbranch_scc1 .Lunit59_%=\n"
    "; ---- waves 4..6 fetch one of the other x tiles each through registers and compute its statistics\n"
    "s_add_u32 s61, s51, s53\n"
    "s_add_u32 s61, s61, 1\n"
    "s_and_b32 s61, s61, 3\n"
    "s_lshl2_add_u32 s60, s52, s61\n"
    "s_lshl_b32 s60, s60, 14\n"
    "s_add_u32 s58, s60, 8192\n"
    "v_lshrrev_b32 v24, 3, v0\n"
    "v_and_b32 v25, 0x70, v1\n"
    "v_lshl_add_u32 v16, v24, 10, v25\n"
    "s_xor_b32 s59, s61, 2\n"
    "s_mul_i32 s59, s59, 16640\n"
    "s_add_u32 s59, s59, 16640\n"
    "v_mul_u32_u24 v26, 1040, v24\n"
    "v_add3_u32 v105, v25, v26, s59\n"
    "s_lshl_b32 s59, s61, 7\n"
    "s_add_u32 s59, s59, 149760\n"
    "v_lshl_add_u32 v106, v24, 3, s59\n"
    "buffer_load_dwordx4 v[128:131], v16, s[4:7], s60 offen offset:0\n"
    "buffer_load_dwordx4 v[132:135], v16, s[4:7], s60 offen offset:128\n"
    "buffer_load_dwordx4 v[136:139], v16, s[4:7], s60 offen offset:256\n"
    "buffer_load_dwordx4 v[140:143], v16, s[4:7], s60 offen offset:384\n"
    "buffer_load_dwordx4 v[144:147], v16, s[4:7], s60 offen offset:512\n"
    "buffer_load_dwordx4 v[148:151], v16, s[4:7], s60 offen offset:640\n"
    "buffer_load_dwordx4 v[152:155], v16, s[4:7], s60 offen offset:768\n"
    "buffer_load_dwordx4 v[156:159], v16, s[4:7], s60 offen offset:896\n"
    "buffer_load_dwordx4 v[160:163], v16, s[4:7], s58 offen offset:0\n"
    "buffer_load_dwordx4 v[164:167], v16, s[4:7], s58 offen offset:128\n"
    "buffer_load_dwordx4 v[168:171], v16, s[4:7], s58 offen offset:256\n"
    "buffer_load_dwordx4 v[172:175], v16, s[4:7], s58 offen offset:384\n"
    "buffer_load_dwordx4 v[176:179], v16, s[4:7], s58 offen offset:512\n"
    "buffer_load_dwordx4 v[180:183], v16, s[4:7], s58 offen offset:640\n"
    "buffer_load_dwordx4 v[184:187], v16, s[4:7], s58 offen offset:768\n"
    "buffer_load_dwordx4 v[188:191], v16, s[4:7], s58 offen offset:896\n"
    "s_setprio 1\n"
    "s_waitcnt vmcnt(15)\n"
    "ds_write_b128 v105, v[128:131] offset:0\n"
    "s_waitcnt vmcnt(14)\n"
    "ds_write_b128 v105, v[132:135] offset:128\n"
    "s_waitcnt vmcnt(13)\n"
    "ds_write_b128 v105, v[136:139] offset:256\n"
    "s_waitcnt vmcnt(12)\n"
    "ds_write_b128 v105, v[140:143] offset:384\n"
    "s_waitcnt vmcnt(11)\n"
    "ds_write_b128 v105, v[144:147] offset:512\n"
    "s_waitcnt vmcnt(10)\n"
    "ds_write_b128 v105, v[148:151] offset:640\n"
    "s_waitcnt vmcnt(9)\n"
    "ds_write_b128 v105, v[152:155] offset:768\n"
    "s_waitcnt vmcnt(8)\n"
    "ds_write_b128 v105, v[156:159] offset:896\n"
    "s_waitcnt vmcnt(7)\n"
    "ds_write_b128 v105, v[160:163] offset:8320\n"
    "s_waitcnt vmcnt(6)\n"
    "ds_write_b128 v105, v[164:167] offset:8448\n"
    "s_waitcnt vmcnt(5)\n"
    "ds_write_b128 v105, v[168:171] offset:8576\n"
    "s_waitcnt vmcnt(4)\n"
    "ds_write_b128 v105, v[172:175] offset:8704\n"
    "s_waitcnt vmcnt(3)\n"
    "ds_write_b128 v105, v[176:179] offset:8832\n"
    "s_waitcnt vmcnt(2)\n"
    "ds_write_b128 v105, v[180:183] offset:8960\n"
    "s_waitcnt vmcnt(1)\n"
    "ds_write_b128 v105, v[184:187] offset:9088\n"
    "s_waitcnt vmcnt(0)\n"
    "ds_write_b128 v105, v[188:191] offset:9216\n"
    "ds_write_b8 v7, v7 offset:96\n"
    "v_mov_b32 v24, 0\n"
    "v_mov_b32 v25, 0\n"
    "v_dot2c_f32_bf16 v24, v128, v20\n"
    "v_dot2c_f32_bf16 v25, v128, v128\n"
    "v_dot2c_f32_bf16 v24, v129, v20\n"
    "v_dot2c_f32_bf16 v25, v129, v129\n"
    "v_dot2c_f32_bf16 v24, v130, v20\n"
    "v_dot2c_f32_bf16 v25, v130, v130\n"
    "v_dot2c_f32_bf16 v24, v131, v20\n"
    "v_dot2c_f32_bf16 v25, v131, v131\n"
    "v_dot2c_f32_bf16 v24, v132, v20\n"
    "v_dot2c_f32_bf16 v25, v132, v132\n"
    "v_dot2c_f32_bf16 v24, v133, v20\n"
    "v_dot2c_f32_bf16 v25, v133, v133\n"
    "v_dot2c_f32_bf16 v24, v134, v20\n"
    "v_dot2c_f32_bf16 v25, v134, v134\n"
    "v_dot2c_f32_bf16 v24, v135, v20\n"
    "v_dot2c_f32_bf16 v25, v135, v135\n"
    "v_dot2c_f32_bf16 v24, v136, v20\n"
    "v_dot2c_f32_bf16 v25, v136, v136\n"
    "v_dot2c_f32_bf16 v24, v137, v20\n"
    "v_dot2c_f32_bf16 v25, v137, v137\n"
    "v_dot2c_f32_bf16 v24, v138, v20\n"
    "v_dot2c_f32_bf16 v25, v138, v138\n"
    "v_dot2c_f32_bf16 v24, v139, v20\n"
    "v_dot2c_f32_bf16 v25, v139, v139\n"
    "v_dot2c_f32_bf16 v24, v140, v20\n"
    "v_dot2c_f32_bf16 v25, v140, v140\n"
    "v_dot2c_f32_bf16 v24, v141, v20\n"
    "v_dot2c_f32_bf16 v25, v141, v141\n"
    "v_dot2c_f32_bf16 v24, v142, v20\n"
    "v_dot2c_f32_bf16 v25, v142, v142\n"
    "v_dot2c_f32_bf16 v24, v143, v20\n"
    "v_dot2c_f32_bf16 v25, v143, v143\n"
    "v_dot2c_f32_bf16 v24, v144, v20\n"
    "v_dot2c_f32_bf16 v25, v144, v144\n"
    "v_dot2c_f32_bf16 v24, v145, v20\n"
    "v_dot2c_f32_bf16 v25, v145, v145\n"
    "v_dot2c_f32_bf16 v24, v146, v20\n"
    "v_dot2c_f32_bf16 v25, v146, v146\n"
    "v_dot2c_f32_bf16 v24, v147, v20\n"
    "v_dot2c_f32_bf16 v25, v147, v147\n"
    "v_dot2c_f32_bf16 v24, v148, v20\n"
    "v_dot2c_f32_bf16 v25, v148, v148\n"
    "v_dot2c_f32_bf16 v24, v149, v20\n"
    "v_dot2c_f32_bf16 v25, v149, v149\n"
    "v_dot2c_f32_bf16 v24, v150, v20\n"
    "v_dot2c_f32_bf16 v25, v150, v150\n"
    "v_dot2c_f32_bf16 v24, v151, v20\n"
    "v_dot2c_f32_bf16 v25, v151, v151\n"
    "v_dot2c_f32_bf16 v24, v152, v20\n"
    "v_dot2c_f32_bf16 v25, v152, v152\n"
    "v_dot2c_f32_bf16 v24, v153, v20\n"
    "v_dot2c_f32_bf16 v25, v153, v153\n"
    "v_dot2c_f32_bf16 v24, v154, v20\n"
    "v_dot2c_f32_bf16 v25, v154, v154\n"
    "v_dot2c_f32_bf16 v24, v155, v20\n"
    "v_dot2c_f32_bf16 v25, v155, v155\n"
    "v_dot2c_f32_bf16 v24, v156, v20\n"
    "v_dot2c_f32_bf16 v25, v156, v156\n"
    "v_dot2c_f32_bf16 v24, v157, v20\n"
    "v_dot2c_f32_bf16 v25, v157, v157\n"
    "v_dot2c_f32_bf16 v24, v158, v20\n"
    "v_dot2c_f32_bf16 v25, v158, v158\n"
    "v_dot2c_f32_bf16 v24, v159, v20\n"
    "v_dot2c_f32_bf16 v25, v159, v159\n"
    "s_nop 1\n"
    "v_add_f32_dpp v24, v24, v24 quad_perm:[1,0,3,2] row_mask:0xf bank_mask:0xf\n"
    "v_add_f32_dpp v25, v25, v25 quad_perm:[1,0,3,2] row_mask:0xf bank_mask:0xf\n"
    "s_nop 0\n"
    "v_add_f32_dpp v24, v24, v24 quad_perm:[2,3,0,1] row_mask:0xf bank_mask:0xf\n"
    "v_add_f32_dpp v25, v25, v25 quad_perm:[2,3,0,1] row_mask:0xf bank_mask:0xf\n"
    "s_nop 0\n"
    "v_add_f32_dpp v24, v24, v24 row_half_mirror row_mask:0xf bank_mask:0xf\n"
    "v_add_f32_dpp v25, v25, v25 row_half_mirror row_mask:0xf bank_mask:0xf\n"
    "v_mul_f32 v24, 0x3b000000, v24\n"
    "v_mul_f32 v25, 0x3b000000, v25\n"
    "v_fma_f32 v25, -v24, v24, v25\n"
    "v_max_f32 v25, 0, v25\n"
    "v_add_f32 v25, 0x3727c5ac, v25\n"
    "v_rsq_f32 v25, v25\n"
    "s_nop 0\n"
    "ds_write_b64 v106, v[24:25] offset:0\n"
    "v_mov_b32 v24, 0\n"
    "v_mov_b32 v25, 0\n"
    "v_dot2c_f32_bf16 v24, v160, v20\n"
    "v_dot2c_f32_bf16 v25, v160, v160\n"
    "v_dot2c_f32_bf16 v24, v161, v20\n"
    "v_dot2c_f32_bf16 v25, v161, v161\n"
    "v_dot2c_f32_bf16 v24, v162, v20\n"
    "v_dot2c_f32_bf16 v25, v162, v162\n"
    "v_dot2c_f32_bf16 v24, v163, v20\n"
    "v_dot2c_f32_bf16 v25, v163, v163\n"
    "v_dot2c_f32_bf16 v24, v164, v20\n"
    "v_dot2c_f32_bf16 v25, v164, v164\n"
    "v_dot2c_f32_bf16 v24, v165, v20\n"
    "v_dot2c_f32_bf16 v25, v165, v165\n"
    "v_dot2c_f32_bf16 v24, v166, v20\n"
    "v_dot2c_f32_bf16 v25, v166, v166\n"
    "v_dot2c_f32_bf16 v24, v167, v20\n"
    "v_dot2c_f32_bf16 v25, v167, v167\n"
    "v_dot2c_f32_bf16 v24, v168, v20\n"
    "v_dot2c_f32_bf16 v25, v168, v168\n"
    "v_dot2c_f32_bf16 v24, v169, v20\n"
    "v_dot2c_f32_bf16 v25, v169, v169\n"
    "v_dot2c_f32_bf16 v24, v170, v20\n"
    "v_dot2c_f32_bf16 v25, v170, v170\n"
    "v_dot2c_f32_bf16 v24, v171, v20\n"
    "v_dot2c_f32_bf16 v25, v171, v171\n"
    "v_dot2c_f32_bf16 v24, v172, v20\n"
    "v_dot2c_f32_bf16 v25, v172, v172\n"
    "v_dot2c_f32_bf16 v24, v173, v20\n"
    "v_dot2c_f32_bf16 v25, v173, v173\n"
    "v_dot2c_f32_bf16 v24, v174, v20\n"
    "v_dot2c_f32_bf16 v25, v174, v174\n"
    "v_dot2c_f32_bf16 v24, v175, v20\n"
    "v_dot2c_f32_bf16 v25, v175, v175\n"
    "v_dot2c_f32_bf16 v24, v176, v20\n"
    "v_dot2c_f32_bf16 v25, v176, v176\n"
    "v_dot2c_f32_bf16 v24, v177, v20\n"
    "v_dot2c_f32_bf16 v25, v177, v177\n"
    "v_dot2c_f32_bf16 v24, v178, v20\n"
    "v_dot2c_f32_bf16 v25, v178, v178\n"
    "v_dot2c_f32_bf16 v24, v179, v20\n"
    "v_dot2c_f32_bf16 v25, v179, v179\n"
    "v_dot2c_f32_bf16 v24, v180, v20\n"
    "v_dot2c_f32_bf16 v25, v180, v180\n"
    "v_dot2c_f32_bf16 v24, v181, v20\n"
    "v_dot2c_f32_bf16 v25, v181, v181\n"
    "v_dot2c_f32_bf16 v24, v182, v20\n"
    "v_dot2c_f32_bf16 v25, v182, v182\n"
    "v_dot2c_f32_bf16 v24, v183, v20\n"
    "v_dot2c_f32_bf16 v25, v183, v183\n"
    "v_dot2c_f32_bf16 v24, v184, v20\n"
    "v_dot2c_f32_bf16 v25, v184, v184\n"
    "v_dot2c_f32_bf16 v24, v185, v20\n"
    "v_dot2c_f32_bf16 v25, v185, v185\n"
    "v_dot2c_f32_bf16 v24, v186, v20\n"
    "v_dot2c_f32_bf16 v25, v186, v186\n"
    "v_dot2c_f32_bf16 v24, v187, v20\n"
    "v_dot2c_f32_bf16 v25, v187, v187\n"
    "v_dot2c_f32_bf16 v24, v188, v20\n"
    "v_dot2c_f32_bf16 v25, v188, v188\n"
    "v_dot2c_f32_bf16 v24, v189, v20\n"
    "v_dot2c_f32_bf16 v25, v189, v189\n"
    "v_dot2c_f32_bf16 v24, v190, v20\n"
    "v_dot2c_f32_bf16 v25, v190, v190\n"
    "v_dot2c_f32_bf16 v24, v191, v20\n"
    "v_dot2c_f32_bf16 v25, v191, v191\n"
    "s_nop 1\n"
    "v_add_f32_dpp v24, v24, v24 quad_perm:[1,0,3,2] row_mask:0xf bank_mask:0xf\n"
    "v_add_f32_dpp v25, v25, v25 quad_perm:[1,0,3,2] row_mask:0xf bank_mask:0xf\n"
    "s_nop 0\n"
    "v_add_f32_dpp v24, v24, v24 quad_perm:[2,3,0,1] row_mask:0xf bank_mask:0xf\n"
    "v_add_f32_dpp v25, v25, v25 quad_perm:[2,3,0,1] row_mask:0xf bank_mask:0xf\n"
    "s_nop 0\n"
    "v_add_f32_dpp v24, v24, v24 row_half_mirror row_mask:0xf bank_mask:0xf\n"
    "v_add_f32_dpp v25, v25, v25 row_half_mirror row_mask:0xf bank_mask:0xf\n"
    "v_mul_f32 v24, 0x3b000000, v24\n"
    "v_mul_f32 v25, 0x3b000000, v25\n"
    "v_fma_f32 v25, -v24, v24, v25\n"
    "v_max_f32 v25, 0, v25\n"
    "v_add_f32 v25, 0x3727c5ac, v25\n"
    "v_rsq_f32 v25, v25\n"
    "s_nop 0\n"
    "ds_write_b64 v106, v[24:25] offset:64\n"
    "s_mov_b32 s62, 0x100000\n"
    "v_mov_b32 v33, 150480\n"
    "s_waitcnt lgkmcnt(0)\n"
    "s_nop 1\n"
    ".Llw62_%=:\n"
    "ds_read_b32 v32, v33\n"
    "s_waitcnt lgkmcnt(0)\n"
    "v_cmp_ne_u32 vcc, 0x43424140, v32\n"
    "s_cbranch_vccz .Llwd63_%=\n"
    "s_sleep 1\n"
    "s_sub_u32 s62, s62, 1\n"
    "s_cbranch_scc0 .Llw62_%=\n"
    ".Llwd63_%=:\n"
    "v_mov_b32 v2, 150448\n"
    "ds_read_b64 v[2:3], v2\n"
    "s_waitcnt lgkmcnt(0)\n"
    "v_readfirstlane_b32 s56, v2\n"
    "v_readfirstlane_b32 s57, v3\n"
    "s_setprio 2\n"
    "s_branch .Ljoin261_%=\n"
    ".Lunit59_%=:\n"
    "; ---- flag address v4: lanes 0..7 packed replicas, lane 8 own line\n"
    "s_mul_i32 s58, s52, 20480\n"
    "s_lshl3_add_u32 s58, s51, s58\n"
    "s_add_u32 s58, s58, 1051008\n"
    "v_mul_u32_u24 v4, 2560, v0\n"
    "v_add_u32 v4, s58, v4\n"
    "s_mul_i32 s58, s55, 2560\n"
    "s_add_u32 s58, s58, 1050624\n"
    "v_mov_b32 v24, s58\n"
    "v_cmp_eq_u32 vcc, 8, v0\n"
    "v_mov_b32 v25, 0x40000000\n"
    "s_nop 0\n"
    "v_cndmask_b32 v4, v4, v24, vcc\n"
    "v_cmp_gt_u32 vcc, 9, v0\n"
    "s_and_b32 s58, s51, 3\n"
    "v_and_b32 v26, 15, v0\n"
    "v_cndmask_b32 v4, v25, v4, vcc\n"
    "v_lshrrev_b32 v27, 4, v0\n"
    "v_and_b32 v28, 1, v27\n"
    "v_lshl_add_u32 v28, v28, 2, v27\n"
    "v_lshrrev_b32 v28, 1, v28\n"
    "v_mul_u32_u24 v28, 4160, v28\n"
    "v_lshl_add_u32 v28, v26, 3, v28\n"
    "s_xor_b32 s59, s58, 2\n"
    "s_mul_i32 s59, s59, 16640\n"
    "s_add_u32 s59, s59, 16640\n"
    "v_mul_u32_u24 v29, 1040, v26\n"
    "v_lshlrev_b32 v30, 8, v27\n"
    "v_add3_u32 v29, v29, v30, s59\n"
    "s_lshr_b32 s60, s58, 1\n"
    "s_add_u32 s60, s60, s67\n"
    "s_lshl_b32 s60, s60, 11\n"
    "s_add_u32 s60, s60, 327680\n"
    "s_and_b32 s61, s58, 1\n"
    "v_and_b32 v30, 1, v27\n"
    "v_lshlrev_b32 v30, 3, v30\n"
    "v_lshl_add_u32 v30, v26, 4, v30\n"
    "v_lshrrev_b32 v31, 5, v0\n"
    "v_mov_b32 v49, 150336\n"
    "s_lshl_b32 s59, s58, 7\n"
    "s_add_u32 s59, s59, 149760\n"
    "s_cmp_ge_u32 s51, 4\n"
    "s_cbranch_scc1 .Lvunit64_%=\n"
    "v_lshl_add_u32 v48, v26, 3, s59\n"
    "v_lshl_add_u32 v30, v31, 9, v30\n"
    "s_lshl_b32 s61, s61, 8\n"
    "s_add_u32 s60, s60, s61\n"
    "s_waitcnt vmcnt(2)\n"
    "v_readfirstlane_b32 s56, v2\n"
    "v_readfirstlane_b32 s57, v3\n"
    "s_add_u32 s56, s56, 0x7f4a7c15\n"
    "s_addc_u32 s57, s57, 0x9e3779b9\n"
    "v_mov_b32 v2, s56\n"
    "v_mov_b32 v3, s57\n"
    "ds_write_b64 v49, v[2:3] offset:112\n"
    "s_mov_b32 s62, 0x100000\n"
    "v_mov_b32 v53, 150336\n"
    "s_waitcnt lgkmcnt(0)\n"
    "s_nop 1\n"
    ".Llw65_%=:\n"
    "ds_read_b32 v52, v53\n"
    "s_waitcnt lgkmcnt(0)\n"
    "v_cmp_ne_u32 vcc, 0x43424140, v52\n"
    "s_cbranch_vccz .Llwd66_%=\n"
    "s_sleep 0\n"
    "s_sub_u32 s62, s62, 1\n"
    "s_cbranch_scc0 .Llw65_%=\n"
    ".Llwd66_%=:\n"
    "ds_read_b64_tr_b16 v[128:129], v28 offset:0\n"
    "ds_read_b64_tr_b16 v[130:131], v28 offset:128\n"
    "ds_read_b128 v[192:195], v29 offset:0\n"
    "ds_read_b64_tr_b16 v[132:133], v28 offset:256\n"
    "ds_read_b64_tr_b16 v[134:135], v28 offset:384\n"
    "ds_read_b128 v[196:199], v29 offset:16\n"
    "ds_read_b64_tr_b16 v[136:137], v28 offset:512\n"
    "ds_read_b64_tr_b16 v[138:139], v28 offset:640\n"
    "ds_read_b128 v[200:203], v29 offset:32\n"
    "ds_read_b64_tr_b16 v[140:141], v28 offset:768\n"
    "ds_read_b64_tr_b16 v[142:143], v28 offset:896\n"
    "ds_read_b128 v[204:207], v29 offset:48\n"
    "ds_write_b8 v7, v7 offset:96\n"
    "s_waitcnt lgkmcnt(10)\n"
    "v_mfma_f32_16x16x32_bf16 v[32:35], v[128:131], v[192:195], 0\n"
    "v_mfma_f32_16x16x32_bf16 v[36:39], v[128:131], v[20:23], 0\n"
    "ds_read_b64_tr_b16 v[144:145], v28 offset:1040\n"
    "ds_read_b64_tr_b16 v[146:147], v28 offset:1168\n"
    "ds_read_b128 v[208:211], v29 offset:64\n"
    "s_waitcnt lgkmcnt(10)\n"
    "v_mfma_f32_16x16x32_bf16 v[32:35], v[132:135], v[196:199], v[32:35]\n"
    "v_mfma_f32_16x16x32_bf16 v[36:39], v[132:135], v[20:23], v[36:39]\n"
    "ds_read_b64_tr_b16 v[148:149], v28 offset:1296\n"
    "ds_read_b64_tr_b16 v[150:151], v28 offset:1424\n"
    "ds_read_b128 v[212:215], v29 offset:80\n"
    "s_waitcnt lgkmcnt(10)\n"
    "v_mfma_f32_16x16x32_bf16 v[32:35], v[136:139], v[200:203], v[32:35]\n"
    "v_mfma_f32_16x16x32_bf16 v[36:39], v[136:139], v[20:23], v[36:39]\n"
    "ds_read_b64_tr_b16 v[152:153], v28 offset:1552\n"
    "ds_read_b64_tr_b16 v[154:155], v28 offset:1680\n"
    "ds_read_b128 v[216:219], v29 offset:96\n"
    "s_waitcnt lgkmcnt(10)\n"
    "v_mfma_f32_16x16x32_bf16 v[32:35], v[140:143], v[204:207], v[32:35]\n"
    "v_mfma_f32_16x16x32_bf16 v[36:39], v[140:143], v[20:23], v[36:39]\n"
    "ds_read_b64_tr_b16 v[156:157], v28 offset:1808\n"
    "ds_read_b64_tr_b16 v[158:159], v28 offset:1936\n"
    "ds_read_b128 v[220:223], v29 offset:112\n"
    "s_waitcnt lgkmcnt(9)\n"
    "v_mfma_f32_16x16x32_bf16 v[32:35], v[144:147], v[208:211], v[32:35]\n"
    "v_mfma_f32_16x16x32_bf16 v[36:39], v[144:147], v[20:23], v[36:39]\n"
    "ds_read_b64_tr_b16 v[160:161], v28 offset:2080\n"
    "ds_read_b64_tr_b16 v[162:163], v28 offset:2208\n"
    "ds_read_b128 v[224:227], v29 offset:128\n"
    "s_waitcnt lgkmcnt(9)\n"
    "v_mfma_f32_16x16x32_bf16 v[32:35], v[148:151], v[212:215], v[32:35]\n"
    "v_mfma_f32_16x16x32_bf16 v[36:39], v[148:151], v[20:23], v[36:39]\n"
    "ds_read_b64_tr_b16 v[164:165], v28 offset:2336\n"
    "ds_read_b64_tr_b16 v[166:167], v28 offset:2464\n"
    "ds_read_b128 v[228:231], v29 offset:144\n"
    "s_waitcnt lgkmcnt(9)\n"
    "v_mfma_f32_16x16x32_bf16 v[32:35], v[152:155], v[216:219], v[32:35]\n"
    "v_mfma_f32_16x16x32_bf16 v[36:39], v[152:155], v[20:23], v[36:39]\n"
    "ds_read_b64_tr_b16 v[168:169], v28 offset:2592\n"
    "ds_read_b64_tr_b16 v[170:171], v28 offset:2720\n"
    "ds_read_b128 v[232:235], v29 offset:160\n"
    "s_waitcnt lgkmcnt(9)\n"
    "v_mfma_f32_16x16x32_bf16 v[32:35], v[156:159], v[220:223], v[32:35]\n"
    "v_mfma_f32_16x16x32_bf16 v[36:39], v[156:159], v[20:23], v[36:39]\n"
    "ds_read_b64_tr_b16 v[172:173], v28 offset:2848\n"
    "ds_read_b64_tr_b16 v[174:175], v28 offset:2976\n"
    "ds_read_b128 v[236:239], v29 offset:176\n"
    "s_waitcnt lgkmcnt(9)\n"
    "v_mfma_f32_16x16x32_bf16 v[32:35], v[160:163], v[224:227], v[32:35]\n"
    "v_mfma_f32_16x16x32_bf16 v[36:39], v[160:163], v[20:23], v[36:39]\n"
    "ds_read_b64_tr_b16 v[176:177], v28 offset:3120\n"
    "ds_read_b64_tr_b16 v[178:179], v28 offset:3248\n"
    "ds_read_b128 v[240:243], v29 offset:192\n"
    "s_waitcnt lgkmcnt(9)\n"
    "v_mfma_f32_16x16x32_bf16 v[32:35], v[164:167], v[228:231], v[32:35]\n"
    "v_mfma_f32_16x16x32_bf16 v[36:39], v[164:167], v[20:23], v[36:39]\n"
    "ds_read_b64_tr_b16 v[180:181], v28 offset:3376\n"
    "ds_read_b64_tr_b16 v[182:183], v28 offset:3504\n"
    "ds_read_b128 v[244:247], v29 offset:208\n"
    "s_waitcnt lgkmcnt(9)\n"
    "v_mfma_f32_16x16x32_bf16 v[32:35], v[168:171], v[232:235], v[32:35]\n"
    "v_mfma_f32_16x16x32_bf16 v[36:39], v[168:171], v[20:23], v[36:39]\n"
    "ds_read_b64_tr_b16 v[184:185], v28 offset:3632\n"
    "ds_read_b64_tr_b16 v[186:187], v28 offset:3760\n"
    "ds_read_b128 v[248:251], v29 offset:224\n"
    "s_waitcnt lgkmcnt(9)\n"
    "v_mfma_f32_16x16x32_bf16 v[32:35], v[172:175], v[236:239], v[32:35]\n"
    "v_mfma_f32_16x16x32_bf16 v[36:39], v[172:175], v[20:23], v[36:39]\n"
    "ds_read_b64_tr_b16 v[188:189], v28 offset:3888\n"
    "ds_read_b64_tr_b16 v[190:191], v28 offset:4016\n"
    "ds_read_b128 v[252:255], v29 offset:240\n"
    "s_waitcnt lgkmcnt(9)\n"
    "v_mfma_f32_16x16x32_bf16 v[32:35], v[176:179], v[240:243], v[32:35]\n"
    "v_mfma_f32_16x16x32_bf16 v[36:39], v[176:179], v[20:23], v[36:39]\n"
    "ds_read_b32 v52, v49 offset:16\n"
    "ds_read_b64 v[40:41], v48\n"
    "s_waitcnt lgkmcnt(8)\n"
    "v_mfma_f32_16x16x32_bf16 v[32:35], v[180:183], v[244:247], v[32:35]\n"
    "v_mfma_f32_16x16x32_bf16 v[36:39], v[180:183], v[20:23], v[36:39]\n"
    "s_waitcnt lgkmcnt(5)\n"
    "v_mfma_f32_16x16x32_bf16 v[32:35], v[184:187], v[248:251], v[32:35]\n"
    "v_mfma_f32_16x16x32_bf16 v[36:39], v[184:187], v[20:23], v[36:39]\n"
    "s_waitcnt lgkmcnt(2)\n"
    "v_mfma_f32_16x16x32_bf16 v[32:35], v[188:191], v[252:255], v[32:35]\n"
    "v_mfma_f32_16x16x32_bf16 v[36:39], v[188:191], v[20:23], v[36:39]\n"
    "s_mov_b32 s62, 0x100000\n"
    "s_branch .Luc68_%=\n"
    ".Lus67_%=:\n"
    "ds_read_b32 v52, v49 offset:16\n"
    "ds_read_b64 v[40:41], v48\n"
    ".Luc68_%=:\n"
    "s_waitcnt lgkmcnt(0)\n"
    "v_cmp_ne_u32 vcc, 0x43424140, v52\n"
    "s_cbranch_vccz .Lud69_%=\n"
    "s_sub_u32 s62, s62, 1\n"
    "s_cbranch_scc0 .Lus67_%=\n"
    ".Lud69_%=:\n"
    "s_nop 3\n"
    "v_fma_f32 v32, -v40, v36, v32\n"
    "v_fma_f32 v33, -v40, v37, v33\n"
    "v_fma_f32 v34, -v40, v38, v34\n"
    "v_fma_f32 v35, -v40, v39, v35\n"
    "v_mul_f32 v32, v41, v32\n"
    "v_mul_f32 v33, v41, v33\n"
    "v_mul_f32 v34, v41, v34\n"
    "v_mul_f32 v35, v41, v35\n"
    "v_cvt_pk_bf16_f32 v32, v32, v33\n"
    "v_cvt_pk_bf16_f32 v33, v34, v35\n"
    "buffer_store_dwordx2 v[32:33], v30, s[44:47], s60 offen sc1\n"
    "s_branch .Ljoin60_%=\n"
    ".Lvunit64_%=:\n"
    "v_lshl_add_u32 v48, v27, 5, s59\n"
    "v_lshl_add_u32 v30, v31, 8, v30\n"
    "s_lshl_b32 s61, s61, 9\n"
    "s_add_u32 s60, s60, s61\n"
    "s_add_u32 s60, s60, 1024\n"
    "s_waitcnt vmcnt(2)\n"
    "v_readfirstlane_b32 s56, v2\n"
    "v_readfirstlane_b32 s57, v3\n"
    "s_add_u32 s56, s56, 0x7f4a7c15\n"
    "s_addc_u32 s57, s57, 0x9e3779b9\n"
    "v_mov_b32 v2, s56\n"
    "v_mov_b32 v3, s57\n"
    "ds_write_b64 v49, v[2:3] offset:112\n"
    "s_mov_b32 s62, 0x100000\n"
    "v_mov_b32 v53, 150336\n"
    "s_waitcnt lgkmcnt(0)\n"
    "s_nop 1\n"
    ".Llw70_%=:\n"
    "ds_read_b32 v52, v53\n"
    "s_waitcnt lgkmcnt(0)\n"
    "v_cmp_ne_u32 vcc, 0x43424140, v52\n"
    "s_cbranch_vccz .Llwd71_%=\n"
    "s_sleep 0\n"
    "s_sub_u32 s62, s62, 1\n"
    "s_cbranch_scc0 .Llw70_%=\n"
    ".Llwd71_%=:\n"
    "ds_read_b64_tr_b16 v[128:129], v28 offset:0\n"
    "ds_read_b64_tr_b16 v[130:131], v28 offset:128\n"
    "ds_read_b128 v[192:195], v29 offset:0\n"
    "ds_read_b64_tr_b16 v[132:133], v28 offset:256\n"
    "ds_read_b64_tr_b16 v[134:135], v28 offset:384\n"
    "ds_read_b128 v[196:199], v29 offset:16\n"
    "ds_read_b64_tr_b16 v[136:137], v28 offset:512\n"
    "ds_read_b64_tr_b16 v[138:139], v28 offset:640\n"
    "ds_read_b128 v[200:203], v29 offset:32\n"
    "ds_read_b64_tr_b16 v[140:141], v28 offset:768\n"
    "ds_read_b64_tr_b16 v[142:143], v28 offset:896\n"
    "ds_read_b128 v[204:207], v29 offset:48\n"
    "ds_write_b8 v7, v7 offset:96\n"
    "s_waitcnt lgkmcnt(10)\n"
    "v_mfma_f32_16x16x32_bf16 v[32:35], v[192:195], v[128:131], 0\n"
    "v_mfma_f32_16x16x32_bf16 v[36:39], v[20:23], v[128:131], 0\n"
    "ds_read_b64_tr_b16 v[144:145], v28 offset:1040\n"
    "ds_read_b64_tr_b16 v[146:147], v28 offset:1168\n"
    "ds_read_b128 v[208:211], v29 offset:64\n"
    "s_waitcnt lgkmcnt(10)\n"
    "v_mfma_f32_16x16x32_bf16 v[32:35], v[196:199], v[132:135], v[32:35]\n"
    "v_mfma_f32_16x16x32_bf16 v[36:39], v[20:23], v[132:135], v[36:39]\n"
    "ds_read_b64_tr_b16 v[148:149], v28 offset:1296\n"
    "ds_read_b64_tr_b16 v[150:151], v28 offset:1424\n"
    "ds_read_b128 v[212:215], v29 offset:80\n"
    "s_waitcnt lgkmcnt(10)\n"
    "v_mfma_f32_16x16x32_bf16 v[32:35], v[200:203], v[136:139], v[32:35]\n"
    "v_mfma_f32_16x16x32_bf16 v[36:39], v[20:23], v[136:139], v[36:39]\n"
    "ds_read_b64_tr_b16 v[152:153], v28 offset:1552\n"
    "ds_read_b64_tr_b16 v[154:155], v28 offset:1680\n"
    "ds_read_b128 v[216:219], v29 offset:96\n"
    "s_waitcnt lgkmcnt(10)\n"
    "v_mfma_f32_16x16x32_bf16 v[32:35], v[204:207], v[140:143], v[32:35]\n"
    "v_mfma_f32_16x16x32_bf16 v[36:39], v[20:23], v[140:143], v[36:39]\n"
    "ds_read_b64_tr_b16 v[156:157], v28 offset:1808\n"
    "ds_read_b64_tr_b16 v[158:159], v28 offset:1936\n"
    "ds_read_b128 v[220:223], v29 offset:112\n"
    "s_waitcnt lgkmcnt(9)\n"
    "v_mfma_f32_16x16x32_bf16 v[32:35], v[208:211], v[144:147], v[32:35]\n"
    "v_mfma_f32_16x16x32_bf16 v[36:39], v[20:23], v[144:147], v[36:39]\n"
    "ds_read_b64_tr_b16 v[160:161], v28 offset:2080\n"
    "ds_read_b64_tr_b16 v[162:163], v28 offset:2208\n"
    "ds_read_b128 v[224:227], v29 offset:128\n"
    "s_waitcnt lgkmcnt(9)\n"
    "v_mfma_f32_16x16x32_bf16 v[32:35], v[212:215], v[148:151], v[32:35]\n"
    "v_mfma_f32_16x16x32_bf16 v[36:39], v[20:23], v[148:151], v[36:39]\n"
    "ds_read_b64_tr_b16 v[164:165], v28 offset:2336\n"
    "ds_read_b64_tr_b16 v[166:167], v28 offset:2464\n"
    "ds_read_b128 v[228:231], v29 offset:144\n"
    "s_waitcnt lgkmcnt(9)\n"
    "v_mfma_f32_16x16x32_bf16 v[32:35], v[216:219], v[152:155], v[32:35]\n"
    "v_mfma_f32_16x16x32_bf16 v[36:39], v[20:23], v[152:155], v[36:39]\n"
    "ds_read_b64_tr_b16 v[168:169], v28 offset:2592\n"
    "ds_read_b64_tr_b16 v[170:171], v28 offset:2720\n"
    "ds_read_b128 v[232:235], v29 offset:160\n"
    "s_waitcnt lgkmcnt(9)\n"
    "v_mfma_f32_16x16x32_bf16 v[32:35], v[220:223], v[156:159], v[32:35]\n"
    "v_mfma_f32_16x16x32_bf16 v[36:39], v[20:23], v[156:159], v[36:39]\n"
    "ds_read_b64_tr_b16 v[172:173], v28 offset:2848\n"
    "ds_read_b64_tr_b16 v[174:175], v28 offset:2976\n"
    "ds_read_b128 v[236:239], v29 offset:176\n"
    "s_waitcnt lgkmcnt(9)\n"
    "v_mfma_f32_16x16x32_bf16 v[32:35], v[224:227], v[160:163], v[32:35]\n"
    "v_mfma_f32_16x16x32_bf16 v[36:39], v[20:23], v[160:163], v[36:39]\n"
    "ds_read_b64_tr_b16 v[176:177], v28 offset:3120\n"
    "ds_read_b64_tr_b16 v[178:179], v28 offset:3248\n"
    "ds_read_b128 v[240:243], v29 offset:192\n"
    "s_waitcnt lgkmcnt(9)\n"
    "v_mfma_f32_16x16x32_bf16 v[32:35], v[228:231], v[164:167], v[32:35]\n"
    "v_mfma_f32_16x16x32_bf16 v[36:39], v[20:23], v[164:167], v[36:39]\n"
    "ds_read_b64_tr_b16 v[180:181], v28 offset:3376\n"
    "ds_read_b64_tr_b16 v[182:183], v28 offset:3504\n"
    "ds_read_b128 v[244:247], v29 offset:208\n"
    "s_waitcnt lgkmcnt(9)\n"
    "v_mfma_f32_16x16x32_bf16 v[32:35], v[232:235], v[168:171], v[32:35]\n"
    "v_mfma_f32_16x16x32_bf16 v[36:39], v[20:23], v[168:171], v[36:39]\n"
    "ds_read_b64_tr_b16 v[184:185], v28 offset:3632\n"
    "ds_read_b64_tr_b16 v[186:187], v28 offset:3760\n"
    "ds_read_b128 v[248:251], v29 offset:224\n"
    "s_waitcnt lgkmcnt(9)\n"
    "v_mfma_f32_16x16x32_bf16 v[32:35], v[236:239], v[172:175], v[32:35]\n"
    "v_mfma_f32_16x16x32_bf16 v[36:39], v[20:23], v[172:175], v[36:39]\n"
    "ds_read_b64_tr_b16 v[188:189], v28 offset:3888\n"
    "ds_read_b64_tr_b16 v[190:191], v28 offset:4016\n"
    "ds_read_b128 v[252:255], v29 offset:240\n"
    "s_waitcnt lgkmcnt(9)\n"
    "v_mfma_f32_16x16x32_bf16 v[32:35], v[240:243], v[176:179], v[32:35]\n"
    "v_mfma_f32_16x16x32_bf16 v[36:39], v[20:23], v[176:179], v[36:39]\n"
    "ds_read_b32 v52, v49 offset:16\n"
    "ds_read_b128 v[40:43], v48\n"
    "ds_read_b128 v[44:47], v48 offset:16\n"
    "s_waitcnt lgkmcnt(9)\n"
    "v_mfma_f32_16x16x32_bf16 v[32:35], v[244:247], v[180:183], v[32:35]\n"
    "v_mfma_f32_16x16x32_bf16 v[36:39], v[20:23], v[180:183], v[36:39]\n"
    "s_waitcnt lgkmcnt(6)\n"
    "v_mfma_f32_16x16x32_bf16 v[32:35], v[248:251], v[184:187], v[32:35]\n"
    "v_mfma_f32_16x16x32_bf16 v[36:39], v[20:23], v[184:187], v[36:39]\n"
    "s_waitcnt lgkmcnt(3)\n"
    "v_mfma_f32_16x16x32_bf16 v[32:35], v[252:255], v[188:191], v[32:35]\n"
    "v_mfma_f32_16x16x32_bf16 v[36:39], v[20:23], v[188:191], v[36:39]\n"
    "s_mov_b32 s62, 0x100000\n"
    "s_branch .Luc73_%=\n"
    ".Lus72_%=:\n"
    "ds_read_b32 v52, v49 offset:16\n"
    "ds_read_b128 v[40:43], v48\n"
    "ds_read_b128 v[44:47], v48 offset:16\n"
    ".Luc73_%=:\n"
    "s_waitcnt lgkmcnt(0)\n"
    "v_cmp_ne_u32 vcc, 0x43424140, v52\n"
    "s_cbranch_vccz .Lud74_%=\n"
    "s_sub_u32 s62, s62, 1\n"
    "s_cbranch_scc0 .Lus72_%=\n"
    ".Lud74_%=:\n"
    "s_nop 3\n"
    "v_fma_f32 v32, -v40, v36, v32\n"
    "v_fma_f32 v33, -v42, v37, v33\n"
    "v_fma_f32 v34, -v44, v38, v34\n"
    "v_fma_f32 v35, -v46, v39, v35\n"
    "v_mul_f32 v32, v41, v32\n"
    "v_mul_f32 v33, v43, v33\n"
    "v_mul_f32 v34, v45, v34\n"
    "v_mul_f32 v35, v47, v35\n"
    "v_cvt_pk_bf16_f32 v32, v32, v33\n"
    "v_cvt_pk_bf16_f32 v33, v34, v35\n"
    "buffer_store_dwordx2 v[32:33], v30, s[44:47], s60 offen sc1\n"
    ".Ljoin60_%=:\n"
    "s_waitcnt vmcnt(0)\n"
    "buffer_store_dwordx2 v[2:3], v4, s[44:47], 0 offen sc1\n"
    "v_mov_b32 v50, 0x43424140\n"
    "ds_write_b32 v49, v50 offset:144\n"
    "s_waitcnt lgkmcnt(0)\n"
    ".Ljoin261_%=:\n"
    "v_mov_b32 v12, v1\n"
    "v_mov_b32 v15, 0xdddddddd\n"
    "v_and_b32 v24, 31, v0\n"
    "v_lshrrev_b32 v25, 5, v0\n"
    "v_lshlrev_b32 v25, 8, v25\n"
    "v_lshl_add_u32 v25, v24, 4, v25\n"
    "v_cmp_eq_u32 vcc, 16, v24\n"
    "v_mov_b32 v30, 2064\n"
    "v_mov_b32 v31, 1024\n"
    "v_cndmask_b32 v30, v30, v31, vcc\n"
    "v_cmp_gt_u32 vcc, 16, v24\n"
    "v_add_u32 v31, 1040, v25\n"
    "v_add_u32 v25, 1552, v25\n"
    "v_cndmask_b32 v13, v30, v31, vcc\n"
    "v_cndmask_b32 v14, v30, v25, vcc\n"
    "s_lshl2_add_u32 s58, s52, s53\n"
    "s_lshl_b32 s59, s53, 10\n"
    "s_add_u32 s59, s59, 158976\n"
    "v_add_u32 v108, s59, v1\n"
    "v_lshrrev_b32 v25, 5, v0\n"
    "v_bfe_u32 v26, v0, 2, 2\n"
    "v_bfe_u32 v27, v0, 4, 1\n"
    "v_and_b32 v28, 3, v0\n"
    "v_lshl_add_u32 v29, v28, 3, s59\n"
    "s_mov_b32 s15, 0x40000000\n"
    "s_cmp_ge_u32 s58, 56\n"
    "s_cbranch_scc1 .Lrt275_%=\n"
    "s_mov_b64 s[16:17], s[24:25]\n"
    "s_mov_b64 s[18:19], s[26:27]\n"
    "v_lshrrev_b32 v24, 4, v0\n"
    "v_lshl_add_u32 v24, v24, 3, v26\n"
    "v_mul_u32_u24 v24, 3584, v24\n"
    "v_lshl_add_u32 v109, v28, 4, v24\n"
    "v_lshl_add_u32 v110, v25, 8, v29\n"
    "v_lshl_add_u32 v110, v26, 6, v110\n"
    "v_lshl_add_u32 v110, v27, 5, v110\n"
    "v_add_u32 v111, 512, v110\n"
    "s_lshr_b32 s59, s58, 3\n"
    "s_and_b32 s58, s58, 7\n"
    "s_mul_i32 s60, s51, 7\n"
    "s_add_u32 s60, s60, s59\n"
    "s_lshl3_add_u32 s59, s60, s58\n"
    "s_lshl_b32 s12, s59, 12\n"
    "s_add_u32 s12, s12, 13631488\n"
    "s_lshl_b32 s15, s59, 7\n"
    "s_add_u32 s15, s15, 458752\n"
    "s_mov_b32 s13, 1024\n"
    "s_mov_b32 s14, 2048\n"
    "s_mul_i32 s58, s58, 229376\n"
    "s_lshl_b32 s60, s60, 6\n"
    "s_add_u32 s58, s58, s60\n"
    "s_mov_b32 s59, 14336\n"
    "s_mov_b32 s60, 114688\n"
    "s_branch .Lrtj77_%=\n"
    ".Lrt275_%=:\n"
    "s_cmp_ge_u32 s58, 112\n"
    "s_cbranch_scc1 .Lrt376_%=\n"
    "s_mov_b64 s[16:17], s[32:33]\n"
    "s_mov_b64 s[18:19], s[34:35]\n"
    "v_lshlrev_b32 v24, 6, v27\n"
    "v_and_b32 v30, 0x70, v1\n"
    "v_xor_b32 v24, v24, v30\n"
    "v_lshrrev_b32 v30, 3, v0\n"
    "v_lshl_add_u32 v109, v30, 10, v24\n"
    "v_lshl_add_u32 v110, v25, 9, v29\n"
    "v_lshl_add_u32 v110, v26, 7, v110\n"
    "v_bfe_u32 v30, v0, 3, 1\n"
    "v_lshl_add_u32 v110, v30, 6, v110\n"
    "v_lshl_add_u32 v110, v27, 5, v110\n"
    "v_xor_b32 v111, 64, v110\n"
    "s_sub_u32 s58, s58, 56\n"
    "s_mul_i32 s12, s51, 229376\n"
    "s_lshl_b32 s60, s58, 12\n"
    "s_add_u32 s12, s12, s60\n"
    "s_add_u32 s12, s12, 15466496\n"
    "s_mov_b32 s13, 1024\n"
    "s_mov_b32 s14, 2048\n"
    "s_lshl_b32 s58, s58, 15\n"
    "s_add_u32 s58, s58, s66\n"
    "s_mov_b32 s59, 8192\n"
    "s_mov_b32 s60, 16384\n"
    "s_branch .Lrtj77_%=\n"
    ".Lrt376_%=:\n"
    "s_mov_b64 s[16:17], s[20:21]\n"
    "s_mov_b64 s[18:19], s[22:23]\n"
    "v_lshl_add_u32 v24, v27, 4, v26\n"
    "v_lshl_add_u32 v24, v25, 2, v24\n"
    "v_lshlrev_b32 v24, 10, v24\n"
    "v_lshl_add_u32 v109, v28, 4, v24\n"
    "v_lshrrev_b32 v25, 2, v0\n"
    "v_lshl_add_u32 v110, v25, 6, v29\n"
    "v_xor_b32 v111, 32, v110\n"
    "s_sub_u32 s58, s58, 112\n"
    "s_lshl_b32 s12, s51, 16\n"
    "s_lshl_b32 s60, s58, 10\n"
    "s_add_u32 s12, s12, s60\n"
    "s_add_u32 s12, s12, 17301504\n"
    "s_mov_b32 s13, 16384\n"
    "s_mov_b32 s14, 32768\n"
    "s_lshl_b32 s58, s58, 15\n"
    "s_add_u32 s58, s58, s66\n"
    "s_mov_b32 s59, 8192\n"
    "s_mov_b32 s60, 64\n"
    ".Lrtj77_%=:\n"
    "s_mov_b32 s8, s58\n"
    "s_add_u32 s9, s58, s59\n"
    "s_add_u32 s10, s58, s60\n"
    "s_add_u32 s11, s9, s60\n"
    "v_cmp_gt_u32 vcc, 32, v0\n"
    "v_lshl_add_u32 v24, v0, 2, s15\n"
    "v_mov_b32 v25, 0x40000000\n"
    "v_cndmask_b32 v107, v25, v24, vcc\n"
    "s_lshl3_add_u32 s58, s67, s51\n"
    "s_lshl2_add_u32 s59, s58, s53\n"
    "s_lshl_b32 s20, s59, 10\n"
    "s_mul_i32 s59, s55, 2560\n"
    "s_add_u32 s59, s59, 1050880\n"
    "s_lshl3_add_u32 s21, s53, s59\n"
    "s_lshl3_add_u32 s25, s49, s59\n"
    "s_add_u32 s59, s53, s98\n"
    "s_and_b32 s59, s59, 3\n"
    "s_lshl2_add_u32 s59, s67, s59\n"
    "s_lshl_b32 s22, s59, 13\n"
    "s_mul_i32 s23, s52, 20480\n"
    "s_add_u32 s23, s23, 1048576\n"
    "s_mul_i32 s24, s58, 14336\n"
    "s_lshl_b32 s59, s49, 11\n"
    "s_add_u32 s24, s24, s59\n"
    "s_lshl3_add_u32 s59, s67, s99\n"
    "s_mul_i32 s26, s59, 14336\n"
    "s_lshl3_add_u32 s59, s67, s49\n"
    "s_lshl_b32 s59, s59, 12\n"
    "s_add_u32 s32, s59, s66\n"
    "s_xor_b32 s59, s49, 4\n"
    "s_mul_i32 s59, s59, 10240\n"
    "s_add_u32 s33, s59, 32768\n"
    "s_lshl3_add_u32 s59, s51, s99\n"
    "s_mul_i32 s59, s59, 28672\n"
    "s_add_u32 s34, s59, 15466496\n"
    "s_lshl_b32 s59, s98, 1\n"
    "s_sub_u32 s59, s51, s59\n"
    "s_and_b32 s59, s59, 7\n"
    "s_lshl_b32 s59, s59, 6\n"
    "s_add_u32 s59, s59, s49\n"
    "s_lshl_b32 s27, s59, 6\n"
    "; ---- poll addresses: v5 local, v104 remote\n"
    "v_bfe_u32 v5, v0, 0, 3\n"
    "v_mul_u32_u24 v5, 2560, v5\n"
    "v_bfe_u32 v104, v0, 3, 3\n"
    "v_mul_u32_u24 v104, 81920, v104\n"
    "v_and_b32 v24, 7, v0\n"
    "v_lshl_add_u32 v104, v24, 3, v104\n"
    "s_lshl2_add_u32 s58, s50, s53\n"
    "s_mul_i32 s0, s58, 20480\n"
    "s_add_u32 s0, s0, 1050624\n"
    "s_lshl3_add_u32 s58, s53, s50\n"
    "s_mul_i32 s1, s58, 2560\n"
    "s_add_u32 s1, s1, 1051008\n"
    "s_sleep 2\n"
    "buffer_load_dwordx2 v[8:9], v5, s[44:47], s0 offen sc1\n"
    "s_barrier\n"
    "s_mov_b32 s62, 0x20000\n"
    "s_branch .Lpchk79_%=\n"
    ".Lpoll78_%=:\n"
    "buffer_load_dwordx2 v[8:9], v5, s[44:47], s0 offen sc1\n"
    ".Lpchk79_%=:\n"
    "s_waitcnt vmcnt(0)\n"
    "v_cmp_ne_u64 vcc, s[56:57], v[8:9]\n"
    "s_cbranch_vccz .Lpdone80_%=\n"
    "s_sleep 4\n"
    "s_sub_u32 s62, s62, 1\n"
    "s_cbranch_scc0 .Lpoll78_%=\n"
    ".Lpdone80_%=:\n"
    "buffer_load_dwordx2 v[8:9], v104, s[44:47], s1 offen sc1\n"
    "s_add_u32 s58, s50, 0\n"
    "s_and_b32 s58, s58, 7\n"
    "s_lshl2_add_u32 s58, s58, s53\n"
    "s_lshl_b32 s58, s58, 12\n"
    "s_mul_i32 m0, s53, 4160\n"
    "s_add_u32 m0, m0, 0\n"
    "s_add_u32 s58, s58, 327680\n"
    "buffer_load_dwordx4 v1, s[44:47], s58 offen sc1 lds\n"
    "s_add_u32 m0, m0, 16\n"
    "s_nop 0\n"
    "buffer_load_dwordx4 v1, s[44:47], s58 offen offset:1024 sc1 lds\n"
    "s_add_u32 m0, m0, 16\n"
    "s_nop 0\n"
    "buffer_load_dwordx4 v1, s[44:47], s58 offen offset:2048 sc1 lds\n"
    "s_add_u32 m0, m0, 16\n"
    "s_nop 0\n"
    "buffer_load_dwordx4 v1, s[44:47], s58 offen offset:3072 sc1 lds\n"
    "s_or_b32 s58, s48, s53\n"
    "s_cmp_eq_u32 s58, 0\n"
    "s_cselect_b32 s58, 0, 0x40000000\n"
    "v_mov_b32 v6, s58\n"
    "v_mov_b32 v16, -1\n"
    "v_mov_b32 v17, -1\n"
    "v_mov_b32 v18, -1\n"
    "v_mov_b32 v19, -1\n"
    "v_or_b32 v24, s63, v1\n"
    "v_lshlrev_b32 v25, 2, v0\n"
    "v_lshrrev_b32 v26, 1, v0\n"
    "v_mul_u32_u24 v26, 20480, v26\n"
    "v_and_b32 v29, 16, v1\n"
    "v_add_u32 v26, v26, v29\n"
    "v_add_u32 v27, -8192, v108\n"
    "s_mul_i32 s58, s51, 7\n"
    "s_add_u32 s58, s58, s49\n"
    "s_lshl_b32 s58, s58, 15\n"
    "s_add_u32 s58, s58, 13631488\n"
    "s_add_u32 s58, s58, s63\n"
    "v_add_u32 v28, s58, v1\n"
    "s_lshl_b32 s35, s98, 13\n"
    "; ---- if the other classes have published already, their rows are requested in front of the barrier\n"
    "s_waitcnt vmcnt(4)\n"
    "v_cmp_ne_u64 vcc, s[56:57], v[8:9]\n"
    "s_cbranch_vccnz .Llate81_%=\n"
    "s_add_u32 s58, s50, 1\n"
    "s_and_b32 s58, s58, 7\n"
    "s_lshl2_add_u32 s58, s58, s53\n"
    "s_lshl_b32 s58, s58, 12\n"
    "s_mul_i32 m0, s53, 4160\n"
    "s_add_u32 m0, m0, 16640\n"
    "s_add_u32 s58, s58, 327680\n"
    "buffer_load_dwordx4 v1, s[44:47], s58 offen sc1 lds\n"
    "s_add_u32 m0, m0, 16\n"
    "s_nop 0\n"
    "buffer_load_dwordx4 v1, s[44:47], s58 offen offset:1024 sc1 lds\n"
    "s_add_u32 m0, m0, 16\n"
    "s_nop 0\n"
    "buffer_load_dwordx4 v1, s[44:47], s58 offen offset:2048 sc1 lds\n"
    "s_add_u32 m0, m0, 16\n"
    "s_nop 0\n"
    "buffer_load_dwordx4 v1, s[44:47], s58 offen offset:3072 sc1 lds\n"
    "s_add_u32 s58, s50, 2\n"
    "s_and_b32 s58, s58, 7\n"
    "s_lshl2_add_u32 s58, s58, s53\n"
    "s_lshl_b32 s58, s58, 12\n"
    "s_mul_i32 m0, s53, 4160\n"
    "s_add_u32 m0, m0, 33280\n"
    "s_add_u32 s58, s58, 327680\n"
    "buffer_load_dwordx4 v1, s[44:47], s58 offen sc1 lds\n"
    "s_add_u32 m0, m0, 16\n"
    "s_nop 0\n"
    "buffer_load_dwordx4 v1, s[44:47], s58 offen offset:1024 sc1 lds\n"
    "s_add_u32 m0, m0, 16\n"
    "s_nop 0\n"
    "buffer_load_dwordx4 v1, s[44:47], s58 offen offset:2048 sc1 lds\n"
    "s_add_u32 m0, m0, 16\n"
    "s_nop 0\n"
    "buffer_load_dwordx4 v1, s[44:47], s58 offen offset:3072 sc1 lds\n"
    "s_waitcnt vmcnt(8)\n"
    "s_barrier\n"
    ".Lmerge82_%=:\n"
    "s_add_u32 s58, s50, 3\n"
    "s_and_b32 s58, s58, 7\n"
    "s_lshl2_add_u32 s58, s58, s53\n"
    "s_lshl_b32 s58, s58, 12\n"
    "s_mul_i32 m0, s53, 4160\n"
    "s_add_u32 m0, m0, 49920\n"
    "s_add_u32 s58, s58, 327680\n"
    "buffer_load_dwordx4 v1, s[44:47], s58 offen sc1 lds\n"
    "s_add_u32 m0, m0, 16\n"
    "s_nop 0\n"
    "buffer_load_dwordx4 v1, s[44:47], s58 offen offset:1024 sc1 lds\n"
    "s_add_u32 m0, m0, 16\n"
    "s_nop 0\n"
    "buffer_load_dwordx4 v1, s[44:47], s58 offen offset:2048 sc1 lds\n"
    "s_add_u32 m0, m0, 16\n"
    "s_nop 0\n"
    "buffer_load_dwordx4 v1, s[44:47], s58 offen offset:3072 sc1 lds\n"
    "s_waitcnt vmcnt(4)\n"
    "s_barrier\n"
    "s_add_u32 s58, s50, 4\n"
    "s_and_b32 s58, s58, 7\n"
    "s_lshl2_add_u32 s58, s58, s53\n"
    "s_lshl_b32 s58, s58, 12\n"
    "s_mul_i32 m0, s53, 4160\n"
    "s_add_u32 m0, m0, 66560\n"
    "s_add_u32 s58, s58, 327680\n"
    "buffer_load_dwordx4 v1, s[44:47], s58 offen sc1 lds\n"
    "s_add_u32 m0, m0, 16\n"
    "s_nop 0\n"
    "buffer_load_dwordx4 v1, s[44:47], s58 offen offset:1024 sc1 lds\n"
    "s_add_u32 m0, m0, 16\n"
    "s_nop 0\n"
    "buffer_load_dwordx4 v1, s[44:47], s58 offen offset:2048 sc1 lds\n"
    "s_add_u32 m0, m0, 16\n"
    "s_nop 0\n"
    "buffer_load_dwordx4 v1, s[44:47], s58 offen offset:3072 sc1 lds\n"
    "s_branch .LBrest58_%=\n"
    ".LAmain5_%=:\n"
    "v_mov_b32 v20, 0x3f803f80\n"
    "v_mov_b32 v21, 0x3f803f80\n"
    "v_mov_b32 v22, 0x3f803f80\n"
    "v_mov_b32 v23, 0x3f803f80\n"
    "s_xor_b32 s58, s61, 32\n"
    "s_mul_i32 s58, s58, 1040\n"
    "s_add_u32 s58, s58, 16640\n"
    "v_and_b32 v24, 3, v0\n"
    "v_mul_u32_u24 v25, 1040, v24\n"
    "v_and_b32 v26, 0x3c0, v1\n"
    "v_add3_u32 v105, v25, v26, s58\n"
    "s_lshl3_add_u32 s58, s61, 149760\n"
    "v_lshl_add_u32 v106, v24, 3, s58\n"
    "s_add_u32 s58, s53, 150336\n"
    "v_mov_b32 v4, s58\n"
    "s_and_b32 s58, s53, 1\n"
    "s_lshl_b32 s58, s58, 6\n"
    "v_lshlrev_b32 v16, 1, v0\n"
    "v_and_b32 v16, 32, v16\n"
    "v_or_b32 v16, s58, v16\n"
    "v_and_b32 v17, 0x70, v1\n"
    "v_xor_b32 v16, v16, v17\n"
    "v_lshrrev_b32 v17, 3, v0\n"
    "v_lshl_add_u32 v16, v17, 10, v16\n"
    "v_and_b32 v26, 15, v0\n"
    "v_lshrrev_b32 v27, 4, v0\n"
    "v_mul_u32_u24 v29, 1040, v26\n"
    "v_lshl_add_u32 v29, v27, 8, v29\n"
    "v_add_u32 v96, 16640, v29\n"
    "v_bfe_u32 v28, v0, 3, 2\n"
    "v_xor_b32 v28, s53, v28\n"
    "v_lshlrev_b32 v28, 5, v28\n"
    "v_bfe_u32 v30, v0, 2, 2\n"
    "v_lshl_add_u32 v28, v30, 7, v28\n"
    "v_and_b32 v30, 3, v0\n"
    "v_lshl_add_u32 v28, v30, 3, v28\n"
    "v_mul_u32_u24 v30, 16640, v27\n"
    "v_add_u32 v28, v28, v30\n"
    "v_add_u32 v97, 83200, v28\n"
    "v_lshlrev_b32 v26, 3, v26\n"
    "v_add_u32 v98, 149760, v26\n"
    "s_lshl_b32 s58, s53, 10\n"
    "s_add_u32 s58, s58, 150784\n"
    "v_add_u32 v99, s58, v1\n"
    "v_mov_b32 v12, v1\n"
    "v_mov_b32 v15, 0xdddddddd\n"
    "v_and_b32 v24, 31, v0\n"
    "v_lshrrev_b32 v25, 5, v0\n"
    "v_lshlrev_b32 v25, 8, v25\n"
    "v_lshl_add_u32 v25, v24, 4, v25\n"
    "v_cmp_eq_u32 vcc, 16, v24\n"
    "v_mov_b32 v30, 2064\n"
    "v_mov_b32 v31, 1024\n"
    "v_cndmask_b32 v30, v30, v31, vcc\n"
    "v_cmp_gt_u32 vcc, 16, v24\n"
    "v_add_u32 v31, 1040, v25\n"
    "v_add_u32 v25, 1552, v25\n"
    "v_cndmask_b32 v13, v30, v31, vcc\n"
    "v_cndmask_b32 v14, v30, v25, vcc\n"
    "s_mul_i32 s59, s53, 16640\n"
    "s_add_u32 s59, s59, 83200\n"
    "s_lshl_b32 s60, s53, 17\n"
    "s_add_u32 s60, s60, s66\n"
    "s_add_u32 s58, s98, 0\n"
    "s_and_b32 s58, s58, 3\n"
    "s_mul_i32 s61, s58, 4160\n"
    "s_add_u32 s61, s61, s59\n"
    "v_add_u32 v108, s61, v1\n"
    "s_lshl_b32 s58, s58, 15\n"
    "s_add_u32 s16, s58, s60\n"
    "s_add_u32 s58, s98, 1\n"
    "s_and_b32 s58, s58, 3\n"
    "s_mul_i32 s61, s58, 4160\n"
    "s_add_u32 s61, s61, s59\n"
    "v_add_u32 v109, s61, v1\n"
    "s_lshl_b32 s58, s58, 15\n"
    "s_add_u32 s17, s58, s60\n"
    "s_add_u32 s58, s98, 2\n"
    "s_and_b32 s58, s58, 3\n"
    "s_mul_i32 s61, s58, 4160\n"
    "s_add_u32 s61, s61, s59\n"
    "v_add_u32 v110, s61, v1\n"
    "s_lshl_b32 s58, s58, 15\n"
    "s_add_u32 s18, s58, s60\n"
    "s_add_u32 s58, s98, 3\n"
    "s_and_b32 s58, s58, 3\n"
    "s_mul_i32 s61, s58, 4160\n"
    "s_add_u32 s61, s61, s59\n"
    "v_add_u32 v111, s61, v1\n"
    "s_lshl_b32 s58, s58, 15\n"
    "s_add_u32 s19, s58, s60\n"
    "s_waitcnt vmcnt(0)\n"
    "ds_write_b8 v4, v4 offset:0\n"
    "ds_read_b128 v[112:115], v105 offset:0\n"
    "ds_read_b128 v[116:119], v105 offset:16\n"
    "ds_read_b128 v[120:123], v105 offset:32\n"
    "ds_read_b128 v[124:127], v105 offset:48\n"
    "v_mov_b32 v24, 0\n"
    "v_mov_b32 v25, 0\n"
    "s_waitcnt lgkmcnt(3)\n"
    "v_dot2c_f32_bf16 v24, v112, v20\n"
    "v_dot2c_f32_bf16 v25, v112, v112\n"
    "v_dot2c_f32_bf16 v24, v113, v20\n"
    "v_dot2c_f32_bf16 v25, v113, v113\n"
    "v_dot2c_f32_bf16 v24, v114, v20\n"
    "v_dot2c_f32_bf16 v25, v114, v114\n"
    "v_dot2c_f32_bf16 v24, v115, v20\n"
    "v_dot2c_f32_bf16 v25, v115, v115\n"
    "s_waitcnt lgkmcnt(2)\n"
    "v_dot2c_f32_bf16 v24, v116, v20\n"
    "v_dot2c_f32_bf16 v25, v116, v116\n"
    "v_dot2c_f32_bf16 v24, v117, v20\n"
    "v_dot2c_f32_bf16 v25, v117, v117\n"
    "v_dot2c_f32_bf16 v24, v118, v20\n"
    "v_dot2c_f32_bf16 v25, v118, v118\n"
    "v_dot2c_f32_bf16 v24, v119, v20\n"
    "v_dot2c_f32_bf16 v25, v119, v119\n"
    "s_waitcnt lgkmcnt(1)\n"
    "v_dot2c_f32_bf16 v24, v120, v20\n"
    "v_dot2c_f32_bf16 v25, v120, v120\n"
    "v_dot2c_f32_bf16 v24, v121, v20\n"
    "v_dot2c_f32_bf16 v25, v121, v121\n"
    "v_dot2c_f32_bf16 v24, v122, v20\n"
    "v_dot2c_f32_bf16 v25, v122, v122\n"
    "v_dot2c_f32_bf16 v24, v123, v20\n"
    "v_dot2c_f32_bf16 v25, v123, v123\n"
    "s_waitcnt lgkmcnt(0)\n"
    "v_dot2c_f32_bf16 v24, v124, v20\n"
    "v_dot2c_f32_bf16 v25, v124, v124\n"
    "v_dot2c_f32_bf16 v24, v125, v20\n"
    "v_dot2c_f32_bf16 v25, v125, v125\n"
    "v_dot2c_f32_bf16 v24, v126, v20\n"
    "v_dot2c_f32_bf16 v25, v126, v126\n"
    "v_dot2c_f32_bf16 v24, v127, v20\n"
    "v_dot2c_f32_bf16 v25, v127, v127\n"
    "s_nop 2\n"
    "v_permlane32_swap_b32 v24, v25\n"
    "v_add_f32 v24, v24, v25\n"
    "s_nop 1\n"
    "v_add_f32_dpp v24, v24, v24 row_ror:8 row_mask:0xf bank_mask:0xf\n"
    "s_nop 1\n"
    "v_add_f32_dpp v24, v24, v24 row_ror:4 row_mask:0xf bank_mask:0xf\n"
    "v_mov_b32 v25, v24\n"
    "s_nop 1\n"
    "v_permlane16_swap_b32 v24, v25\n"
    "v_add_f32 v24, v24, v25\n"
    "v_mul_f32 v24, 0x3b000000, v24\n"
    "v_mov_b32 v25, v24\n"
    "s_nop 1\n"
    "v_permlane32_swap_b32 v24, v25\n"
    "v_fma_f32 v25, -v24, v24, v25\n"
    "v_max_f32 v25, 0, v25\n"
    "v_add_f32 v25, 0x3727c5ac, v25\n"
    "v_rsq_f32 v25, v25\n"
    "s_nop 0\n"
    "ds_write_b64 v106, v[24:25] offset:0\n"
    "ds_write_b8 v4, v4 offset:16\n"
    "s_setprio 0\n"
    "buffer_load_dwordx4 v[32:35], v16, s[8:11], s16 offen\n"
    "s_add_u32 s16, s16, 8192\n"
    "buffer_load_dwordx4 v[36:39], v16, s[8:11], s16 offen\n"
    "s_add_u32 s16, s16, 8192\n"
    "buffer_load_dwordx4 v[40:43], v16, s[8:11], s16 offen\n"
    "s_add_u32 s16, s16, 8192\n"
    "buffer_load_dwordx4 v[44:47], v16, s[8:11], s16 offen\n"
    "buffer_load_dwordx4 v[48:51], v16, s[8:11], s17 offen\n"
    "s_add_u32 s17, s17, 8192\n"
    "buffer_load_dwordx4 v[52:55], v16, s[8:11], s17 offen\n"
    "s_add_u32 s17, s17, 8192\n"
    "buffer_load_dwordx4 v[56:59], v16, s[8:11], s17 offen\n"
    "s_add_u32 s17, s17, 8192\n"
    "buffer_load_dwordx4 v[60:63], v16, s[8:11], s17 offen\n"
    "buffer_load_dwordx4 v[64:67], v16, s[8:11], s18 offen\n"
    "s_add_u32 s18, s18, 8192\n"
    "buffer_load_dwordx4 v[68:71], v16, s[8:11], s18 offen\n"
    "s_lshl3_add_u32 s55, s52, s51\n"
    "s_lshl_b32 s67, s52, 1\n"
    "s_cmp_eq_u32 s49, 7\n"
    "s_cselect_b32 s63, 0x40000000, 0\n"
    "s_lshl1_add_u32 s99, s98, s49\n"
    "s_and_b32 s99, s99, 7\n"
    "s_mov_b32 s3, 0\n"
    "s_mov_b32 s62, 0x100000\n"
    "v_mov_b32 v25, 150432\n"
    "s_waitcnt lgkmcnt(0)\n"
    "s_nop 1\n"
    ".Llw6_%=:\n"
    "ds_read_b32 v24, v25\n"
    "s_waitcnt lgkmcnt(0)\n"
    "v_cmp_ne_u32 vcc, 0x43424140, v24\n"
    "s_cbranch_vccz .Llwd7_%=\n"
    "s_sleep 1\n"
    "s_sub_u32 s62, s62, 1\n"
    "s_cbranch_scc0 .Llw6_%=\n"
    ".Llwd7_%=:\n"
    "v_mov_b32 v2, 150448\n"
    "ds_read_b64 v[2:3], v2\n"
    "s_add_u32 s18, s18, 8192\n"
    "buffer_load_dwordx4 v[72:75], v16, s[8:11], s18 offen\n"
    "ds_read_b128 v[128:131], v96 offset:0\n"
    "ds_read_b128 v[132:135], v96 offset:16640\n"
    "ds_read_b128 v[136:139], v96 offset:16\n"
    "ds_read_b128 v[140:143], v96 offset:16656\n"
    "s_waitcnt vmcnt(10)\n"
    "ds_write_b128 v108, v[32:35] offset:0\n"
    "s_add_u32 s18, s18, 8192\n"
    "buffer_load_dwordx4 v[76:79], v16, s[8:11], s18 offen\n"
    "ds_read_b128 v[144:147], v96 offset:32\n"
    "ds_read_b128 v[148:151], v96 offset:16672\n"
    "ds_read_b128 v[152:155], v96 offset:48\n"
    "ds_read_b128 v[156:159], v96 offset:16688\n"
    "s_waitcnt vmcnt(10)\n"
    "ds_write_b128 v108, v[36:39] offset:1040\n"
    "buffer_load_dwordx4 v[80:83], v16, s[8:11], s19 offen\n"
    "ds_read_b128 v[160:163], v96 offset:64\n"
    "ds_read_b128 v[164:167], v96 offset:16704\n"
    "ds_read_b128 v[168:171], v96 offset:80\n"
    "ds_read_b128 v[172:175], v96 offset:16720\n"
    "s_waitcnt vmcnt(10)\n"
    "ds_write_b128 v108, v[40:43] offset:2080\n"
    "s_add_u32 s19, s19, 8192\n"
    "buffer_load_dwordx4 v[84:87], v16, s[8:11], s19 offen\n"
    "ds_read_b128 v[176:179], v96 offset:96\n"
    "ds_read_b128 v[180:183], v96 offset:16736\n"
    "ds_read_b128 v[184:187], v96 offset:112\n"
    "ds_read_b128 v[188:191], v96 offset:16752\n"
    "s_waitcnt vmcnt(10)\n"
    "ds_write_b128 v108, v[44:47] offset:3120\n"
    "s_add_u32 s19, s19, 8192\n"
    "buffer_load_dwordx4 v[88:91], v16, s[8:11], s19 offen\n"
    "ds_read_b128 v[192:195], v96 offset:128\n"
    "ds_read_b128 v[196:199], v96 offset:16768\n"
    "ds_read_b128 v[200:203], v96 offset:144\n"
    "ds_read_b128 v[204:207], v96 offset:16784\n"
    "s_waitcnt vmcnt(10)\n"
    "ds_write_b128 v109, v[48:51] offset:0\n"
    "s_add_u32 s19, s19, 8192\n"
    "buffer_load_dwordx4 v[92:95], v16, s[8:11], s19 offen\n"
    "ds_read_b128 v[208:211], v96 offset:160\n"
    "ds_read_b128 v[212:215], v96 offset:16800\n"
    "ds_read_b128 v[216:219], v96 offset:176\n"
    "ds_read_b128 v[220:223], v96 offset:16816\n"
    "s_waitcnt vmcnt(10)\n"
    "ds_write_b128 v109, v[52:55] offset:1040\n"
    "ds_read_b128 v[224:227], v96 offset:192\n"
    "ds_read_b128 v[228:231], v96 offset:16832\n"
    "ds_read_b128 v[232:235], v96 offset:208\n"
    "ds_read_b128 v[236:239], v96 offset:16848\n"
    "s_waitcnt vmcnt(9)\n"
    "ds_write_b128 v109, v[56:59] offset:2080\n"
    "ds_read_b128 v[240:243], v96 offset:224\n"
    "ds_read_b128 v[244:247], v96 offset:16864\n"
    "ds_read_b128 v[248:251], v96 offset:240\n"
    "ds_read_b128 v[252:255], v96 offset:16880\n"
    "s_waitcnt vmcnt(8)\n"
    "ds_write_b128 v109, v[60:63] offset:3120\n"
    "s_waitcnt vmcnt(7)\n"
    "ds_write_b128 v110, v[64:67] offset:0\n"
    "s_waitcnt vmcnt(6)\n"
    "ds_write_b128 v110, v[68:71] offset:1040\n"
    "s_waitcnt vmcnt(5)\n"
    "ds_write_b128 v110, v[72:75] offset:2080\n"
    "s_waitcnt vmcnt(4)\n"
    "ds_write_b128 v110, v[76:79] offset:3120\n"
    "s_waitcnt vmcnt(3)\n"
    "ds_write_b128 v111, v[80:83] offset:0\n"
    "s_waitcnt vmcnt(2)\n"
    "ds_write_b128 v111, v[84:87] offset:1040\n"
    "s_waitcnt vmcnt(1)\n"
    "ds_write_b128 v111, v[88:91] offset:2080\n"
    "s_waitcnt vmcnt(0)\n"
    "ds_write_b128 v111, v[92:95] offset:3120\n"
    "s_lshl3_add_u32 s58, s67, s51\n"
    "s_lshl2_add_u32 s59, s58, s53\n"
    "s_lshl_b32 s20, s59, 10\n"
    "s_mul_i32 s59, s55, 2560\n"
    "s_add_u32 s59, s59, 1050880\n"
    "s_lshl3_add_u32 s21, s53, s59\n"
    "s_lshl3_add_u32 s25, s49, s59\n"
    "s_add_u32 s59, s53, s98\n"
    "s_and_b32 s59, s59, 3\n"
    "s_lshl2_add_u32 s59, s67, s59\n"
    "s_lshl_b32 s22, s59, 13\n"
    "s_mul_i32 s23, s52, 20480\n"
    "s_add_u32 s23, s23, 1048576\n"
    "s_mul_i32 s24, s58, 14336\n"
    "s_lshl_b32 s59, s49, 11\n"
    "s_add_u32 s24, s24, s59\n"
    "s_lshl3_add_u32 s59, s67, s99\n"
    "s_mul_i32 s26, s59, 14336\n"
    "s_lshl3_add_u32 s59, s67, s49\n"
    "s_lshl_b32 s59, s59, 12\n"
    "s_add_u32 s32, s59, s66\n"
    "s_xor_b32 s59, s49, 4\n"
    "s_mul_i32 s59, s59, 10240\n"
    "s_add_u32 s33, s59, 32768\n"
    "s_lshl3_add_u32 s59, s51, s99\n"
    "s_mul_i32 s59, s59, 28672\n"
    "s_add_u32 s34, s59, 15466496\n"
    "s_lshl_b32 s59, s98, 1\n"
    "s_sub_u32 s59, s51, s59\n"
    "s_and_b32 s59, s59, 7\n"
    "s_lshl_b32 s59, s59, 6\n"
    "s_add_u32 s59, s59, s49\n"
    "s_lshl_b32 s27, s59, 6\n"
    "s_waitcnt lgkmcnt(14)\n"
    "v_readfirstlane_b32 s56, v2\n"
    "v_readfirstlane_b32 s57, v3\n"
    "s_mul_i32 s58, s55, 2560\n"
    "s_add_u32 s58, s58, 1048576\n"
    "s_lshl_b32 s8, s53, 7\n"
    "s_add_u32 s8, s8, s58\n"
    "s_lshl_b32 s10, s49, 7\n"
    "s_add_u32 s10, s10, s58\n"
    "s_add_u32 s10, s10, 1024\n"
    "s_add_u32 s58, s53, s98\n"
    "s_and_b32 s58, s58, 3\n"
    "s_mul_i32 s58, s58, 5120\n"
    "s_add_u32 s9, s58, s23\n"
    "s_mul_i32 s58, s99, 2560\n"
    "s_add_u32 s58, s58, s23\n"
    "s_add_u32 s11, s58, 1024\n"
    "s_and_b32 s58, s53, 1\n"
    "s_mul_i32 s19, s58, 2080\n"
    "s_lshl_b32 s58, s58, 11\n"
    "s_sub_u32 s18, 1024, s58\n"
    "s_mul_i32 s58, s51, 7\n"
    "s_add_u32 s58, s58, s49\n"
    "s_lshl_b32 s58, s58, 15\n"
    "s_add_u32 s58, s58, 13631488\n"
    "s_add_u32 s58, s58, s63\n"
    "v_add_u32 v6, s58, v1\n"
    "s_lshl_b32 s35, s98, 13\n"
    "s_lshl2_add_u32 s58, s51, s53\n"
    "s_lshl_b32 s58, s58, 14\n"
    "s_add_u32 s58, s58, 17301504\n"
    "v_add_u32 v7, s58, v1\n"
    "s_lshl_b32 s61, s98, 12\n"
    "v_bfe_u32 v5, v0, 2, 3\n"
    "v_mul_u32_u24 v5, 2560, v5\n"
    "v_and_b32 v24, 3, v0\n"
    "v_lshl_add_u32 v5, v24, 7, v5\n"
    "v_and_b32 v24, 15, v0\n"
    "v_mul_u32_u24 v24, 1040, v24\n"
    "v_lshrrev_b32 v25, 4, v0\n"
    "v_lshlrev_b32 v25, 3, v25\n"
    "s_lshl_b32 s58, s53, 5\n"
    "s_add_u32 s58, s58, s66\n"
    "s_add_u32 s58, s58, 16640\n"
    "v_add3_u32 v24, v24, v25, s58\n"
    "ds_read_b64 v[108:109], v24 offset:33280\n"
    "ds_read_b64 v[110:111], v24 offset:49920\n"
    "ds_read_b64 v[124:125], v24\n"
    "ds_read_b64 v[126:127], v24 offset:16640\n"
    "s_waitcnt lgkmcnt(0)\n"
    "s_barrier\n"
    "s_setprio 3\n"
    "; ---- Q projection of head 4j+hh: tokens 32..63 (for the partner wave, kept in v120..123) first, then tokens 0..31\n"
    "ds_read_b64_tr_b16 v[32:33], v97 offset:0\n"
    "ds_read_b64_tr_b16 v[34:35], v97 offset:512\n"
    "ds_read_b64_tr_b16 v[36:37], v97 offset:1040\n"
    "ds_read_b64_tr_b16 v[38:39], v97 offset:1552\n"
    "ds_read_b64_tr_b16 v[40:41], v97 offset:2080\n"
    "ds_read_b64_tr_b16 v[42:43], v97 offset:2592\n"
    "s_waitcnt lgkmcnt(4)\n"
    "v_mfma_f32_16x16x32_bf16 v[112:115], v[32:35], v[128:131], 0\n"
    "ds_read_b64_tr_b16 v[44:45], v97 offset:3120\n"
    "ds_read_b64_tr_b16 v[46:47], v97 offset:3632\n"
    "v_mfma_f32_16x16x32_bf16 v[116:119], v[32:35], v[132:135], 0\n"
    "ds_read_b128 v[128:131], v96 offset:33280\n"
    "v_mfma_f32_16x16x32_bf16 v[16:19], v[32:35], v[20:23], 0\n"
    "ds_read_b128 v[132:135], v96 offset:49920\n"
    "s_waitcnt lgkmcnt(6)\n"
    "v_mfma_f32_16x16x32_bf16 v[112:115], v[36:39], v[136:139], v[112:115]\n"
    "ds_read_b64_tr_b16 v[48:49], v97 offset:4160\n"
    "ds_read_b64_tr_b16 v[50:51], v97 offset:4672\n"
    "v_mfma_f32_16x16x32_bf16 v[116:119], v[36:39], v[140:143], v[116:119]\n"
    "ds_read_b128 v[136:139], v96 offset:33296\n"
    "v_mfma_f32_16x16x32_bf16 v[16:19], v[36:39], v[20:23], v[16:19]\n"
    "ds_read_b128 v[140:143], v96 offset:49936\n"
    "s_waitcnt lgkmcnt(8)\n"
    "v_mfma_f32_16x16x32_bf16 v[112:115], v[40:43], v[144:147], v[112:115]\n"
    "ds_read_b64_tr_b16 v[52:53], v97 offset:5200\n"
    "ds_read_b64_tr_b16 v[54:55], v97 offset:5712\n"
    "v_mfma_f32_16x16x32_bf16 v[116:119], v[40:43], v[148:151], v[116:119]\n"
    "ds_read_b128 v[144:147], v96 offset:33312\n"
    "v_mfma_f32_16x16x32_bf16 v[16:19], v[40:43], v[20:23], v[16:19]\n"
    "ds_read_b128 v[148:151], v96 offset:49952\n"
    "s_waitcnt lgkmcnt(10)\n"
    "v_mfma_f32_16x16x32_bf16 v[112:115], v[44:47], v[152:155], v[112:115]\n"
    "ds_read_b64_tr_b16 v[56:57], v97 offset:6240\n"
    "ds_read_b64_tr_b16 v[58:59], v97 offset:6752\n"
    "v_mfma_f32_16x16x32_bf16 v[116:119], v[44:47], v[156:159], v[116:119]\n"
    "ds_read_b128 v[152:155], v96 offset:33328\n"
    "v_mfma_f32_16x16x32_bf16 v[16:19], v[44:47], v[20:23], v[16:19]\n"
    "ds_read_b128 v[156:159], v96 offset:49968\n"
    "s_waitcnt lgkmcnt(10)\n"
    "v_mfma_f32_16x16x32_bf16 v[112:115], v[48:51], v[160:163], v[112:115]\n"
    "ds_read_b64_tr_b16 v[60:61], v97 offset:7280\n"
    "ds_read_b64_tr_b16 v[62:63], v97 offset:7792\n"
    "v_mfma_f32_16x16x32_bf16 v[116:119], v[48:51], v[164:167], v[116:119]\n"
    "ds_read_b128 v[160:163], v96 offset:33344\n"
    "v_mfma_f32_16x16x32_bf16 v[16:19], v[48:51], v[20:23], v[16:19]\n"
    "ds_read_b128 v[164:167], v96 offset:49984\n"
    "s_waitcnt lgkmcnt(10)\n"
    "v_mfma_f32_16x16x32_bf16 v[112:115], v[52:55], v[168:171], v[112:115]\n"
    "ds_read_b64_tr_b16 v[64:65], v97 offset:8320\n"
    "ds_read_b64_tr_b16 v[66:67], v97 offset:8832\n"
    "v_mfma_f32_16x16x32_bf16 v[116:119], v[52:55], v[172:175], v[116:119]\n"
    "ds_read_b128 v[168:171], v96 offset:33360\n"
    "v_mfma_f32_16x16x32_bf16 v[16:19], v[52:55], v[20:23], v[16:19]\n"
    "ds_read_b128 v[172:175], v96 offset:50000\n"
    "s_waitcnt lgkmcnt(10)\n"
    "v_mfma_f32_16x16x32_bf16 v[112:115], v[56:59], v[176:179], v[112:115]\n"
    "ds_read_b64_tr_b16 v[68:69], v97 offset:9360\n"
    "ds_read_b64_tr_b16 v[70:71], v97 offset:9872\n"
    "v_mfma_f32_16x16x32_bf16 v[116:119], v[56:59], v[180:183], v[116:119]\n"
    "ds_read_b128 v[176:179], v96 offset:33376\n"
    "v_mfma_f32_16x16x32_bf16 v[16:19], v[56:59], v[20:23], v[16:19]\n"
    "ds_read_b128 v[180:183], v96 offset:50016\n"
    "s_waitcnt lgkmcnt(10)\n"
    "v_mfma_f32_16x16x32_bf16 v[112:115], v[60:63], v[184:187], v[112:115]\n"
    "ds_read_b64_tr_b16 v[72:73], v97 offset:10400\n"
    "ds_read_b64_tr_b16 v[74:75], v97 offset:10912\n"
    "v_mfma_f32_16x16x32_bf16 v[116:119], v[60:63], v[188:191], v[116:119]\n"
    "ds_read_b128 v[184:187], v96 offset:33392\n"
    "v_mfma_f32_16x16x32_bf16 v[16:19], v[60:63], v[20:23], v[16:19]\n"
    "ds_read_b128 v[188:191], v96 offset:50032\n"
    "s_waitcnt lgkmcnt(10)\n"
    "v_mfma_f32_16x16x32_bf16 v[112:115], v[64:67], v[192:195], v[112:115]\n"
    "ds_read_b64_tr_b16 v[76:77], v97 offset:11440\n"
    "ds_read_b64_tr_b16 v[78:79], v97 offset:11952\n"
    "v_mfma_f32_16x16x32_bf16 v[116:119], v[64:67], v[196:199], v[116:119]\n"
    "ds_read_b128 v[192:195], v96 offset:33408\n"
    "v_mfma_f32_16x16x32_bf16 v[16:19], v[64:67], v[20:23], v[16:19]\n"
    "ds_read_b128 v[196:199], v96 offset:50048\n"
    "s_waitcnt lgkmcnt(10)\n"
    "v_mfma_f32_16x16x32_bf16 v[112:115], v[68:71], v[200:203], v[112:115]\n"
    "ds_read_b64_tr_b16 v[80:81], v97 offset:12480\n"
    "ds_read_b64_tr_b16 v[82:83], v97 offset:12992\n"
    "v_mfma_f32_16x16x32_bf16 v[116:119], v[68:71], v[204:207], v[116:119]\n"
    "ds_read_b128 v[200:203], v96 offset:33424\n"
    "v_mfma_f32_16x16x32_bf16 v[16:19], v[68:71], v[20:23], v[16:19]\n"
    "ds_read_b128 v[204:207], v96 offset:50064\n"
    "s_waitcnt lgkmcnt(10)\n"
    "v_mfma_f32_16x16x32_bf16 v[112:115], v[72:75], v[208:211], v[112:115]\n"
    "ds_read_b64_tr_b16 v[84:85], v97 offset:13520\n"
    "ds_read_b64_tr_b16 v[86:87], v97 offset:14032\n"
    "v_mfma_f32_16x16x32_bf16 v[116:119], v[72:75], v[212:215], v[116:119]\n"
    "ds_read_b128 v[208:211], v96 offset:33440\n"
    "v_mfma_f32_16x16x32_bf16 v[16:19], v[72:75], v[20:23], v[16:19]\n"
    "ds_read_b128 v[212:215], v96 offset:50080\n"
    "s_waitcnt lgkmcnt(10)\n"
    "v_mfma_f32_16x16x32_bf16 v[112:115], v[76:79], v[216:219], v[112:115]\n"
    "ds_read_b64_tr_b16 v[88:89], v97 offset:14560\n"
    "ds_read_b64_tr_b16 v[90:91], v97 offset:15072\n"
    "v_mfma_f32_16x16x32_bf16 v[116:119], v[76:79], v[220:223], v[116:119]\n"
    "ds_read_b128 v[216:219], v96 offset:33456\n"
    "v_mfma_f32_16x16x32_bf16 v[16:19], v[76:79], v[20:23], v[16:19]\n"
    "ds_read_b128 v[220:223], v96 offset:50096\n"
    "s_waitcnt lgkmcnt(10)\n"
    "v_mfma_f32_16x16x32_bf16 v[112:115], v[80:83], v[224:227], v[112:115]\n"
    "ds_read_b64_tr_b16 v[92:93], v97 offset:15600\n"
    "ds_read_b64_tr_b16 v[94:95], v97 offset:16112\n"
    "v_mfma_f32_16x16x32_bf16 v[116:119], v[80:83], v[228:231], v[116:119]\n"
    "ds_read_b128 v[224:227], v96 offset:33472\n"
    "v_mfma_f32_16x16x32_bf16 v[16:19], v[80:83], v[20:23], v[16:19]\n"
    "ds_read_b128 v[228:231], v96 offset:50112\n"
    "s_waitcnt lgkmcnt(10)\n"
    "v_mfma_f32_16x16x32_bf16 v[112:115], v[84:87], v[232:235], v[112:115]\n"
    "ds_read_b64 v[100:101], v98 offset:256\n"
    "ds_read_b64 v[102:103], v98 offset:384\n"
    "v_mfma_f32_16x16x32_bf16 v[116:119], v[84:87], v[236:239], v[116:119]\n"
    "ds_read_b128 v[232:235], v96 offset:33488\n"
    "v_mfma_f32_16x16x32_bf16 v[16:19], v[84:87], v[20:23], v[16:19]\n"
    "ds_read_b128 v[236:239], v96 offset:50128\n"
    "s_waitcnt lgkmcnt(10)\n"
    "v_mfma_f32_16x16x32_bf16 v[112:115], v[88:91], v[240:243], v[112:115]\n"
    "ds_read_b64 v[104:105], v98 offset:0\n"
    "ds_read_b64 v[106:107], v98 offset:128\n"
    "v_mfma_f32_16x16x32_bf16 v[116:119], v[88:91], v[244:247], v[116:119]\n"
    "ds_read_b128 v[240:243], v96 offset:33504\n"
    "v_mfma_f32_16x16x32_bf16 v[16:19], v[88:91], v[20:23], v[16:19]\n"
    "ds_read_b128 v[244:247], v96 offset:50144\n"
    "s_waitcnt lgkmcnt(10)\n"
    "v_mfma_f32_16x16x32_bf16 v[112:115], v[92:95], v[248:251], v[112:115]\n"
    "v_mfma_f32_16x16x32_bf16 v[116:119], v[92:95], v[252:255], v[116:119]\n"
    "ds_read_b128 v[248:251], v96 offset:33520\n"
    "v_mfma_f32_16x16x32_bf16 v[16:19], v[92:95], v[20:23], v[16:19]\n"
    "ds_read_b128 v[252:255], v96 offset:50160\n"
    "v_mfma_f32_16x16x32_bf16 v[24:27], v[32:35], v[128:131], 0\n"
    "v_mfma_f32_16x16x32_bf16 v[28:31], v[32:35], v[132:135], 0\n"
    "v_mfma_f32_16x16x32_bf16 v[24:27], v[36:39], v[136:139], v[24:27]\n"
    "v_mfma_f32_16x16x32_bf16 v[28:31], v[36:39], v[140:143], v[28:31]\n"
    "v_mfma_f32_16x16x32_bf16 v[24:27], v[40:43], v[144:147], v[24:27]\n"
    "v_mov_b32 v128, 0\n"
    "v_mfma_f32_16x16x32_bf16 v[28:31], v[40:43], v[148:151], v[28:31]\n"
    "v_mov_b32 v129, 0\n"
    "v_mfma_f32_16x16x32_bf16 v[24:27], v[44:47], v[152:155], v[24:27]\n"
    "s_waitcnt lgkmcnt(9)\n"
    "v_mul_f32 v101, 0x3eb8aa3b, v101\n"
    "v_mov_b32 v130, 0\n"
    "v_mfma_f32_16x16x32_bf16 v[28:31], v[44:47], v[156:159], v[28:31]\n"
    "s_waitcnt lgkmcnt(8)\n"
    "v_mul_f32 v103, 0x3eb8aa3b, v103\n"
    "v_mov_b32 v131, 0\n"
    "v_mfma_f32_16x16x32_bf16 v[24:27], v[48:51], v[160:163], v[24:27]\n"
    "v_fma_f32 v112, -v100, v16, v112\n"
    "v_mov_b32 v132, 0\n"
    "v_mfma_f32_16x16x32_bf16 v[28:31], v[48:51], v[164:167], v[28:31]\n"
    "v_fma_f32 v113, -v100, v17, v113\n"
    "v_mov_b32 v133, 0\n"
    "v_mfma_f32_16x16x32_bf16 v[24:27], v[52:55], v[168:171], v[24:27]\n"
    "v_fma_f32 v114, -v100, v18, v114\n"
    "v_mov_b32 v134, 0\n"
    "v_mfma_f32_16x16x32_bf16 v[28:31], v[52:55], v[172:175], v[28:31]\n"
    "v_fma_f32 v115, -v100, v19, v115\n"
    "v_mov_b32 v135, 0\n"
    "v_mfma_f32_16x16x32_bf16 v[24:27], v[56:59], v[176:179], v[24:27]\n"
    "v_fma_f32 v116, -v102, v16, v116\n"
    "v_mov_b32 v136, 0\n"
    "v_mfma_f32_16x16x32_bf16 v[28:31], v[56:59], v[180:183], v[28:31]\n"
    "v_fma_f32 v117, -v102, v17, v117\n"
    "v_mov_b32 v137, 0\n"
    "v_mfma_f32_16x16x32_bf16 v[24:27], v[60:63], v[184:187], v[24:27]\n"
    "v_fma_f32 v118, -v102, v18, v118\n"
    "v_mov_b32 v138, 0\n"
    "v_mfma_f32_16x16x32_bf16 v[28:31], v[60:63], v[188:191], v[28:31]\n"
    "v_fma_f32 v119, -v102, v19, v119\n"
    "v_mov_b32 v139, 0\n"
    "v_mfma_f32_16x16x32_bf16 v[24:27], v[64:67], v[192:195], v[24:27]\n"
    "v_mul_f32 v112, v101, v112\n"
    "v_mov_b32 v140, 0\n"
    "v_mfma_f32_16x16x32_bf16 v[28:31], v[64:67], v[196:199], v[28:31]\n"
    "v_mul_f32 v113, v101, v113\n"
    "v_mov_b32 v141, 0\n"
    "v_mfma_f32_16x16x32_bf16 v[24:27], v[68:71], v[200:203], v[24:27]\n"
    "v_mul_f32 v114, v101, v114\n"
    "v_mov_b32 v142, 0\n"
    "v_mfma_f32_16x16x32_bf16 v[28:31], v[68:71], v[204:207], v[28:31]\n"
    "v_mul_f32 v115, v101, v115\n"
    "v_mov_b32 v143, 0\n"
    "v_mfma_f32_16x16x32_bf16 v[24:27], v[72:75], v[208:211], v[24:27]\n"
    "v_mul_f32 v116, v103, v116\n"
    "v_mov_b32 v48, 0\n"
    "v_mfma_f32_16x16x32_bf16 v[28:31], v[72:75], v[212:215], v[28:31]\n"
    "v_mul_f32 v117, v103, v117\n"
    "v_mov_b32 v49, 0\n"
    "v_mfma_f32_16x16x32_bf16 v[24:27], v[76:79], v[216:219], v[24:27]\n"
    "v_mul_f32 v118, v103, v118\n"
    "v_mov_b32 v50, 0\n"
    "v_mfma_f32_16x16x32_bf16 v[28:31], v[76:79], v[220:223], v[28:31]\n"
    "v_mul_f32 v119, v103, v119\n"
    "v_mov_b32 v51, 0\n"
    "v_mfma_f32_16x16x32_bf16 v[24:27], v[80:83], v[224:227], v[24:27]\n"
    "v_cvt_pk_bf16_f32 v120, v112, v113\n"
    "v_mov_b32 v52, 0\n"
    "v_mfma_f32_16x16x32_bf16 v[28:31], v[80:83], v[228:231], v[28:31]\n"
    "v_cvt_pk_bf16_f32 v122, v116, v117\n"
    "v_mov_b32 v53, 0\n"
    "s_waitcnt lgkmcnt(7)\n"
    "v_mfma_f32_16x16x32_bf16 v[24:27], v[84:87], v[232:235], v[24:27]\n"
    "v_cvt_pk_bf16_f32 v121, v114, v115\n"
    "v_mov_b32 v54, 0\n"
    "s_waitcnt lgkmcnt(6)\n"
    "v_mfma_f32_16x16x32_bf16 v[28:31], v[84:87], v[236:239], v[28:31]\n"
    "v_cvt_pk_bf16_f32 v123, v118, v119\n"
    "v_mov_b32 v55, 0\n"
    "s_waitcnt lgkmcnt(3)\n"
    "v_mfma_f32_16x16x32_bf16 v[24:27], v[88:91], v[240:243], v[24:27]\n"
    "v_permlane16_swap_b32 v120, v122\n"
    "v_mov_b32 v56, 0\n"
    "s_waitcnt lgkmcnt(2)\n"
    "v_mfma_f32_16x16x32_bf16 v[28:31], v[88:91], v[244:247], v[28:31]\n"
    "v_permlane16_swap_b32 v121, v123\n"
    "v_mov_b32 v57, 0\n"
    "s_waitcnt lgkmcnt(1)\n"
    "v_mfma_f32_16x16x32_bf16 v[24:27], v[92:95], v[248:251], v[24:27]\n"
    "v_mul_f32 v105, 0x3eb8aa3b, v105\n"
    "v_mov_b32 v58, 0\n"
    "s_waitcnt lgkmcnt(0)\n"
    "v_mfma_f32_16x16x32_bf16 v[28:31], v[92:95], v[252:255], v[28:31]\n"
    "v_mul_f32 v107, 0x3eb8aa3b, v107\n"
    "v_mov_b32 v59, 0\n"
    "v_mov_b32 v60, 0\n"
    "v_mov_b32 v61, 0\n"
    "v_mov_b32 v62, 0\n"
    "v_mov_b32 v63, 0\n"
    "v_fma_f32 v24, -v104, v16, v24\n"
    "v_fma_f32 v25, -v104, v17, v25\n"
    "v_fma_f32 v26, -v104, v18, v26\n"
    "v_fma_f32 v27, -v104, v19, v27\n"
    "v_fma_f32 v28, -v106, v16, v28\n"
    "v_fma_f32 v29, -v106, v17, v29\n"
    "v_fma_f32 v30, -v106, v18, v30\n"
    "v_fma_f32 v31, -v106, v19, v31\n"
    "v_mul_f32 v24, v105, v24\n"
    "v_mul_f32 v25, v105, v25\n"
    "v_mul_f32 v26, v105, v26\n"
    "v_mul_f32 v27, v105, v27\n"
    "v_mul_f32 v28, v107, v28\n"
    "v_mul_f32 v29, v107, v29\n"
    "v_mul_f32 v30, v107, v30\n"
    "v_mul_f32 v31, v107, v31\n"
    "v_cvt_pk_bf16_f32 v8, v24, v25\n"
    "v_cvt_pk_bf16_f32 v10, v28, v29\n"
    "v_cvt_pk_bf16_f32 v9, v26, v27\n"
    "v_cvt_pk_bf16_f32 v11, v30, v31\n"
    "v_permlane16_swap_b32 v8, v10\n"
    "s_nop 0\n"
    "v_permlane16_swap_b32 v9, v11\n"
    "s_setprio 1\n"
    "v_add_u32 v98, s18, v99\n"
    "s_barrier\n"
    "ds_read_b128 v[64:67], v12 offset:0\n"
    "ds_read_b128 v[68:71], v13 offset:0\n"
    "s_waitcnt lgkmcnt(1)\n"
    "v_mfma_f32_32x32x16_bf16 v[16:31], v[64:67], v[8:11], -4.0\n"
    "ds_read_b128 v[64:67], v12 offset:2080\n"
    "ds_read_b128 v[72:75], v13 offset:2080\n"
    "s_nop 9\n"
    "v_exp_f32 v16, v16\n"
    "v_exp_f32 v17, v17\n"
    "v_exp_f32 v18, v18\n"
    "s_waitcnt lgkmcnt(1)\n"
    "ds_read_b128 v[76:79], v14 offset:0\n"
    "v_exp_f32 v19, v19\n"
    "v_exp_f32 v20, v20\n"
    "v_exp_f32 v21, v21\n"
    "v_mfma_f32_32x32x16_bf16 v[32:47], v[64:67], v[8:11], -4.0\n"
    "ds_read_b128 v[64:67], v12 offset:4160\n"
    "v_exp_f32 v22, v22\n"
    "v_exp_f32 v23, v23\n"
    "v_exp_f32 v24, v24\n"
    "v_exp_f32 v25, v25\n"
    "v_exp_f32 v26, v26\n"
    "v_smfmac_f32_32x32x32_bf16 v[48:63], v[68:71], v[16:23], v15\n"
    "ds_read_b128 v[68:71], v13 offset:4160\n"
    "v_exp_f32 v27, v27\n"
    "v_exp_f32 v28, v28\n"
    "v_exp_f32 v29, v29\n"
    "v_exp_f32 v30, v30\n"
    "v_exp_f32 v31, v31\n"
    "v_exp_f32 v32, v32\n"
    "v_exp_f32 v33, v33\n"
    "v_exp_f32 v34, v34\n"
    "s_waitcnt lgkmcnt(1)\n"
    "v_smfmac_f32_32x32x32_bf16 v[48:63], v[76:79], v[24:31], v15\n"
    "ds_read_b128 v[76:79], v14 offset:2080\n"
    "v_exp_f32 v35, v35\n"
    "v_exp_f32 v36, v36\n"
    "v_exp_f32 v37, v37\n"
    "v_mfma_f32_32x32x16_bf16 v[16:31], v[64:67], v[8:11], -4.0\n"
    "ds_read_b128 v[64:67], v12 offset:6240\n"
    "v_exp_f32 v38, v38\n"
    "v_exp_f32 v39, v39\n"
    "v_exp_f32 v40, v40\n"
    "v_exp_f32 v41, v41\n"
    "v_exp_f32 v42, v42\n"
    "v_smfmac_f32_32x32x32_bf16 v[48:63], v[72:75], v[32:39], v15\n"
    "ds_read_b128 v[72:75], v13 offset:6240\n"
    "v_exp_f32 v43, v43\n"
    "v_exp_f32 v44, v44\n"
    "v_exp_f32 v45, v45\n"
    "v_exp_f32 v46, v46\n"
    "v_exp_f32 v47, v47\n"
    "v_exp_f32 v16, v16\n"
    "v_exp_f32 v17, v17\n"
    "v_exp_f32 v18, v18\n"
    "s_waitcnt lgkmcnt(1)\n"
    "v_smfmac_f32_32x32x32_bf16 v[48:63], v[76:79], v[40:47], v15\n"
    "ds_read_b128 v[76:79], v14 offset:4160\n"
    "v_exp_f32 v19, v19\n"
    "v_exp_f32 v20, v20\n"
    "v_exp_f32 v21, v21\n"
    "v_mfma_f32_32x32x16_bf16 v[32:47], v[64:67], v[8:11], -4.0\n"
    "ds_read_b128 v[64:67], v12 offset:8320\n"
    "v_exp_f32 v22, v22\n"
    "v_exp_f32 v23, v23\n"
    "v_exp_f32 v24, v24\n"
    "v_exp_f32 v25, v25\n"
    "v_exp_f32 v26, v26\n"
    "v_smfmac_f32_32x32x32_bf16 v[48:63], v[68:71], v[16:23], v15\n"
    "ds_read_b128 v[68:71], v13 offset:8320\n"
    "v_exp_f32 v27, v27\n"
    "v_exp_f32 v28, v28\n"
    "v_exp_f32 v29, v29\n"
    "v_exp_f32 v30, v30\n"
    "v_exp_f32 v31, v31\n"
    "s_barrier\n"
    "v_exp_f32 v32, v32\n"
    "v_exp_f32 v33, v33\n"
    "v_exp_f32 v34, v34\n"
    "s_waitcnt lgkmcnt(1)\n"
    "v_smfmac_f32_32x32x32_bf16 v[48:63], v[76:79], v[24:31], v15\n"
    "ds_read_b128 v[76:79], v14 offset:6240\n"
    "v_exp_f32 v35, v35\n"
    "v_exp_f32 v36, v36\n"
    "v_exp_f32 v37, v37\n"
    "v_mfma_f32_32x32x16_bf16 v[16:31], v[64:67], v[8:11], -4.0\n"
    "ds_read_b128 v[64:67], v12 offset:10400\n"
    "v_exp_f32 v38, v38\n"
    "v_exp_f32 v39, v39\n"
    "v_exp_f32 v40, v40\n"
    "v_exp_f32 v41, v41\n"
    "v_exp_f32 v42, v42\n"
    "v_smfmac_f32_32x32x32_bf16 v[48:63], v[72:75], v[32:39], v15\n"
    "ds_read_b128 v[72:75], v13 offset:10400\n"
    "v_exp_f32 v43, v43\n"
    "v_exp_f32 v44, v44\n"
    "v_exp_f32 v45, v45\n"
    "v_exp_f32 v46, v46\n"
    "v_exp_f32 v47, v47\n"
    "v_exp_f32 v16, v16\n"
    "v_exp_f32 v17, v17\n"
    "v_exp_f32 v18, v18\n"
    "s_waitcnt lgkmcnt(1)\n"
    "v_smfmac_f32_32x32x32_bf16 v[48:63], v[76:79], v[40:47], v15\n"
    "ds_read_b128 v[76:79], v14 offset:8320\n"
    "v_exp_f32 v19, v19\n"
    "v_exp_f32 v20, v20\n"
    "v_exp_f32 v21, v21\n"
    "v_mfma_f32_32x32x16_bf16 v[32:47], v[64:67], v[8:11], -4.0\n"
    "ds_read_b128 v[64:67], v12 offset:12480\n"
    "v_exp_f32 v22, v22\n"
    "v_exp_f32 v23, v23\n"
    "v_exp_f32 v24, v24\n"
    "v_exp_f32 v25, v25\n"
    "v_exp_f32 v26, v26\n"
    "v_smfmac_f32_32x32x32_bf16 v[48:63], v[68:71], v[16:23], v15\n"
    "ds_read_b128 v[68:71], v13 offset:12480\n"
    "v_exp_f32 v27, v27\n"
    "v_exp_f32 v28, v28\n"
    "v_exp_f32 v29, v29\n"
    "v_exp_f32 v30, v30\n"
    "v_exp_f32 v31, v31\n"
    "v_exp_f32 v32, v32\n"
    "v_exp_f32 v33, v33\n"
    "v_exp_f32 v34, v34\n"
    "s_waitcnt lgkmcnt(1)\n"
    "v_smfmac_f32_32x32x32_bf16 v[48:63], v[76:79], v[24:31], v15\n"
    "ds_read_b128 v[76:79], v14 offset:10400\n"
    "v_exp_f32 v35, v35\n"
    "v_exp_f32 v36, v36\n"
    "v_exp_f32 v37, v37\n"
    "v_mfma_f32_32x32x16_bf16 v[16:31], v[64:67], v[8:11], -4.0\n"
    "ds_read_b128 v[64:67], v12 offset:14560\n"
    "v_exp_f32 v38, v38\n"
    "v_exp_f32 v39, v39\n"
    "v_exp_f32 v40, v40\n"
    "v_exp_f32 v41, v41\n"
    "v_exp_f32 v42, v42\n"
    "v_smfmac_f32_32x32x32_bf16 v[48:63], v[72:75], v[32:39], v15\n"
    "ds_read_b128 v[72:75], v13 offset:14560\n"
    "v_exp_f32 v43, v43\n"
    "v_exp_f32 v44, v44\n"
    "v_exp_f32 v45, v45\n"
    "v_exp_f32 v46, v46\n"
    "v_exp_f32 v47, v47\n"
    "v_exp_f32 v16, v16\n"
    "v_exp_f32 v17, v17\n"
    "v_exp_f32 v18, v18\n"
    "s_waitcnt lgkmcnt(1)\n"
    "v_smfmac_f32_32x32x32_bf16 v[48:63], v[76:79], v[40:47], v15\n"
    "ds_read_b128 v[76:79], v14 offset:12480\n"
    "v_exp_f32 v19, v19\n"
    "v_exp_f32 v20, v20\n"
    "v_exp_f32 v21, v21\n"
    "v_mfma_f32_32x32x16_bf16 v[32:47], v[64:67], v[8:11], -4.0\n"
    "ds_read_b128 v[64:67], v12 offset:16640\n"
    "v_exp_f32 v22, v22\n"
    "v_exp_f32 v23, v23\n"
    "v_exp_f32 v24, v24\n"
    "v_exp_f32 v25, v25\n"
    "v_exp_f32 v26, v26\n"
    "v_smfmac_f32_32x32x32_bf16 v[48:63], v[68:71], v[16:23], v15\n"
    "ds_read_b128 v[68:71], v13 offset:16640\n"
    "v_exp_f32 v27, v27\n"
    "v_exp_f32 v28, v28\n"
    "v_exp_f32 v29, v29\n"
    "v_exp_f32 v30, v30\n"
    "v_exp_f32 v31, v31\n"
    "v_exp_f32 v32, v32\n"
    "v_exp_f32 v33, v33\n"
    "v_exp_f32 v34, v34\n"
    "s_waitcnt lgkmcnt(1)\n"
    "v_smfmac_f32_32x32x32_bf16 v[48:63], v[76:79], v[24:31], v15\n"
    "ds_read_b128 v[76:79], v14 offset:14560\n"
    "v_exp_f32 v35, v35\n"
    "v_exp_f32 v36, v36\n"
    "v_exp_f32 v37, v37\n"
    "v_mfma_f32_32x32x16_bf16 v[16:31], v[64:67], v[8:11], -4.0\n"
    "ds_read_b128 v[64:67], v12 offset:18720\n"
    "v_exp_f32 v38, v38\n"
    "v_exp_f32 v39, v39\n"
    "v_exp_f32 v40, v40\n"
    "v_exp_f32 v41, v41\n"
    "v_exp_f32 v42, v42\n"
    "v_smfmac_f32_32x32x32_bf16 v[48:63], v[72:75], v[32:39], v15\n"
    "ds_read_b128 v[72:75], v13 offset:18720\n"
    "v_exp_f32 v43, v43\n"
    "v_exp_f32 v44, v44\n"
    "v_exp_f32 v45, v45\n"
    "v_exp_f32 v46, v46\n"
    "v_exp_f32 v47, v47\n"
    "v_exp_f32 v16, v16\n"
    "v_exp_f32 v17, v17\n"
    "v_exp_f32 v18, v18\n"
    "s_waitcnt lgkmcnt(1)\n"
    "v_smfmac_f32_32x32x32_bf16 v[48:63], v[76:79], v[40:47], v15\n"
    "ds_read_b128 v[76:79], v14 offset:16640\n"
    "v_exp_f32 v19, v19\n"
    "v_exp_f32 v20, v20\n"
    "v_exp_f32 v21, v21\n"
    "v_mfma_f32_32x32x16_bf16 v[32:47], v[64:67], v[8:11], -4.0\n"
    "ds_read_b128 v[64:67], v12 offset:20800\n"
    "v_exp_f32 v22, v22\n"
    "v_exp_f32 v23, v23\n"
    "v_exp_f32 v24, v24\n"
    "v_exp_f32 v25, v25\n"
    "v_exp_f32 v26, v26\n"
    "v_smfmac_f32_32x32x32_bf16 v[48:63], v[68:71], v[16:23], v15\n"
    "ds_read_b128 v[68:71], v13 offset:20800\n"
    "v_exp_f32 v27, v27\n"
    "v_exp_f32 v28, v28\n"
    "v_exp_f32 v29, v29\n"
    "v_exp_f32 v30, v30\n"
    "v_exp_f32 v31, v31\n"
    "s_barrier\n"
    "ds_write_b128 v99, v[120:123]\n"
    "ds_write_b128 v99, v[8:11] offset:4096\n"
    "v_exp_f32 v32, v32\n"
    "v_exp_f32 v33, v33\n"
    "v_exp_f32 v34, v34\n"
    "s_waitcnt lgkmcnt(3)\n"
    "v_smfmac_f32_32x32x32_bf16 v[48:63], v[76:79], v[24:31], v15\n"
    "ds_read_b128 v[76:79], v14 offset:18720\n"
    "v_exp_f32 v35, v35\n"
    "v_exp_f32 v36, v36\n"
    "v_exp_f32 v37, v37\n"
    "v_mfma_f32_32x32x16_bf16 v[16:31], v[64:67], v[8:11], -4.0\n"
    "ds_read_b128 v[64:67], v12 offset:22880\n"
    "v_exp_f32 v38, v38\n"
    "v_exp_f32 v39, v39\n"
    "v_exp_f32 v40, v40\n"
    "v_exp_f32 v41, v41\n"
    "v_exp_f32 v42, v42\n"
    "v_smfmac_f32_32x32x32_bf16 v[48:63], v[72:75], v[32:39], v15\n"
    "ds_read_b128 v[72:75], v13 offset:22880\n"
    "v_exp_f32 v43, v43\n"
    "v_exp_f32 v44, v44\n"
    "v_exp_f32 v45, v45\n"
    "v_exp_f32 v46, v46\n"
    "v_exp_f32 v47, v47\n"
    "v_exp_f32 v16, v16\n"
    "v_exp_f32 v17, v17\n"
    "v_exp_f32 v18, v18\n"
    "s_waitcnt lgkmcnt(1)\n"
    "v_smfmac_f32_32x32x32_bf16 v[48:63], v[76:79], v[40:47], v15\n"
    "ds_read_b128 v[76:79], v14 offset:20800\n"
    "v_exp_f32 v19, v19\n"
    "v_exp_f32 v20, v20\n"
    "v_exp_f32 v21, v21\n"
    "v_mfma_f32_32x32x16_bf16 v[32:47], v[64:67], v[8:11], -4.0\n"
    "v_add_u32 v12, s19, v12\n"
    "ds_read_b128 v[64:67], v12 offset:24960\n"
    "s_barrier\n"
    "ds_read_b128 v[144:147], v98 offset:4096\n"
    "v_exp_f32 v22, v22\n"
    "v_exp_f32 v23, v23\n"
    "v_exp_f32 v24, v24\n"
    "v_exp_f32 v25, v25\n"
    "v_exp_f32 v26, v26\n"
    "v_smfmac_f32_32x32x32_bf16 v[48:63], v[68:71], v[16:23], v15\n"
    "v_add_u32 v13, s19, v13\n"
    "ds_read_b128 v[68:71], v13 offset:24960\n"
    "v_exp_f32 v27, v27\n"
    "v_exp_f32 v28, v28\n"
    "v_exp_f32 v29, v29\n"
    "v_exp_f32 v30, v30\n"
    "v_exp_f32 v31, v31\n"
    "v_exp_f32 v32, v32\n"
    "v_exp_f32 v33, v33\n"
    "v_exp_f32 v34, v34\n"
    "s_waitcnt lgkmcnt(2)\n"
    "v_smfmac_f32_32x32x32_bf16 v[48:63], v[76:79], v[24:31], v15\n"
    "ds_read_b128 v[76:79], v14 offset:22880\n"
    "v_exp_f32 v35, v35\n"
    "v_exp_f32 v36, v36\n"
    "v_exp_f32 v37, v37\n"
    "s_waitcnt lgkmcnt(2)\n"
    "v_mfma_f32_32x32x16_bf16 v[16:31], v[64:67], v[144:147], -4.0\n"
    "v_exp_f32 v38, v38\n"
    "v_exp_f32 v39, v39\n"
    "v_exp_f32 v40, v40\n"
    "v_exp_f32 v41, v41\n"
    "v_exp_f32 v42, v42\n"
    "v_smfmac_f32_32x32x32_bf16 v[48:63], v[72:75], v[32:39], v15\n"
    "ds_read_b128 v[72:75], v13 offset:29120\n"
    "v_exp_f32 v43, v43\n"
    "v_exp_f32 v44, v44\n"
    "v_exp_f32 v45, v45\n"
    "v_exp_f32 v46, v46\n"
    "v_exp_f32 v47, v47\n"
    "v_exp_f32 v16, v16\n"
    "v_exp_f32 v17, v17\n"
    "v_exp_f32 v18, v18\n"
    "s_waitcnt lgkmcnt(1)\n"
    "v_smfmac_f32_32x32x32_bf16 v[48:63], v[76:79], v[40:47], v15\n"
    "v_add_u32 v14, s19, v14\n"
    "ds_read_b128 v[76:79], v14 offset:24960\n"
    "v_exp_f32 v19, v19\n"
    "v_exp_f32 v20, v20\n"
    "v_exp_f32 v21, v21\n"
    "v_mfma_f32_32x32x16_bf16 v[32:47], v[64:67], v[8:11], -4.0\n"
    "ds_read_b128 v[64:67], v12 offset:29120\n"
    "v_exp_f32 v22, v22\n"
    "v_exp_f32 v23, v23\n"
    "v_exp_f32 v24, v24\n"
    "v_exp_f32 v25, v25\n"
    "v_exp_f32 v26, v26\n"
    "v_smfmac_f32_32x32x32_bf16 v[128:143], v[68:71], v[16:23], v15\n"
    "v_exp_f32 v27, v27\n"
    "v_exp_f32 v28, v28\n"
    "v_exp_f32 v29, v29\n"
    "v_exp_f32 v30, v30\n"
    "v_exp_f32 v31, v31\n"
    "v_exp_f32 v32, v32\n"
    "v_exp_f32 v33, v33\n"
    "v_exp_f32 v34, v34\n"
    "s_waitcnt lgkmcnt(0)\n"
    "v_smfmac_f32_32x32x32_bf16 v[128:143], v[76:79], v[24:31], v15\n"
    "v_exp_f32 v35, v35\n"
    "v_exp_f32 v36, v36\n"
    "v_exp_f32 v37, v37\n"
    "v_mfma_f32_32x32x16_bf16 v[16:31], v[64:67], v[144:147], -4.0\n"
    "v_exp_f32 v38, v38\n"
    "v_exp_f32 v39, v39\n"
    "v_exp_f32 v40, v40\n"
    "v_exp_f32 v41, v41\n"
    "v_exp_f32 v42, v42\n"
    "v_smfmac_f32_32x32x32_bf16 v[48:63], v[68:71], v[32:39], v15\n"
    "ds_read_b128 v[68:71], v13 offset:33280\n"
    "v_exp_f32 v43, v43\n"
    "v_exp_f32 v44, v44\n"
    "v_exp_f32 v45, v45\n"
    "v_exp_f32 v46, v46\n"
    "v_exp_f32 v47, v47\n"
    "v_exp_f32 v16, v16\n"
    "v_exp_f32 v17, v17\n"
    "v_exp_f32 v18, v18\n"
    "v_smfmac_f32_32x32x32_bf16 v[48:63], v[76:79], v[40:47], v15\n"
    "ds_read_b128 v[76:79], v14 offset:29120\n"
    "v_exp_f32 v19, v19\n"
    "v_exp_f32 v20, v20\n"
    "v_exp_f32 v21, v21\n"
    "v_mfma_f32_32x32x16_bf16 v[32:47], v[64:67], v[8:11], -4.0\n"
    "ds_read_b128 v[64:67], v12 offset:33280\n"
    "v_exp_f32 v22, v22\n"
    "v_exp_f32 v23, v23\n"
    "v_exp_f32 v24, v24\n"
    "v_exp_f32 v25, v25\n"
    "v_exp_f32 v26, v26\n"
    "v_smfmac_f32_32x32x32_bf16 v[128:143], v[72:75], v[16:23], v15\n"
    "v_exp_f32 v27, v27\n"
    "v_exp_f32 v28, v28\n"
    "v_exp_f32 v29, v29\n"
    "v_exp_f32 v30, v30\n"
    "v_exp_f32 v31, v31\n"
    "v_exp_f32 v32, v32\n"
    "v_exp_f32 v33, v33\n"
    "v_exp_f32 v34, v34\n"
    "s_waitcnt lgkmcnt(0)\n"
    "v_smfmac_f32_32x32x32_bf16 v[128:143], v[76:79], v[24:31], v15\n"
    "v_exp_f32 v35, v35\n"
    "v_exp_f32 v36, v36\n"
    "v_exp_f32 v37, v37\n"
    "v_mfma_f32_32x32x16_bf16 v[16:31], v[64:67], v[144:147], -4.0\n"
    "v_exp_f32 v38, v38\n"
    "v_exp_f32 v39, v39\n"
    "v_exp_f32 v40, v40\n"
    "v_exp_f32 v41, v41\n"
    "v_exp_f32 v42, v42\n"
    "v_smfmac_f32_32x32x32_bf16 v[48:63], v[72:75], v[32:39], v15\n"
    "ds_read_b128 v[72:75], v13 offset:37440\n"
    "v_exp_f32 v43, v43\n"
    "v_exp_f32 v44, v44\n"
    "v_exp_f32 v45, v45\n"
    "v_exp_f32 v46, v46\n"
    "v_exp_f32 v47, v47\n"
    "v_exp_f32 v16, v16\n"
    "v_exp_f32 v17, v17\n"
    "v_exp_f32 v18, v18\n"
    "v_smfmac_f32_32x32x32_bf16 v[48:63], v[76:79], v[40:47], v15\n"
    "ds_read_b128 v[76:79], v14 offset:33280\n"
    "v_exp_f32 v19, v19\n"
    "v_exp_f32 v20, v20\n"
    "v_exp_f32 v21, v21\n"
    "v_mfma_f32_32x32x16_bf16 v[32:47], v[64:67], v[8:11], -4.0\n"
    "ds_read_b128 v[64:67], v12 offset:37440\n"
    "v_exp_f32 v22, v22\n"
    "v_exp_f32 v23, v23\n"
    "v_exp_f32 v24, v24\n"
    "v_exp_f32 v25, v25\n"
    "v_exp_f32 v26, v26\n"
    "v_smfmac_f32_32x32x32_bf16 v[128:143], v[68:71], v[16:23], v15\n"
    "v_exp_f32 v27, v27\n"
    "v_exp_f32 v28, v28\n"
    "v_exp_f32 v29, v29\n"
    "v_exp_f32 v30, v30\n"
    "v_exp_f32 v31, v31\n"
    "v_exp_f32 v32, v32\n"
    "v_exp_f32 v33, v33\n"
    "v_exp_f32 v34, v34\n"
    "s_waitcnt lgkmcnt(0)\n"
    "v_smfmac_f32_32x32x32_bf16 v[128:143], v[76:79], v[24:31], v15\n"
    "v_exp_f32 v35, v35\n"
    "v_exp_f32 v36, v36\n"
    "v_exp_f32 v37, v37\n"
    "v_mfma_f32_32x32x16_bf16 v[16:31], v[64:67], v[144:147], -4.0\n"
    "v_exp_f32 v38, v38\n"
    "v_exp_f32 v39, v39\n"
    "v_exp_f32 v40, v40\n"
    "v_exp_f32 v41, v41\n"
    "v_exp_f32 v42, v42\n"
    "v_smfmac_f32_32x32x32_bf16 v[48:63], v[68:71], v[32:39], v15\n"
    "ds_read_b128 v[68:71], v13 offset:41600\n"
    "v_exp_f32 v43, v43\n"
    "v_exp_f32 v44, v44\n"
    "v_exp_f32 v45, v45\n"
    "v_exp_f32 v46, v46\n"
    "v_exp_f32 v47, v47\n"
    "v_exp_f32 v16, v16\n"
    "v_exp_f32 v17, v17\n"
    "v_exp_f32 v18, v18\n"
    "v_smfmac_f32_32x32x32_bf16 v[48:63], v[76:79], v[40:47], v15\n"
    "ds_read_b128 v[76:79], v14 offset:37440\n"
    "v_exp_f32 v19, v19\n"
    "v_exp_f32 v20, v20\n"
    "v_exp_f32 v21, v21\n"
    "v_mfma_f32_32x32x16_bf16 v[32:47], v[64:67], v[8:11], -4.0\n"
    "ds_read_b128 v[64:67], v12 offset:41600\n"
    "v_exp_f32 v22, v22\n"
    "v_exp_f32 v23, v23\n"
    "v_exp_f32 v24, v24\n"
    "v_exp_f32 v25, v25\n"
    "v_exp_f32 v26, v26\n"
    "v_smfmac_f32_32x32x32_bf16 v[128:143], v[72:75], v[16:23], v15\n"
    "v_exp_f32 v27, v27\n"
    "v_exp_f32 v28, v28\n"
    "v_exp_f32 v29, v29\n"
    "v_exp_f32 v30, v30\n"
    "v_exp_f32 v31, v31\n"
    "v_exp_f32 v32, v32\n"
    "v_exp_f32 v33, v33\n"
    "v_exp_f32 v34, v34\n"
    "s_waitcnt lgkmcnt(0)\n"
    "v_smfmac_f32_32x32x32_bf16 v[128:143], v[76:79], v[24:31], v15\n"
    "v_exp_f32 v35, v35\n"
    "v_exp_f32 v36, v36\n"
    "v_exp_f32 v37, v37\n"
    "v_mfma_f32_32x32x16_bf16 v[16:31], v[64:67], v[144:147], -4.0\n"
    "v_exp_f32 v38, v38\n"
    "v_exp_f32 v39, v39\n"
    "v_exp_f32 v40, v40\n"
    "v_exp_f32 v41, v41\n"
    "v_exp_f32 v42, v42\n"
    "v_smfmac_f32_32x32x32_bf16 v[48:63], v[72:75], v[32:39], v15\n"
    "ds_read_b128 v[72:75], v13 offset:45760\n"
    "v_exp_f32 v43, v43\n"
    "v_exp_f32 v44, v44\n"
    "v_exp_f32 v45, v45\n"
    "v_exp_f32 v46, v46\n"
    "v_exp_f32 v47, v47\n"
    "v_exp_f32 v16, v16\n"
    "v_exp_f32 v17, v17\n"
    "v_exp_f32 v18, v18\n"
    "v_smfmac_f32_32x32x32_bf16 v[48:63], v[76:79], v[40:47], v15\n"
    "ds_read_b128 v[76:79], v14 offset:41600\n"
    "v_exp_f32 v19, v19\n"
    "v_exp_f32 v20, v20\n"
    "v_exp_f32 v21, v21\n"
    "v_mfma_f32_32x32x16_bf16 v[32:47], v[64:67], v[8:11], -4.0\n"
    "ds_read_b128 v[64:67], v12 offset:45760\n"
    "v_exp_f32 v22, v22\n"
    "v_exp_f32 v23, v23\n"
    "v_exp_f32 v24, v24\n"
    "v_exp_f32 v25, v25\n"
    "v_exp_f32 v26, v26\n"
    "v_smfmac_f32_32x32x32_bf16 v[128:143], v[68:71], v[16:23], v15\n"
    "v_exp_f32 v27, v27\n"
    "v_exp_f32 v28, v28\n"
    "v_exp_f32 v29, v29\n"
    "v_exp_f32 v30, v30\n"
    "v_exp_f32 v31, v31\n"
    "v_exp_f32 v32, v32\n"
    "v_exp_f32 v33, v33\n"
    "v_exp_f32 v34, v34\n"
    "s_waitcnt lgkmcnt(0)\n"
    "v_smfmac_f32_32x32x32_bf16 v[128:143], v[76:79], v[24:31], v15\n"
    "v_exp_f32 v35, v35\n"
    "v_exp_f32 v36, v36\n"
    "v_exp_f32 v37, v37\n"
    "v_mfma_f32_32x32x16_bf16 v[16:31], v[64:67], v[144:147], -4.0\n"
    "v_exp_f32 v38, v38\n"
    "v_exp_f32 v39, v39\n"
    "v_exp_f32 v40, v40\n"
    "v_exp_f32 v41, v41\n"
    "v_exp_f32 v42, v42\n"
    "v_smfmac_f32_32x32x32_bf16 v[48:63], v[68:71], v[32:39], v15\n"
    "ds_read_b128 v[68:71], v13 offset:49920\n"
    "v_exp_f32 v43, v43\n"
    "v_exp_f32 v44, v44\n"
    "v_exp_f32 v45, v45\n"
    "v_exp_f32 v46, v46\n"
    "v_exp_f32 v47, v47\n"
    "v_exp_f32 v16, v16\n"
    "v_exp_f32 v17, v17\n"
    "v_exp_f32 v18, v18\n"
    "v_smfmac_f32_32x32x32_bf16 v[48:63], v[76:79], v[40:47], v15\n"
    "ds_read_b128 v[76:79], v14 offset:45760\n"
    "v_exp_f32 v19, v19\n"
    "v_exp_f32 v20, v20\n"
    "v_exp_f32 v21, v21\n"
    "v_mfma_f32_32x32x16_bf16 v[32:47], v[64:67], v[8:11], -4.0\n"
    "ds_read_b128 v[64:67], v12 offset:49920\n"
    "v_exp_f32 v22, v22\n"
    "v_exp_f32 v23, v23\n"
    "v_exp_f32 v24, v24\n"
    "v_exp_f32 v25, v25\n"
    "v_exp_f32 v26, v26\n"
    "v_smfmac_f32_32x32x32_bf16 v[128:143], v[72:75], v[16:23], v15\n"
    "v_exp_f32 v27, v27\n"
    "v_exp_f32 v28, v28\n"
    "v_exp_f32 v29, v29\n"
    "v_exp_f32 v30, v30\n"
    "v_exp_f32 v31, v31\n"
    "v_exp_f32 v32, v32\n"
    "v_exp_f32 v33, v33\n"
    "v_exp_f32 v34, v34\n"
    "s_waitcnt lgkmcnt(0)\n"
    "v_smfmac_f32_32x32x32_bf16 v[128:143], v[76:79], v[24:31], v15\n"
    "v_exp_f32 v35, v35\n"
    "v_exp_f32 v36, v36\n"
    "v_exp_f32 v37, v37\n"
    "v_mfma_f32_32x32x16_bf16 v[16:31], v[64:67], v[144:147], -4.0\n"
    "v_exp_f32 v38, v38\n"
    "v_exp_f32 v39, v39\n"
    "v_exp_f32 v40, v40\n"
    "v_exp_f32 v41, v41\n"
    "v_exp_f32 v42, v42\n"
    "v_smfmac_f32_32x32x32_bf16 v[48:63], v[72:75], v[32:39], v15\n"
    "ds_read_b128 v[72:75], v13 offset:54080\n"
    "v_exp_f32 v43, v43\n"
    "v_exp_f32 v44, v44\n"
    "v_exp_f32 v45, v45\n"
    "v_exp_f32 v46, v46\n"
    "v_exp_f32 v47, v47\n"
    "v_exp_f32 v16, v16\n"
    "v_exp_f32 v17, v17\n"
    "v_exp_f32 v18, v18\n"
    "v_smfmac_f32_32x32x32_bf16 v[48:63], v[76:79], v[40:47], v15\n"
    "ds_read_b128 v[76:79], v14 offset:49920\n"
    "v_exp_f32 v19, v19\n"
    "v_exp_f32 v20, v20\n"
    "v_exp_f32 v21, v21\n"
    "v_mfma_f32_32x32x16_bf16 v[32:47], v[64:67], v[8:11], -4.0\n"
    "ds_read_b128 v[64:67], v12 offset:54080\n"
    "v_exp_f32 v22, v22\n"
    "v_exp_f32 v23, v23\n"
    "v_exp_f32 v24, v24\n"
    "v_exp_f32 v25, v25\n"
    "v_exp_f32 v26, v26\n"
    "v_smfmac_f32_32x32x32_bf16 v[128:143], v[68:71], v[16:23], v15\n"
    "v_exp_f32 v27, v27\n"
    "v_exp_f32 v28, v28\n"
    "v_exp_f32 v29, v29\n"
    "v_exp_f32 v30, v30\n"
    "v_exp_f32 v31, v31\n"
    "v_exp_f32 v32, v32\n"
    "v_exp_f32 v33, v33\n"
    "v_exp_f32 v34, v34\n"
    "s_waitcnt lgkmcnt(0)\n"
    "v_smfmac_f32_32x32x32_bf16 v[128:143], v[76:79], v[24:31], v15\n"
    "v_exp_f32 v35, v35\n"
    "v_exp_f32 v36, v36\n"
    "v_exp_f32 v37, v37\n"
    "v_mfma_f32_32x32x16_bf16 v[16:31], v[64:67], v[144:147], -4.0\n"
    "v_exp_f32 v38, v38\n"
    "v_exp_f32 v39, v39\n"
    "v_exp_f32 v40, v40\n"
    "v_exp_f32 v41, v41\n"
    "v_exp_f32 v42, v42\n"
    "v_smfmac_f32_32x32x32_bf16 v[48:63], v[68:71], v[32:39], v15\n"
    "ds_read_b128 v[68:71], v13 offset:58240\n"
    "v_exp_f32 v43, v43\n"
    "v_exp_f32 v44, v44\n"
    "v_exp_f32 v45, v45\n"
    "v_exp_f32 v46, v46\n"
    "v_exp_f32 v47, v47\n"
    "v_exp_f32 v16, v16\n"
    "v_exp_f32 v17, v17\n"
    "v_exp_f32 v18, v18\n"
    "v_smfmac_f32_32x32x32_bf16 v[48:63], v[76:79], v[40:47], v15\n"
    "ds_read_b128 v[76:79], v14 offset:54080\n"
    "v_exp_f32 v19, v19\n"
    "v_exp_f32 v20, v20\n"
    "v_exp_f32 v21, v21\n"
    "v_mfma_f32_32x32x16_bf16 v[32:47], v[64:67], v[8:11], -4.0\n"
    "ds_read_b128 v[64:67], v12 offset:58240\n"
    "v_exp_f32 v22, v22\n"
    "v_exp_f32 v23, v23\n"
    "v_exp_f32 v24, v24\n"
    "v_exp_f32 v25, v25\n"
    "v_exp_f32 v26, v26\n"
    "v_smfmac_f32_32x32x32_bf16 v[128:143], v[72:75], v[16:23], v15\n"
    "v_exp_f32 v27, v27\n"
    "v_exp_f32 v28, v28\n"
    "v_exp_f32 v29, v29\n"
    "v_exp_f32 v30, v30\n"
    "v_exp_f32 v31, v31\n"
    "v_exp_f32 v32, v32\n"
    "v_exp_f32 v33, v33\n"
    "v_exp_f32 v34, v34\n"
    "s_waitcnt lgkmcnt(0)\n"
    "v_smfmac_f32_32x32x32_bf16 v[128:143], v[76:79], v[24:31], v15\n"
    "v_exp_f32 v35, v35\n"
    "v_exp_f32 v36, v36\n"
    "v_exp_f32 v37, v37\n"
    "v_mfma_f32_32x32x16_bf16 v[16:31], v[64:67], v[144:147], -4.0\n"
    "v_exp_f32 v38, v38\n"
    "v_exp_f32 v39, v39\n"
    "v_exp_f32 v40, v40\n"
    "v_exp_f32 v41, v41\n"
    "v_exp_f32 v42, v42\n"
    "v_smfmac_f32_32x32x32_bf16 v[48:63], v[72:75], v[32:39], v15\n"
    "ds_read_b128 v[72:75], v13 offset:62400\n"
    "v_exp_f32 v43, v43\n"
    "v_exp_f32 v44, v44\n"
    "v_exp_f32 v45, v45\n"
    "v_exp_f32 v46, v46\n"
    "v_exp_f32 v47, v47\n"
    "v_exp_f32 v16, v16\n"
    "v_exp_f32 v17, v17\n"
    "v_exp_f32 v18, v18\n"
    "v_smfmac_f32_32x32x32_bf16 v[48:63], v[76:79], v[40:47], v15\n"
    "ds_read_b128 v[76:79], v14 offset:58240\n"
    "v_exp_f32 v19, v19\n"
    "v_exp_f32 v20, v20\n"
    "v_exp_f32 v21, v21\n"
    "v_mfma_f32_32x32x16_bf16 v[32:47], v[64:67], v[8:11], -4.0\n"
    "ds_read_b128 v[64:67], v12 offset:62400\n"
    "v_exp_f32 v22, v22\n"
    "v_exp_f32 v23, v23\n"
    "v_exp_f32 v24, v24\n"
    "v_exp_f32 v25, v25\n"
    "v_exp_f32 v26, v26\n"
    "v_smfmac_f32_32x32x32_bf16 v[128:143], v[68:71], v[16:23], v15\n"
    "v_exp_f32 v27, v27\n"
    "v_exp_f32 v28, v28\n"
    "v_exp_f32 v29, v29\n"
    "v_exp_f32 v30, v30\n"
    "v_exp_f32 v31, v31\n"
    "v_exp_f32 v32, v32\n"
    "v_exp_f32 v33, v33\n"
    "v_exp_f32 v34, v34\n"
    "s_waitcnt lgkmcnt(0)\n"
    "v_smfmac_f32_32x32x32_bf16 v[128:143], v[76:79], v[24:31], v15\n"
    "v_exp_f32 v35, v35\n"
    "v_exp_f32 v36, v36\n"
    "v_exp_f32 v37, v37\n"
    "v_mfma_f32_32x32x16_bf16 v[16:31], v[64:67], v[144:147], -4.0\n"
    "v_exp_f32 v38, v38\n"
    "v_exp_f32 v39, v39\n"
    "v_exp_f32 v40, v40\n"
    "v_exp_f32 v41, v41\n"
    "v_exp_f32 v42, v42\n"
    "v_smfmac_f32_32x32x32_bf16 v[48:63], v[68:71], v[32:39], v15\n"
    "v_add_u32 v13, 66560, v13\n"
    "ds_read_b128 v[68:71], v13 offset:0\n"
    "v_exp_f32 v43, v43\n"
    "v_exp_f32 v44, v44\n"
    "v_exp_f32 v45, v45\n"
    "v_exp_f32 v46, v46\n"
    "v_exp_f32 v47, v47\n"
    "v_exp_f32 v16, v16\n"
    "v_exp_f32 v17, v17\n"
    "v_exp_f32 v18, v18\n"
    "v_smfmac_f32_32x32x32_bf16 v[48:63], v[76:79], v[40:47], v15\n"
    "ds_read_b128 v[76:79], v14 offset:62400\n"
    "v_exp_f32 v19, v19\n"
    "v_exp_f32 v20, v20\n"
    "v_exp_f32 v21, v21\n"
    "v_mfma_f32_32x32x16_bf16 v[32:47], v[64:67], v[8:11], -4.0\n"
    "v_add_u32 v12, 66560, v12\n"
    "ds_read_b128 v[64:67], v12 offset:0\n"
    "v_exp_f32 v22, v22\n"
    "v_exp_f32 v23, v23\n"
    "v_exp_f32 v24, v24\n"
    "v_exp_f32 v25, v25\n"
    "v_exp_f32 v26, v26\n"
    "v_smfmac_f32_32x32x32_bf16 v[128:143], v[72:75], v[16:23], v15\n"
    "v_exp_f32 v27, v27\n"
    "v_exp_f32 v28, v28\n"
    "v_exp_f32 v29, v29\n"
    "v_exp_f32 v30, v30\n"
    "v_exp_f32 v31, v31\n"
    "v_exp_f32 v32, v32\n"
    "v_exp_f32 v33, v33\n"
    "v_exp_f32 v34, v34\n"
    "s_waitcnt lgkmcnt(0)\n"
    "v_smfmac_f32_32x32x32_bf16 v[128:143], v[76:79], v[24:31], v15\n"
    "v_exp_f32 v35, v35\n"
    "v_exp_f32 v36, v36\n"
    "v_exp_f32 v37, v37\n"
    "v_mfma_f32_32x32x16_bf16 v[16:31], v[64:67], v[144:147], -4.0\n"
    "v_exp_f32 v38, v38\n"
    "v_exp_f32 v39, v39\n"
    "v_exp_f32 v40, v40\n"
    "v_exp_f32 v41, v41\n"
    "v_exp_f32 v42, v42\n"
    "v_smfmac_f32_32x32x32_bf16 v[48:63], v[72:75], v[32:39], v15\n"
    "ds_read_b128 v[72:75], v13 offset:4160\n"
    "v_exp_f32 v43, v43\n"
    "v_exp_f32 v44, v44\n"
    "v_exp_f32 v45, v45\n"
    "v_exp_f32 v46, v46\n"
    "v_exp_f32 v47, v47\n"
    "v_exp_f32 v16, v16\n"
    "v_exp_f32 v17, v17\n"
    "v_exp_f32 v18, v18\n"
    "v_smfmac_f32_32x32x32_bf16 v[48:63], v[76:79], v[40:47], v15\n"
    "v_add_u32 v14, 66560, v14\n"
    "ds_read_b128 v[76:79], v14 offset:0\n"
    "v_exp_f32 v19, v19\n"
    "v_exp_f32 v20, v20\n"
    "v_exp_f32 v21, v21\n"
    "v_mfma_f32_32x32x16_bf16 v[32:47], v[64:67], v[8:11], -4.0\n"
    "ds_read_b128 v[64:67], v12 offset:4160\n"
    "v_exp_f32 v22, v22\n"
    "v_exp_f32 v23, v23\n"
    "v_exp_f32 v24, v24\n"
    "v_exp_f32 v25, v25\n"
    "v_exp_f32 v26, v26\n"
    "v_smfmac_f32_32x32x32_bf16 v[128:143], v[68:71], v[16:23], v15\n"
    "v_exp_f32 v27, v27\n"
    "v_exp_f32 v28, v28\n"
    "v_exp_f32 v29, v29\n"
    "v_exp_f32 v30, v30\n"
    "v_exp_f32 v31, v31\n"
    "v_exp_f32 v32, v32\n"
    "v_exp_f32 v33, v33\n"
    "v_exp_f32 v34, v34\n"
    "s_waitcnt lgkmcnt(0)\n"
    "v_smfmac_f32_32x32x32_bf16 v[128:143], v[76:79], v[24:31], v15\n"
    "v_exp_f32 v35, v35\n"
    "v_exp_f32 v36, v36\n"
    "v_exp_f32 v37, v37\n"
    "v_mfma_f32_32x32x16_bf16 v[16:31], v[64:67], v[144:147], -4.0\n"
    "v_exp_f32 v38, v38\n"
    "v_exp_f32 v39, v39\n"
    "v_exp_f32 v40, v40\n"
    "v_exp_f32 v41, v41\n"
    "v_exp_f32 v42, v42\n"
    "v_smfmac_f32_32x32x32_bf16 v[48:63], v[68:71], v[32:39], v15\n"
    "ds_read_b128 v[68:71], v13 offset:8320\n"
    "v_exp_f32 v43, v43\n"
    "v_exp_f32 v44, v44\n"
    "v_exp_f32 v45, v45\n"
    "v_exp_f32 v46, v46\n"
    "v_exp_f32 v47, v47\n"
    "v_exp_f32 v16, v16\n"
    "v_exp_f32 v17, v17\n"
    "v_exp_f32 v18, v18\n"
    "v_smfmac_f32_32x32x32_bf16 v[48:63], v[76:79], v[40:47], v15\n"
    "ds_read_b128 v[76:79], v14 offset:4160\n"
    "v_exp_f32 v19, v19\n"
    "v_exp_f32 v20, v20\n"
    "v_exp_f32 v21, v21\n"
    "v_mfma_f32_32x32x16_bf16 v[32:47], v[64:67], v[8:11], -4.0\n"
    "ds_read_b128 v[64:67], v12 offset:8320\n"
    "v_exp_f32 v22, v22\n"
    "v_exp_f32 v23, v23\n"
    "v_exp_f32 v24, v24\n"
    "v_exp_f32 v25, v25\n"
    "v_exp_f32 v26, v26\n"
    "v_smfmac_f32_32x32x32_bf16 v[128:143], v[72:75], v[16:23], v15\n"
    "v_exp_f32 v27, v27\n"
    "v_exp_f32 v28, v28\n"
    "v_exp_f32 v29, v29\n"
    "v_exp_f32 v30, v30\n"
    "v_exp_f32 v31, v31\n"
    "s_barrier\n"
    "v_exp_f32 v32, v32\n"
    "v_exp_f32 v33, v33\n"
    "v_exp_f32 v34, v34\n"
    "s_waitcnt lgkmcnt(0)\n"
    "v_smfmac_f32_32x32x32_bf16 v[128:143], v[76:79], v[24:31], v15\n"
    "v_exp_f32 v35, v35\n"
    "v_exp_f32 v36, v36\n"
    "v_exp_f32 v37, v37\n"
    "v_mfma_f32_32x32x16_bf16 v[16:31], v[64:67], v[144:147], -4.0\n"
    "v_exp_f32 v38, v38\n"
    "v_exp_f32 v39, v39\n"
    "v_exp_f32 v40, v40\n"
    "v_exp_f32 v41, v41\n"
    "v_exp_f32 v42, v42\n"
    "v_smfmac_f32_32x32x32_bf16 v[48:63], v[72:75], v[32:39], v15\n"
    "ds_read_b128 v[72:75], v13 offset:12480\n"
    "v_exp_f32 v43, v43\n"
    "v_exp_f32 v44, v44\n"
    "v_exp_f32 v45, v45\n"
    "v_exp_f32 v46, v46\n"
    "v_exp_f32 v47, v47\n"
    "v_exp_f32 v16, v16\n"
    "v_exp_f32 v17, v17\n"
    "v_exp_f32 v18, v18\n"
    "v_smfmac_f32_32x32x32_bf16 v[48:63], v[76:79], v[40:47], v15\n"
    "ds_read_b128 v[76:79], v14 offset:8320\n"
    "v_exp_f32 v19, v19\n"
    "v_exp_f32 v20, v20\n"
    "v_exp_f32 v21, v21\n"
    "v_mfma_f32_32x32x16_bf16 v[32:47], v[64:67], v[8:11], -4.0\n"
    "ds_read_b128 v[64:67], v12 offset:12480\n"
    "v_exp_f32 v22, v22\n"
    "v_exp_f32 v23, v23\n"
    "v_exp_f32 v24, v24\n"
    "v_exp_f32 v25, v25\n"
    "v_exp_f32 v26, v26\n"
    "v_smfmac_f32_32x32x32_bf16 v[128:143], v[68:71], v[16:23], v15\n"
    "v_exp_f32 v27, v27\n"
    "v_exp_f32 v28, v28\n"
    "v_exp_f32 v29, v29\n"
    "v_exp_f32 v30, v30\n"
    "v_exp_f32 v31, v31\n"
    "v_exp_f32 v32, v32\n"
    "v_exp_f32 v33, v33\n"
    "v_exp_f32 v34, v34\n"
    "s_waitcnt lgkmcnt(0)\n"
    "v_smfmac_f32_32x32x32_bf16 v[128:143], v[76:79], v[24:31], v15\n"
    "v_exp_f32 v35, v35\n"
    "v_exp_f32 v36, v36\n"
    "v_exp_f32 v37, v37\n"
    "v_mfma_f32_32x32x16_bf16 v[16:31], v[64:67], v[144:147], -4.0\n"
    "v_exp_f32 v38, v38\n"
    "v_exp_f32 v39, v39\n"
    "v_exp_f32 v40, v40\n"
    "v_exp_f32 v41, v41\n"
    "v_exp_f32 v42, v42\n"
    "v_smfmac_f32_32x32x32_bf16 v[48:63], v[68:71], v[32:39], v15\n"
    "ds_read_b128 v[68:71], v13 offset:16640\n"
    "v_exp_f32 v43, v43\n"
    "v_exp_f32 v44, v44\n"
    "v_exp_f32 v45, v45\n"
    "v_exp_f32 v46, v46\n"
    "v_exp_f32 v47, v47\n"
    "v_exp_f32 v16, v16\n"
    "v_exp_f32 v17, v17\n"
    "v_exp_f32 v18, v18\n"
    "v_smfmac_f32_32x32x32_bf16 v[48:63], v[76:79], v[40:47], v15\n"
    "ds_read_b128 v[76:79], v14 offset:12480\n"
    "v_exp_f32 v19, v19\n"
    "v_exp_f32 v20, v20\n"
    "v_exp_f32 v21, v21\n"
    "v_mfma_f32_32x32x16_bf16 v[32:47], v[64:67], v[8:11], -4.0\n"
    "ds_read_b128 v[64:67], v12 offset:16640\n"
    "v_exp_f32 v22, v22\n"
    "v_exp_f32 v23, v23\n"
    "v_exp_f32 v24, v24\n"
    "v_exp_f32 v25, v25\n"
    "v_exp_f32 v26, v26\n"
    "v_smfmac_f32_32x32x32_bf16 v[128:143], v[72:75], v[16:23], v15\n"
    "v_exp_f32 v27, v27\n"
    "v_exp_f32 v28, v28\n"
    "v_exp_f32 v29, v29\n"
    "v_exp_f32 v30, v30\n"
    "v_exp_f32 v31, v31\n"
    "v_exp_f32 v32, v32\n"
    "v_exp_f32 v33, v33\n"
    "v_exp_f32 v34, v34\n"
    "s_waitcnt lgkmcnt(0)\n"
    "v_smfmac_f32_32x32x32_bf16 v[128:143], v[76:79], v[24:31], v15\n"
    "v_exp_f32 v35, v35\n"
    "v_exp_f32 v36, v36\n"
    "v_exp_f32 v37, v37\n"
    "v_mfma_f32_32x32x16_bf16 v[16:31], v[64:67], v[144:147], -4.0\n"
    "v_exp_f32 v38, v38\n"
    "v_exp_f32 v39, v39\n"
    "v_exp_f32 v40, v40\n"
    "v_exp_f32 v41, v41\n"
    "v_exp_f32 v42, v42\n"
    "v_smfmac_f32_32x32x32_bf16 v[48:63], v[72:75], v[32:39], v15\n"
    "ds_read_b128 v[72:75], v13 offset:20800\n"
    "v_exp_f32 v43, v43\n"
    "v_exp_f32 v44, v44\n"
    "v_exp_f32 v45, v45\n"
    "v_exp_f32 v46, v46\n"
    "v_exp_f32 v47, v47\n"
    "v_exp_f32 v16, v16\n"
    "v_exp_f32 v17, v17\n"
    "v_exp_f32 v18, v18\n"
    "v_smfmac_f32_32x32x32_bf16 v[48:63], v[76:79], v[40:47], v15\n"
    "ds_read_b128 v[76:79], v14 offset:16640\n"
    "v_exp_f32 v19, v19\n"
    "v_exp_f32 v20, v20\n"
    "v_exp_f32 v21, v21\n"
    "v_mfma_f32_32x32x16_bf16 v[32:47], v[64:67], v[8:11], -4.0\n"
    "ds_read_b128 v[64:67], v12 offset:20800\n"
    "v_exp_f32 v22, v22\n"
    "v_exp_f32 v23, v23\n"
    "v_exp_f32 v24, v24\n"
    "v_exp_f32 v25, v25\n"
    "v_exp_f32 v26, v26\n"
    "v_smfmac_f32_32x32x32_bf16 v[128:143], v[68:71], v[16:23], v15\n"
    "v_exp_f32 v27, v27\n"
    "v_exp_f32 v28, v28\n"
    "v_exp_f32 v29, v29\n"
    "v_exp_f32 v30, v30\n"
    "v_exp_f32 v31, v31\n"
    "v_exp_f32 v32, v32\n"
    "v_exp_f32 v33, v33\n"
    "v_exp_f32 v34, v34\n"
    "s_waitcnt lgkmcnt(0)\n"
    "v_smfmac_f32_32x32x32_bf16 v[128:143], v[76:79], v[24:31], v15\n"
    "v_exp_f32 v35, v35\n"
    "v_exp_f32 v36, v36\n"
    "v_exp_f32 v37, v37\n"
    "v_mfma_f32_32x32x16_bf16 v[16:31], v[64:67], v[144:147], -4.0\n"
    "v_exp_f32 v38, v38\n"
    "v_exp_f32 v39, v39\n"
    "v_exp_f32 v40, v40\n"
    "v_exp_f32 v41, v41\n"
    "v_exp_f32 v42, v42\n"
    "v_smfmac_f32_32x32x32_bf16 v[48:63], v[68:71], v[32:39], v15\n"
    "ds_read_b128 v[68:71], v13 offset:24960\n"
    "v_exp_f32 v43, v43\n"
    "v_exp_f32 v44, v44\n"
    "v_exp_f32 v45, v45\n"
    "v_exp_f32 v46, v46\n"
    "v_exp_f32 v47, v47\n"
    "v_exp_f32 v16, v16\n"
    "v_exp_f32 v17, v17\n"
    "v_exp_f32 v18, v18\n"
    "v_smfmac_f32_32x32x32_bf16 v[48:63], v[76:79], v[40:47], v15\n"
    "ds_read_b128 v[76:79], v14 offset:20800\n"
    "v_exp_f32 v19, v19\n"
    "v_exp_f32 v20, v20\n"
    "v_exp_f32 v21, v21\n"
    "v_mfma_f32_32x32x16_bf16 v[32:47], v[64:67], v[8:11], -4.0\n"
    "ds_read_b128 v[64:67], v12 offset:24960\n"
    "v_exp_f32 v22, v22\n"
    "v_exp_f32 v23, v23\n"
    "v_exp_f32 v24, v24\n"
    "v_exp_f32 v25, v25\n"
    "v_exp_f32 v26, v26\n"
    "v_smfmac_f32_32x32x32_bf16 v[128:143], v[72:75], v[16:23], v15\n"
    "v_exp_f32 v27, v27\n"
    "v_exp_f32 v28, v28\n"
    "v_exp_f32 v29, v29\n"
    "v_exp_f32 v30, v30\n"
    "v_exp_f32 v31, v31\n"
    "v_exp_f32 v32, v32\n"
    "v_exp_f32 v33, v33\n"
    "v_exp_f32 v34, v34\n"
    "s_waitcnt lgkmcnt(0)\n"
    "v_smfmac_f32_32x32x32_bf16 v[128:143], v[76:79], v[24:31], v15\n"
    "v_exp_f32 v35, v35\n"
    "v_exp_f32 v36, v36\n"
    "v_exp_f32 v37, v37\n"
    "v_mfma_f32_32x32x16_bf16 v[16:31], v[64:67], v[144:147], -4.0\n"
    "v_exp_f32 v38, v38\n"
    "v_exp_f32 v39, v39\n"
    "v_exp_f32 v40, v40\n"
    "v_exp_f32 v41, v41\n"
    "v_exp_f32 v42, v42\n"
    "v_smfmac_f32_32x32x32_bf16 v[48:63], v[72:75], v[32:39], v15\n"
    "ds_read_b128 v[72:75], v13 offset:29120\n"
    "v_exp_f32 v43, v43\n"
    "v_exp_f32 v44, v44\n"
    "v_exp_f32 v45, v45\n"
    "v_exp_f32 v46, v46\n"
    "v_exp_f32 v47, v47\n"
    "v_exp_f32 v16, v16\n"
    "v_exp_f32 v17, v17\n"
    "v_exp_f32 v18, v18\n"
    "v_smfmac_f32_32x32x32_bf16 v[48:63], v[76:79], v[40:47], v15\n"
    "ds_read_b128 v[76:79], v14 offset:24960\n"
    "v_exp_f32 v19, v19\n"
    "v_exp_f32 v20, v20\n"
    "v_exp_f32 v21, v21\n"
    "v_mfma_f32_32x32x16_bf16 v[32:47], v[64:67], v[8:11], -4.0\n"
    "ds_read_b128 v[64:67], v12 offset:29120\n"
    "v_exp_f32 v22, v22\n"
    "v_exp_f32 v23, v23\n"
    "v_exp_f32 v24, v24\n"
    "v_exp_f32 v25, v25\n"
    "v_exp_f32 v26, v26\n"
    "v_smfmac_f32_32x32x32_bf16 v[128:143], v[68:71], v[16:23], v15\n"
    "v_exp_f32 v27, v27\n"
    "v_exp_f32 v28, v28\n"
    "v_exp_f32 v29, v29\n"
    "v_exp_f32 v30, v30\n"
    "v_exp_f32 v31, v31\n"
    "v_exp_f32 v32, v32\n"
    "v_exp_f32 v33, v33\n"
    "v_exp_f32 v34, v34\n"
    "s_waitcnt lgkmcnt(0)\n"
    "v_smfmac_f32_32x32x32_bf16 v[128:143], v[76:79], v[24:31], v15\n"
    "v_exp_f32 v35, v35\n"
    "v_exp_f32 v36, v36\n"
    "v_exp_f32 v37, v37\n"
    "v_mfma_f32_32x32x16_bf16 v[16:31], v[64:67], v[144:147], -4.0\n"
    "v_exp_f32 v38, v38\n"
    "v_exp_f32 v39, v39\n"
    "v_exp_f32 v40, v40\n"
    "v_exp_f32 v41, v41\n"
    "v_exp_f32 v42, v42\n"
    "v_smfmac_f32_32x32x32_bf16 v[48:63], v[68:71], v[32:39], v15\n"
    "ds_read_b128 v[68:71], v13 offset:33280\n"
    "v_exp_f32 v43, v43\n"
    "v_exp_f32 v44, v44\n"
    "v_exp_f32 v45, v45\n"
    "v_exp_f32 v46, v46\n"
    "v_exp_f32 v47, v47\n"
    "v_exp_f32 v16, v16\n"
    "v_exp_f32 v17, v17\n"
    "v_exp_f32 v18, v18\n"
    "v_smfmac_f32_32x32x32_bf16 v[48:63], v[76:79], v[40:47], v15\n"
    "ds_read_b128 v[76:79], v14 offset:29120\n"
    "v_exp_f32 v19, v19\n"
    "v_exp_f32 v20, v20\n"
    "v_exp_f32 v21, v21\n"
    "v_mfma_f32_32x32x16_bf16 v[32:47], v[64:67], v[8:11], -4.0\n"
    "ds_read_b128 v[64:67], v12 offset:33280\n"
    "v_exp_f32 v22, v22\n"
    "v_exp_f32 v23, v23\n"
    "v_exp_f32 v24, v24\n"
    "v_exp_f32 v25, v25\n"
    "v_exp_f32 v26, v26\n"
    "v_smfmac_f32_32x32x32_bf16 v[128:143], v[72:75], v[16:23], v15\n"
    "v_exp_f32 v27, v27\n"
    "v_exp_f32 v28, v28\n"
    "v_exp_f32 v29, v29\n"
    "v_exp_f32 v30, v30\n"
    "v_exp_f32 v31, v31\n"
    "v_exp_f32 v32, v32\n"
    "v_exp_f32 v33, v33\n"
    "v_exp_f32 v34, v34\n"
    "s_waitcnt lgkmcnt(0)\n"
    "v_smfmac_f32_32x32x32_bf16 v[128:143], v[76:79], v[24:31], v15\n"
    "v_exp_f32 v35, v35\n"
    "v_exp_f32 v36, v36\n"
    "v_exp_f32 v37, v37\n"
    "v_mfma_f32_32x32x16_bf16 v[16:31], v[64:67], v[144:147], -4.0\n"
    "v_exp_f32 v38, v38\n"
    "v_exp_f32 v39, v39\n"
    "v_exp_f32 v40, v40\n"
    "v_exp_f32 v41, v41\n"
    "v_exp_f32 v42, v42\n"
    "v_smfmac_f32_32x32x32_bf16 v[48:63], v[72:75], v[32:39], v15\n"
    "ds_read_b128 v[72:75], v13 offset:37440\n"
    "v_exp_f32 v43, v43\n"
    "v_exp_f32 v44, v44\n"
    "v_exp_f32 v45, v45\n"
    "v_exp_f32 v46, v46\n"
    "v_exp_f32 v47, v47\n"
    "v_exp_f32 v16, v16\n"
    "v_exp_f32 v17, v17\n"
    "v_exp_f32 v18, v18\n"
    "v_smfmac_f32_32x32x32_bf16 v[48:63], v[76:79], v[40:47], v15\n"
    "ds_read_b128 v[76:79], v14 offset:33280\n"
    "v_exp_f32 v19, v19\n"
    "v_exp_f32 v20, v20\n"
    "v_exp_f32 v21, v21\n"
    "v_mfma_f32_32x32x16_bf16 v[32:47], v[64:67], v[8:11], -4.0\n"
    "ds_read_b128 v[64:67], v12 offset:37440\n"
    "v_exp_f32 v22, v22\n"
    "v_exp_f32 v23, v23\n"
    "v_exp_f32 v24, v24\n"
    "v_exp_f32 v25, v25\n"
    "v_exp_f32 v26, v26\n"
    "v_smfmac_f32_32x32x32_bf16 v[128:143], v[68:71], v[16:23], v15\n"
    "v_exp_f32 v27, v27\n"
    "v_exp_f32 v28, v28\n"
    "v_exp_f32 v29, v29\n"
    "v_exp_f32 v30, v30\n"
    "v_exp_f32 v31, v31\n"
    "v_exp_f32 v32, v32\n"
    "v_exp_f32 v33, v33\n"
    "v_exp_f32 v34, v34\n"
    "s_waitcnt lgkmcnt(0)\n"
    "v_smfmac_f32_32x32x32_bf16 v[128:143], v[76:79], v[24:31], v15\n"
    "v_exp_f32 v35, v35\n"
    "v_exp_f32 v36, v36\n"
    "v_exp_f32 v37, v37\n"
    "v_mfma_f32_32x32x16_bf16 v[16:31], v[64:67], v[144:147], -4.0\n"
    "v_exp_f32 v38, v38\n"
    "v_exp_f32 v39, v39\n"
    "v_exp_f32 v40, v40\n"
    "v_exp_f32 v41, v41\n"
    "v_exp_f32 v42, v42\n"
    "v_smfmac_f32_32x32x32_bf16 v[48:63], v[68:71], v[32:39], v15\n"
    "ds_read_b128 v[68:71], v13 offset:41600\n"
    "v_exp_f32 v43, v43\n"
    "v_exp_f32 v44, v44\n"
    "v_exp_f32 v45, v45\n"
    "v_exp_f32 v46, v46\n"
    "v_exp_f32 v47, v47\n"
    "v_exp_f32 v16, v16\n"
    "v_exp_f32 v17, v17\n"
    "v_exp_f32 v18, v18\n"
    "v_smfmac_f32_32x32x32_bf16 v[48:63], v[76:79], v[40:47], v15\n"
    "ds_read_b128 v[76:79], v14 offset:37440\n"
    "v_exp_f32 v19, v19\n"
    "v_exp_f32 v20, v20\n"
    "v_exp_f32 v21, v21\n"
    "v_mfma_f32_32x32x16_bf16 v[32:47], v[64:67], v[8:11], -4.0\n"
    "ds_read_b128 v[64:67], v12 offset:41600\n"
    "v_exp_f32 v22, v22\n"
    "v_exp_f32 v23, v23\n"
    "v_exp_f32 v24, v24\n"
    "v_exp_f32 v25, v25\n"
    "v_exp_f32 v26, v26\n"
    "v_smfmac_f32_32x32x32_bf16 v[128:143], v[72:75], v[16:23], v15\n"
    "v_exp_f32 v27, v27\n"
    "v_exp_f32 v28, v28\n"
    "v_exp_f32 v29, v29\n"
    "v_exp_f32 v30, v30\n"
    "v_exp_f32 v31, v31\n"
    "v_exp_f32 v32, v32\n"
    "v_exp_f32 v33, v33\n"
    "v_exp_f32 v34, v34\n"
    "s_waitcnt lgkmcnt(0)\n"
    "v_smfmac_f32_32x32x32_bf16 v[128:143], v[76:79], v[24:31], v15\n"
    "v_exp_f32 v35, v35\n"
    "v_exp_f32 v36, v36\n"
    "v_exp_f32 v37, v37\n"
    "v_mfma_f32_32x32x16_bf16 v[16:31], v[64:67], v[144:147], -4.0\n"
    "v_exp_f32 v38, v38\n"
    "v_exp_f32 v39, v39\n"
    "v_exp_f32 v40, v40\n"
    "v_exp_f32 v41, v41\n"
    "v_exp_f32 v42, v42\n"
    "v_smfmac_f32_32x32x32_bf16 v[48:63], v[72:75], v[32:39], v15\n"
    "ds_read_b128 v[72:75], v13 offset:45760\n"
    "v_exp_f32 v43, v43\n"
    "v_exp_f32 v44, v44\n"
    "v_exp_f32 v45, v45\n"
    "v_exp_f32 v46, v46\n"
    "v_exp_f32 v47, v47\n"
    "v_exp_f32 v16, v16\n"
    "v_exp_f32 v17, v17\n"
    "v_exp_f32 v18, v18\n"
    "v_smfmac_f32_32x32x32_bf16 v[48:63], v[76:79], v[40:47], v15\n"
    "ds_read_b128 v[76:79], v14 offset:41600\n"
    "v_exp_f32 v19, v19\n"
    "v_exp_f32 v20, v20\n"
    "v_exp_f32 v21, v21\n"
    "v_mfma_f32_32x32x16_bf16 v[32:47], v[64:67], v[8:11], -4.0\n"
    "ds_read_b128 v[64:67], v12 offset:45760\n"
    "v_exp_f32 v22, v22\n"
    "v_exp_f32 v23, v23\n"
    "v_exp_f32 v24, v24\n"
    "v_exp_f32 v25, v25\n"
    "v_exp_f32 v26, v26\n"
    "v_smfmac_f32_32x32x32_bf16 v[128:143], v[68:71], v[16:23], v15\n"
    "v_exp_f32 v27, v27\n"
    "v_exp_f32 v28, v28\n"
    "v_exp_f32 v29, v29\n"
    "v_exp_f32 v30, v30\n"
    "v_exp_f32 v31, v31\n"
    "v_exp_f32 v32, v32\n"
    "v_exp_f32 v33, v33\n"
    "v_exp_f32 v34, v34\n"
    "s_waitcnt lgkmcnt(0)\n"
    "v_smfmac_f32_32x32x32_bf16 v[128:143], v[76:79], v[24:31], v15\n"
    "v_exp_f32 v35, v35\n"
    "v_exp_f32 v36, v36\n"
    "v_exp_f32 v37, v37\n"
    "v_mfma_f32_32x32x16_bf16 v[16:31], v[64:67], v[144:147], -4.0\n"
    "v_exp_f32 v38, v38\n"
    "v_exp_f32 v39, v39\n"
    "v_exp_f32 v40, v40\n"
    "v_exp_f32 v41, v41\n"
    "v_exp_f32 v42, v42\n"
    "v_smfmac_f32_32x32x32_bf16 v[48:63], v[68:71], v[32:39], v15\n"
    "ds_read_b128 v[68:71], v13 offset:49920\n"
    "v_exp_f32 v43, v43\n"
    "v_exp_f32 v44, v44\n"
    "v_exp_f32 v45, v45\n"
    "v_exp_f32 v46, v46\n"
    "v_exp_f32 v47, v47\n"
    "v_exp_f32 v16, v16\n"
    "v_exp_f32 v17, v17\n"
    "v_exp_f32 v18, v18\n"
    "v_smfmac_f32_32x32x32_bf16 v[48:63], v[76:79], v[40:47], v15\n"
    "ds_read_b128 v[76:79], v14 offset:45760\n"
    "v_exp_f32 v19, v19\n"
    "v_exp_f32 v20, v20\n"
    "v_exp_f32 v21, v21\n"
    "v_mfma_f32_32x32x16_bf16 v[32:47], v[64:67], v[8:11], -4.0\n"
    "ds_read_b128 v[64:67], v12 offset:49920\n"
    "v_exp_f32 v22, v22\n"
    "v_exp_f32 v23, v23\n"
    "v_exp_f32 v24, v24\n"
    "v_exp_f32 v25, v25\n"
    "v_exp_f32 v26, v26\n"
    "v_smfmac_f32_32x32x32_bf16 v[128:143], v[72:75], v[16:23], v15\n"
    "v_exp_f32 v27, v27\n"
    "v_exp_f32 v28, v28\n"
    "v_exp_f32 v29, v29\n"
    "v_exp_f32 v30, v30\n"
    "v_exp_f32 v31, v31\n"
    "v_exp_f32 v32, v32\n"
    "v_exp_f32 v33, v33\n"
    "v_exp_f32 v34, v34\n"
    "s_waitcnt lgkmcnt(0)\n"
    "v_smfmac_f32_32x32x32_bf16 v[128:143], v[76:79], v[24:31], v15\n"
    "v_exp_f32 v35, v35\n"
    "v_exp_f32 v36, v36\n"
    "v_exp_f32 v37, v37\n"
    "v_mfma_f32_32x32x16_bf16 v[16:31], v[64:67], v[144:147], -4.0\n"
    "v_exp_f32 v38, v38\n"
    "v_exp_f32 v39, v39\n"
    "v_exp_f32 v40, v40\n"
    "v_exp_f32 v41, v41\n"
    "v_exp_f32 v42, v42\n"
    "v_smfmac_f32_32x32x32_bf16 v[48:63], v[72:75], v[32:39], v15\n"
    "ds_read_b128 v[72:75], v13 offset:54080\n"
    "v_exp_f32 v43, v43\n"
    "v_exp_f32 v44, v44\n"
    "v_exp_f32 v45, v45\n"
    "v_exp_f32 v46, v46\n"
    "v_exp_f32 v47, v47\n"
    "v_exp_f32 v16, v16\n"
    "v_exp_f32 v17, v17\n"
    "v_exp_f32 v18, v18\n"
    "v_smfmac_f32_32x32x32_bf16 v[48:63], v[76:79], v[40:47], v15\n"
    "ds_read_b128 v[76:79], v14 offset:49920\n"
    "v_exp_f32 v19, v19\n"
    "v_exp_f32 v20, v20\n"
    "v_exp_f32 v21, v21\n"
    "v_mfma_f32_32x32x16_bf16 v[32:47], v[64:67], v[8:11], -4.0\n"
    "ds_read_b128 v[64:67], v12 offset:54080\n"
    "v_exp_f32 v22, v22\n"
    "v_exp_f32 v23, v23\n"
    "v_exp_f32 v24, v24\n"
    "v_exp_f32 v25, v25\n"
    "v_exp_f32 v26, v26\n"
    "v_smfmac_f32_32x32x32_bf16 v[128:143], v[68:71], v[16:23], v15\n"
    "v_exp_f32 v27, v27\n"
    "v_exp_f32 v28, v28\n"
    "v_exp_f32 v29, v29\n"
    "v_exp_f32 v30, v30\n"
    "v_exp_f32 v31, v31\n"
    "v_exp_f32 v32, v32\n"
    "v_exp_f32 v33, v33\n"
    "v_exp_f32 v34, v34\n"
    "s_waitcnt lgkmcnt(0)\n"
    "v_smfmac_f32_32x32x32_bf16 v[128:143], v[76:79], v[24:31], v15\n"
    "v_exp_f32 v35, v35\n"
    "v_exp_f32 v36, v36\n"
    "v_exp_f32 v37, v37\n"
    "v_mfma_f32_32x32x16_bf16 v[16:31], v[64:67], v[144:147], -4.0\n"
    "v_exp_f32 v38, v38\n"
    "v_exp_f32 v39, v39\n"
    "v_exp_f32 v40, v40\n"
    "v_exp_f32 v41, v41\n"
    "v_exp_f32 v42, v42\n"
    "v_smfmac_f32_32x32x32_bf16 v[48:63], v[68:71], v[32:39], v15\n"
    "ds_read_b128 v[68:71], v13 offset:58240\n"
    "v_exp_f32 v43, v43\n"
    "v_exp_f32 v44, v44\n"
    "v_exp_f32 v45, v45\n"
    "v_exp_f32 v46, v46\n"
    "v_exp_f32 v47, v47\n"
    "v_exp_f32 v16, v16\n"
    "v_exp_f32 v17, v17\n"
    "v_exp_f32 v18, v18\n"
    "v_smfmac_f32_32x32x32_bf16 v[48:63], v[76:79], v[40:47], v15\n"
    "ds_read_b128 v[76:79], v14 offset:54080\n"
    "v_exp_f32 v19, v19\n"
    "v_exp_f32 v20, v20\n"
    "v_exp_f32 v21, v21\n"
    "v_mfma_f32_32x32x16_bf16 v[32:47], v[64:67], v[8:11], -4.0\n"
    "ds_read_b128 v[64:67], v12 offset:58240\n"
    "v_exp_f32 v22, v22\n"
    "v_exp_f32 v23, v23\n"
    "v_exp_f32 v24, v24\n"
    "v_exp_f32 v25, v25\n"
    "v_exp_f32 v26, v26\n"
    "v_smfmac_f32_32x32x32_bf16 v[128:143], v[72:75], v[16:23], v15\n"
    "v_exp_f32 v27, v27\n"
    "v_exp_f32 v28, v28\n"
    "v_exp_f32 v29, v29\n"
    "v_exp_f32 v30, v30\n"
    "v_exp_f32 v31, v31\n"
    "v_exp_f32 v32, v32\n"
    "v_exp_f32 v33, v33\n"
    "v_exp_f32 v34, v34\n"
    "s_waitcnt lgkmcnt(0)\n"
    "v_smfmac_f32_32x32x32_bf16 v[128:143], v[76:79], v[24:31], v15\n"
    "v_exp_f32 v35, v35\n"
    "v_exp_f32 v36, v36\n"
    "v_exp_f32 v37, v37\n"
    "v_mfma_f32_32x32x16_bf16 v[16:31], v[64:67], v[144:147], -4.0\n"
    "v_exp_f32 v38, v38\n"
    "v_exp_f32 v39, v39\n"
    "v_exp_f32 v40, v40\n"
    "v_exp_f32 v41, v41\n"
    "v_exp_f32 v42, v42\n"
    "v_smfmac_f32_32x32x32_bf16 v[48:63], v[72:75], v[32:39], v15\n"
    "ds_read_b128 v[72:75], v13 offset:62400\n"
    "v_exp_f32 v43, v43\n"
    "v_exp_f32 v44, v44\n"
    "v_exp_f32 v45, v45\n"
    "v_exp_f32 v46, v46\n"
    "v_exp_f32 v47, v47\n"
    "v_exp_f32 v16, v16\n"
    "v_exp_f32 v17, v17\n"
    "v_exp_f32 v18, v18\n"
    "v_smfmac_f32_32x32x32_bf16 v[48:63], v[76:79], v[40:47], v15\n"
    "ds_read_b128 v[76:79], v14 offset:58240\n"
    "v_exp_f32 v19, v19\n"
    "v_exp_f32 v20, v20\n"
    "v_exp_f32 v21, v21\n"
    "v_mfma_f32_32x32x16_bf16 v[32:47], v[64:67], v[8:11], -4.0\n"
    "ds_read_b128 v[64:67], v12 offset:62400\n"
    "v_exp_f32 v22, v22\n"
    "v_exp_f32 v23, v23\n"
    "v_exp_f32 v24, v24\n"
    "v_exp_f32 v25, v25\n"
    "v_exp_f32 v26, v26\n"
    "v_smfmac_f32_32x32x32_bf16 v[128:143], v[68:71], v[16:23], v15\n"
    "v_exp_f32 v27, v27\n"
    "v_exp_f32 v28, v28\n"
    "v_exp_f32 v29, v29\n"
    "v_exp_f32 v30, v30\n"
    "v_exp_f32 v31, v31\n"
    "v_exp_f32 v32, v32\n"
    "v_exp_f32 v33, v33\n"
    "v_exp_f32 v34, v34\n"
    "s_waitcnt lgkmcnt(0)\n"
    "v_smfmac_f32_32x32x32_bf16 v[128:143], v[76:79], v[24:31], v15\n"
    "v_exp_f32 v35, v35\n"
    "v_exp_f32 v36, v36\n"
    "v_exp_f32 v37, v37\n"
    "v_mfma_f32_32x32x16_bf16 v[16:31], v[64:67], v[144:147], -4.0\n"
    "v_exp_f32 v38, v38\n"
    "v_exp_f32 v39, v39\n"
    "v_exp_f32 v40, v40\n"
    "v_exp_f32 v41, v41\n"
    "v_exp_f32 v42, v42\n"
    "v_smfmac_f32_32x32x32_bf16 v[48:63], v[68:71], v[32:39], v15\n"
    "v_exp_f32 v43, v43\n"
    "v_exp_f32 v44, v44\n"
    "v_exp_f32 v45, v45\n"
    "v_exp_f32 v46, v46\n"
    "v_exp_f32 v47, v47\n"
    "s_nop 0\n"
    "v_exp_f32 v16, v16\n"
    "v_exp_f32 v17, v17\n"
    "v_exp_f32 v18, v18\n"
    "v_smfmac_f32_32x32x32_bf16 v[48:63], v[76:79], v[40:47], v15\n"
    "ds_read_b128 v[76:79], v14 offset:62400\n"
    "v_exp_f32 v19, v19\n"
    "v_exp_f32 v20, v20\n"
    "v_exp_f32 v21, v21\n"
    "v_mfma_f32_32x32x16_bf16 v[32:47], v[64:67], v[8:11], -4.0\n"
    "v_exp_f32 v22, v22\n"
    "v_exp_f32 v23, v23\n"
    "v_exp_f32 v24, v24\n"
    "v_exp_f32 v25, v25\n"
    "v_exp_f32 v26, v26\n"
    "v_smfmac_f32_32x32x32_bf16 v[128:143], v[72:75], v[16:23], v15\n"
    "v_exp_f32 v27, v27\n"
    "v_exp_f32 v28, v28\n"
    "v_exp_f32 v29, v29\n"
    "v_exp_f32 v30, v30\n"
    "v_exp_f32 v31, v31\n"
    "s_nop 0\n"
    "v_exp_f32 v32, v32\n"
    "v_exp_f32 v33, v33\n"
    "v_exp_f32 v34, v34\n"
    "s_waitcnt lgkmcnt(0)\n"
    "v_smfmac_f32_32x32x32_bf16 v[128:143], v[76:79], v[24:31], v15\n"
    "v_exp_f32 v35, v35\n"
    "v_exp_f32 v36, v36\n"
    "v_exp_f32 v37, v37\n"
    "s_xor_b32 s58, s53, 1\n"
    "s_lshl_b32 s58, s58, 10\n"
    "s_lshl1_add_u32 s59, s58, 133120\n"
    "s_add_u32 s58, s58, 159488\n"
    "v_add_u32 v12, s59, v1\n"
    "v_lshl_add_u32 v13, v0, 2, s58\n"
    "v_exp_f32 v38, v38\n"
    "v_exp_f32 v39, v39\n"
    "v_exp_f32 v40, v40\n"
    "v_exp_f32 v41, v41\n"
    "v_exp_f32 v42, v42\n"
    "v_smfmac_f32_32x32x32_bf16 v[48:63], v[72:75], v[32:39], v15\n"
    "ds_write_b128 v12, v[128:131]\n"
    "v_exp_f32 v43, v43\n"
    "v_exp_f32 v44, v44\n"
    "ds_write_b128 v12, v[132:135] offset:1024\n"
    "v_exp_f32 v45, v45\n"
    "v_exp_f32 v46, v46\n"
    "ds_write_b32 v13, v136\n"
    "v_exp_f32 v47, v47\n"
    "s_lshl_b32 s58, s18, 1\n"
    "v_subrev_u32 v13, s18, v13\n"
    "v_smfmac_f32_32x32x32_bf16 v[48:63], v[76:79], v[40:47], v15\n"
    "v_subrev_u32 v12, s58, v12\n"
    "ds_write_b8 v4, v4 offset:160\n"
    "s_mov_b32 s62, 0x100000\n"
    "v_mov_b32 v17, 150496\n"
    "s_waitcnt lgkmcnt(0)\n"
    "s_nop 6\n"
    ".Llw8_%=:\n"
    "ds_read_b32 v16, v17\n"
    "s_waitcnt lgkmcnt(0)\n"
    "v_cmp_ne_u32 vcc, 0x43424140, v16\n"
    "s_cbranch_vccz .Llwd9_%=\n"
    "s_sleep 0\n"
    "s_sub_u32 s62, s62, 1\n"
    "s_cbranch_scc0 .Llw8_%=\n"
    ".Llwd9_%=:\n"
    "ds_read_b32 v72, v13\n"
    "ds_read_b128 v[64:67], v12\n"
    "ds_read_b128 v[68:71], v12 offset:1024\n"
    "s_waitcnt lgkmcnt(2)\n"
    "v_add_f32 v56, v56, v72\n"
    "s_waitcnt lgkmcnt(1)\n"
    "v_add_f32 v48, v48, v64\n"
    "v_add_f32 v49, v49, v65\n"
    "v_add_f32 v50, v50, v66\n"
    "v_add_f32 v51, v51, v67\n"
    "s_waitcnt lgkmcnt(0)\n"
    "v_add_f32 v52, v52, v68\n"
    "v_add_f32 v53, v53, v69\n"
    "v_add_f32 v54, v54, v70\n"
    "v_add_f32 v55, v55, v71\n"
    ".Lhop0_%=:\n"
    ".Lpad0_%=:\n"
    "s_cbranch_execz .Lpad1_%=\n"
    "; ---- safety check of the un-shifted exponentials: row sum finite and far from the f32 range ends\n"
    "v_mov_b32 v16, v56\n"
    "v_mov_b32 v17, v56\n"
    "s_nop 1\n"
    "v_permlane32_swap_b32 v16, v17\n"
    "v_max_f32 v17, 0x21800000, v16\n"
    "v_min_f32 v17, 0x59000000, v17\n"
    "v_cmp_neq_f32 vcc, v17, v16\n"
    "s_cbranch_vccnz .Lfb2_%=\n"
    ".LretA3_%=:\n"
    "v_rcp_f32 v16, v16\n"
    "s_add_u32 s59, s20, 2097152\n"
    "v_mul_f32 v48, v16, v48\n"
    "v_mul_f32 v49, v16, v49\n"
    "v_mul_f32 v50, v16, v50\n"
    "v_mul_f32 v51, v16, v51\n"
    "v_mul_f32 v52, v16, v52\n"
    "v_mul_f32 v53, v16, v53\n"
    "v_mul_f32 v54, v16, v54\n"
    "v_mul_f32 v55, v16, v55\n"
    "v_cvt_pk_bf16_f32 v48, v48, v49\n"
    "v_cvt_pk_bf16_f32 v49, v50, v51\n"
    "v_cvt_pk_bf16_f32 v50, v52, v53\n"
    "v_cvt_pk_bf16_f32 v51, v54, v55\n"
    "buffer_store_dwordx4 v[48:51], v1, s[44:47], s59 offen sc1\n"
    "s_setprio 2\n"
    "v_lshlrev_b32 v24, 16, v108\n"
    "v_and_b32 v25, 0xffff0000, v108\n"
    "v_lshlrev_b32 v26, 16, v109\n"
    "v_and_b32 v27, 0xffff0000, v109\n"
    "v_lshlrev_b32 v28, 16, v110\n"
    "v_and_b32 v29, 0xffff0000, v110\n"
    "v_lshlrev_b32 v30, 16, v111\n"
    "v_and_b32 v31, 0xffff0000, v111\n"
    "v_mov_b32 v10, v124\n"
    "v_mov_b32 v11, v125\n"
    "v_mov_b32 v14, v126\n"
    "v_mov_b32 v15, v127\n"
    "v_mov_b32 v19, v5\n"
    "s_mov_b32 s62, 0x100000\n"
    "v_mov_b32 v33, 150464\n"
    "s_nop 2\n"
    ".Llw10_%=:\n"
    "ds_read_b32 v32, v33\n"
    "s_waitcnt lgkmcnt(0)\n"
    "v_cmp_ne_u32 vcc, 0x43424140, v32\n"
    "s_cbranch_vccz .Llwd11_%=\n"
    "s_sleep 4\n"
    "s_sub_u32 s62, s62, 1\n"
    "s_cbranch_scc0 .Llw10_%=\n"
    ".Llwd11_%=:\n"
    "s_add_u32 s61, s61, 4096\n"
    "s_and_b32 s61, s61, 0x3fff\n"
    "buffer_load_dwordx4 v[80:83], v7, s[44:47], s61 offen offset:0 sc1\n"
    "buffer_load_dwordx4 v[84:87], v7, s[44:47], s61 offen offset:1024 sc1\n"
    "buffer_load_dwordx4 v[88:91], v7, s[44:47], s61 offen offset:2048 sc1\n"
    "buffer_load_dwordx4 v[92:95], v7, s[44:47], s61 offen offset:3072 sc1\n"
    "s_add_u32 s61, s61, 4096\n"
    "s_and_b32 s61, s61, 0x3fff\n"
    "buffer_load_dwordx4 v[96:99], v7, s[44:47], s61 offen offset:0 sc1\n"
    "buffer_load_dwordx4 v[100:103], v7, s[44:47], s61 offen offset:1024 sc1\n"
    "buffer_load_dwordx4 v[104:107], v7, s[44:47], s61 offen offset:2048 sc1\n"
    "buffer_load_dwordx4 v[108:111], v7, s[44:47], s61 offen offset:3072 sc1\n"
    "v_bfe_u32 v20, v0, 4, 1\n"
    "v_lshlrev_b32 v20, 10, v20\n"
    "v_lshrrev_b32 v21, 5, v0\n"
    "v_lshl_add_u32 v20, v21, 9, v20\n"
    "v_and_b32 v21, 0xf0, v1\n"
    "v_add3_u32 v20, v20, v21, 0\n"
    "s_add_u32 s59, s8, 64\n"
    "s_waitcnt vmcnt(8)\n"
    "s_mov_b64 exec, 1\n"
    "buffer_store_dwordx2 v[2:3], off, s[44:47], s59 sc1\n"
    "s_mov_b64 exec, -1\n"
    "s_add_u32 s61, s61, 4096\n"
    "s_and_b32 s61, s61, 0x3fff\n"
    "buffer_load_dwordx4 v[112:115], v7, s[44:47], s61 offen offset:0 sc1\n"
    "buffer_load_dwordx4 v[116:119], v7, s[44:47], s61 offen offset:1024 sc1\n"
    "buffer_load_dwordx4 v[120:123], v7, s[44:47], s61 offen offset:2048 sc1\n"
    "buffer_load_dwordx4 v[124:127], v7, s[44:47], s61 offen offset:3072 sc1\n"
    "s_add_u32 s61, s61, 4096\n"
    "s_and_b32 s61, s61, 0x3fff\n"
    "buffer_load_dwordx4 v[64:67], v7, s[44:47], s61 offen offset:0 sc1\n"
    "buffer_load_dwordx4 v[68:71], v7, s[44:47], s61 offen offset:1024 sc1\n"
    "buffer_load_dwordx4 v[72:75], v7, s[44:47], s61 offen offset:2048 sc1\n"
    "buffer_load_dwordx4 v[76:79], v7, s[44:47], s61 offen offset:3072 sc1\n"
    "buffer_load_dwordx4 v[128:131], v6, s[44:47], s35 offen offset:0 sc1\n"
    "buffer_load_dwordx4 v[132:135], v6, s[44:47], s35 offen offset:1024 sc1\n"
    "buffer_load_dwordx4 v[136:139], v6, s[44:47], s35 offen offset:2048 sc1\n"
    "buffer_load_dwordx4 v[140:143], v6, s[44:47], s35 offen offset:3072 sc1\n"
    "s_add_u32 s35, s35, 4096\n"
    "buffer_load_dwordx4 v[144:147], v6, s[44:47], s35 offen offset:0 sc1\n"
    "buffer_load_dwordx4 v[148:151], v6, s[44:47], s35 offen offset:1024 sc1\n"
    "buffer_load_dwordx4 v[152:155], v6, s[44:47], s35 offen offset:2048 sc1\n"
    "buffer_load_dwordx4 v[156:159], v6, s[44:47], s35 offen offset:3072 sc1\n"
    "s_add_u32 s35, s35, 4096\n"
    "s_and_b32 s35, s35, 0x7fff\n"
    "buffer_load_dwordx4 v[160:163], v6, s[44:47], s35 offen offset:0 sc1\n"
    "buffer_load_dwordx4 v[164:167], v6, s[44:47], s35 offen offset:1024 sc1\n"
    "buffer_load_dwordx4 v[168:171], v6, s[44:47], s35 offen offset:2048 sc1\n"
    "buffer_load_dwordx4 v[172:175], v6, s[44:47], s35 offen offset:3072 sc1\n"
    "s_add_u32 s35, s35, 4096\n"
    "buffer_load_dwordx4 v[176:179], v6, s[44:47], s35 offen offset:0 sc1\n"
    "buffer_load_dwordx4 v[180:183], v6, s[44:47], s35 offen offset:1024 sc1\n"
    "buffer_load_dwordx4 v[184:187], v6, s[44:47], s35 offen offset:2048 sc1\n"
    "buffer_load_dwordx4 v[188:191], v6, s[44:47], s35 offen offset:3072 sc1\n"
    "s_add_u32 s59, s23, 64\n"
    "buffer_load_dwordx2 v[16:17], v19, s[44:47], s59 offen sc1\n"
    "s_add_u32 s35, s35, 4096\n"
    "s_and_b32 s35, s35, 0x7fff\n"
    "buffer_load_dwordx4 v[192:195], v6, s[44:47], s35 offen offset:0 sc1\n"
    "buffer_load_dwordx4 v[196:199], v6, s[44:47], s35 offen offset:1024 sc1\n"
    "buffer_load_dwordx4 v[200:203], v6, s[44:47], s35 offen offset:2048 sc1\n"
    "buffer_load_dwordx4 v[204:207], v6, s[44:47], s35 offen offset:3072 sc1\n"
    "s_add_u32 s35, s35, 4096\n"
    "buffer_load_dwordx4 v[208:211], v6, s[44:47], s35 offen offset:0 sc1\n"
    "buffer_load_dwordx4 v[212:215], v6, s[44:47], s35 offen offset:1024 sc1\n"
    "buffer_load_dwordx4 v[216:219], v6, s[44:47], s35 offen offset:2048 sc1\n"
    "buffer_load_dwordx4 v[220:223], v6, s[44:47], s35 offen offset:3072 sc1\n"
    "s_waitcnt vmcnt(8)\n"
    "v_cmp_ne_u64 vcc, s[56:57], v[16:17]\n"
    "s_cbranch_vccnz .Lpagain13_%=\n"
    ".Lpad1_%=:\n"
    "s_cbranch_execz .Lpad2_%=\n"
    ".Lpok12_%=:\n"
    "s_mov_b32 s62, 0x100000\n"
    "v_mov_b32 v33, 150368\n"
    "s_nop 2\n"
    ".Llw17_%=:\n"
    "ds_read_b32 v32, v33\n"
    "s_waitcnt lgkmcnt(0)\n"
    "v_cmp_ne_u32 vcc, 0x43424140, v32\n"
    "s_cbranch_vccz .Llwd18_%=\n"
    "s_sleep 4\n"
    "s_sub_u32 s62, s62, 1\n"
    "s_cbranch_scc0 .Llw17_%=\n"
    ".Llwd18_%=:\n"
    "s_add_u32 s58, s22, 2097152\n"
    "s_add_u32 s61, s58, 4096\n"
    "s_lshl_b32 m0, s53, 13\n"
    "s_add_u32 m0, m0, 0\n"
    "s_nop 0\n"
    "buffer_load_dwordx4 v1, s[44:47], s58 offen sc1 lds\n"
    "buffer_load_dwordx4 v1, s[44:47], s58 offen offset:1024 sc1 lds\n"
    "buffer_load_dwordx4 v1, s[44:47], s58 offen offset:2048 sc1 lds\n"
    "buffer_load_dwordx4 v1, s[44:47], s58 offen offset:3072 sc1 lds\n"
    "s_add_u32 m0, m0, 4096\n"
    "s_nop 0\n"
    "buffer_load_dwordx4 v1, s[44:47], s61 offen sc1 lds\n"
    "buffer_load_dwordx4 v1, s[44:47], s61 offen offset:1024 sc1 lds\n"
    "buffer_load_dwordx4 v1, s[44:47], s61 offen offset:2048 sc1 lds\n"
    "buffer_load_dwordx4 v1, s[44:47], s61 offen offset:3072 sc1 lds\n"
    "s_add_u32 s35, s35, 4096\n"
    "s_and_b32 s35, s35, 0x7fff\n"
    "buffer_load_dwordx4 v[224:227], v6, s[44:47], s35 offen offset:0 sc1\n"
    "buffer_load_dwordx4 v[228:231], v6, s[44:47], s35 offen offset:1024 sc1\n"
    "buffer_load_dwordx4 v[232:235], v6, s[44:47], s35 offen offset:2048 sc1\n"
    "buffer_load_dwordx4 v[236:239], v6, s[44:47], s35 offen offset:3072 sc1\n"
    "s_add_u32 s35, s35, 4096\n"
    "buffer_load_dwordx4 v[240:243], v6, s[44:47], s35 offen offset:0 sc1\n"
    "buffer_load_dwordx4 v[244:247], v6, s[44:47], s35 offen offset:1024 sc1\n"
    "buffer_load_dwordx4 v[248:251], v6, s[44:47], s35 offen offset:2048 sc1\n"
    "buffer_load_dwordx4 v[252:255], v6, s[44:47], s35 offen offset:3072 sc1\n"
    "s_waitcnt vmcnt(8)\n"
    "ds_write_b8 v4, v4 offset:80\n"
    "s_mov_b32 s62, 0x100000\n"
    "v_mov_b32 v33, 150416\n"
    "s_waitcnt lgkmcnt(0)\n"
    "s_nop 1\n"
    ".Llw19_%=:\n"
    "ds_read_b32 v32, v33\n"
    "s_waitcnt lgkmcnt(0)\n"
    "v_cmp_ne_u32 vcc, 0x43424140, v32\n"
    "s_cbranch_vccz .Llwd20_%=\n"
    "s_sleep 1\n"
    "s_sub_u32 s62, s62, 1\n"
    "s_cbranch_scc0 .Llw19_%=\n"
    ".Llwd20_%=:\n"
    "s_setprio 0\n"
    "ds_read_b128 v[32:35], v20 offset:0\n"
    "ds_read_b128 v[36:39], v20 offset:256\n"
    "ds_read_b128 v[40:43], v20 offset:2048\n"
    "ds_read_b128 v[44:47], v20 offset:2304\n"
    "ds_read_b128 v[48:51], v20 offset:4096\n"
    "ds_read_b128 v[52:55], v20 offset:4352\n"
    "s_waitcnt lgkmcnt(5)\n"
    "v_mfma_f32_16x16x32_bf16 v[24:27], v[64:67], v[32:35], v[24:27]\n"
    "ds_read_b128 v[32:35], v20 offset:6144\n"
    "s_waitcnt lgkmcnt(5)\n"
    "v_mfma_f32_16x16x32_bf16 v[28:31], v[64:67], v[36:39], v[28:31]\n"
    "ds_read_b128 v[36:39], v20 offset:6400\n"
    "s_waitcnt lgkmcnt(5)\n"
    "v_mfma_f32_16x16x32_bf16 v[24:27], v[68:71], v[40:43], v[24:27]\n"
    "ds_read_b128 v[40:43], v20 offset:8192\n"
    "s_waitcnt lgkmcnt(5)\n"
    "v_mfma_f32_16x16x32_bf16 v[28:31], v[68:71], v[44:47], v[28:31]\n"
    "ds_read_b128 v[44:47], v20 offset:8448\n"
    "s_waitcnt lgkmcnt(5)\n"
    "v_mfma_f32_16x16x32_bf16 v[24:27], v[72:75], v[48:51], v[24:27]\n"
    "ds_read_b128 v[48:51], v20 offset:10240\n"
    "s_waitcnt lgkmcnt(5)\n"
    "v_mfma_f32_16x16x32_bf16 v[28:31], v[72:75], v[52:55], v[28:31]\n"
    "ds_read_b128 v[52:55], v20 offset:10496\n"
    "s_waitcnt lgkmcnt(5)\n"
    "v_mfma_f32_16x16x32_bf16 v[24:27], v[76:79], v[32:35], v[24:27]\n"
    "ds_read_b128 v[32:35], v20 offset:12288\n"
    "s_waitcnt lgkmcnt(5)\n"
    "v_mfma_f32_16x16x32_bf16 v[28:31], v[76:79], v[36:39], v[28:31]\n"
    "ds_read_b128 v[36:39], v20 offset:12544\n"
    "s_waitcnt lgkmcnt(5)\n"
    "v_mfma_f32_16x16x32_bf16 v[24:27], v[80:83], v[40:43], v[24:27]\n"
    "ds_read_b128 v[40:43], v20 offset:14336\n"
    "s_waitcnt lgkmcnt(5)\n"
    "v_mfma_f32_16x16x32_bf16 v[28:31], v[80:83], v[44:47], v[28:31]\n"
    "ds_read_b128 v[44:47], v20 offset:14592\n"
    "s_waitcnt lgkmcnt(5)\n"
    "v_mfma_f32_16x16x32_bf16 v[24:27], v[84:87], v[48:51], v[24:27]\n"
    "ds_read_b128 v[48:51], v20 offset:16384\n"
    "s_waitcnt lgkmcnt(5)\n"
    "v_mfma_f32_16x16x32_bf16 v[28:31], v[84:87], v[52:55], v[28:31]\n"
    "ds_read_b128 v[52:55], v20 offset:16640\n"
    "s_waitcnt lgkmcnt(5)\n"
    "v_mfma_f32_16x16x32_bf16 v[24:27], v[88:91], v[32:35], v[24:27]\n"
    "ds_read_b128 v[32:35], v20 offset:18432\n"
    "s_waitcnt lgkmcnt(5)\n"
    "v_mfma_f32_16x16x32_bf16 v[28:31], v[88:91], v[36:39], v[28:31]\n"
    "ds_read_b128 v[36:39], v20 offset:18688\n"
    "s_waitcnt lgkmcnt(5)\n"
    "v_mfma_f32_16x16x32_bf16 v[24:27], v[92:95], v[40:43], v[24:27]\n"
    "ds_read_b128 v[40:43], v20 offset:20480\n"
    "s_waitcnt lgkmcnt(5)\n"
    "v_mfma_f32_16x16x32_bf16 v[28:31], v[92:95], v[44:47], v[28:31]\n"
    "ds_read_b128 v[44:47], v20 offset:20736\n"
    "s_waitcnt lgkmcnt(5)\n"
    "v_mfma_f32_16x16x32_bf16 v[24:27], v[96:99], v[48:51], v[24:27]\n"
    "ds_read_b128 v[48:51], v20 offset:22528\n"
    "s_waitcnt lgkmcnt(5)\n"
    "v_mfma_f32_16x16x32_bf16 v[28:31], v[96:99], v[52:55], v[28:31]\n"
    "ds_read_b128 v[52:55], v20 offset:22784\n"
    "s_waitcnt lgkmcnt(5)\n"
    "v_mfma_f32_16x16x32_bf16 v[24:27], v[100:103], v[32:35], v[24:27]\n"
    "ds_read_b128 v[32:35], v20 offset:24576\n"
    "s_waitcnt lgkmcnt(5)\n"
    "v_mfma_f32_16x16x32_bf16 v[28:31], v[100:103], v[36:39], v[28:31]\n"
    "ds_read_b128 v[36:39], v20 offset:24832\n"
    "s_waitcnt lgkmcnt(5)\n"
    "v_mfma_f32_16x16x32_bf16 v[24:27], v[104:107], v[40:43], v[24:27]\n"
    ".Lpad2_%=:\n"
    "s_cbranch_execz .Lpad3_%=\n"
    "ds_read_b128 v[40:43], v20 offset:26624\n"
    "s_waitcnt lgkmcnt(5)\n"
    "v_mfma_f32_16x16x32_bf16 v[28:31], v[104:107], v[44:47], v[28:31]\n"
    "ds_read_b128 v[44:47], v20 offset:26880\n"
    "s_waitcnt lgkmcnt(5)\n"
    "v_mfma_f32_16x16x32_bf16 v[24:27], v[108:111], v[48:51], v[24:27]\n"
    "ds_read_b128 v[48:51], v20 offset:28672\n"
    "s_waitcnt lgkmcnt(5)\n"
    "v_mfma_f32_16x16x32_bf16 v[28:31], v[108:111], v[52:55], v[28:31]\n"
    "ds_read_b128 v[52:55], v20 offset:28928\n"
    "s_waitcnt lgkmcnt(5)\n"
    "v_mfma_f32_16x16x32_bf16 v[24:27], v[112:115], v[32:35], v[24:27]\n"
    "ds_read_b128 v[32:35], v20 offset:30720\n"
    "s_waitcnt lgkmcnt(5)\n"
    "v_mfma_f32_16x16x32_bf16 v[28:31], v[112:115], v[36:39], v[28:31]\n"
    "ds_read_b128 v[36:39], v20 offset:30976\n"
    "s_waitcnt lgkmcnt(5)\n"
    "v_mfma_f32_16x16x32_bf16 v[24:27], v[116:119], v[40:43], v[24:27]\n"
    "s_waitcnt lgkmcnt(4)\n"
    "v_mfma_f32_16x16x32_bf16 v[28:31], v[116:119], v[44:47], v[28:31]\n"
    "s_waitcnt lgkmcnt(3)\n"
    "v_mfma_f32_16x16x32_bf16 v[24:27], v[120:123], v[48:51], v[24:27]\n"
    "s_waitcnt lgkmcnt(2)\n"
    "v_mfma_f32_16x16x32_bf16 v[28:31], v[120:123], v[52:55], v[28:31]\n"
    "s_waitcnt lgkmcnt(1)\n"
    "v_mfma_f32_16x16x32_bf16 v[24:27], v[124:127], v[32:35], v[24:27]\n"
    "s_waitcnt lgkmcnt(0)\n"
    "v_mfma_f32_16x16x32_bf16 v[28:31], v[124:127], v[36:39], v[28:31]\n"
    "s_setprio 2\n"
    "s_nop 4\n"
    "v_cvt_pk_bf16_f32 v32, v24, v25\n"
    "v_cvt_pk_bf16_f32 v33, v26, v27\n"
    "v_cvt_pk_bf16_f32 v34, v28, v29\n"
    "v_cvt_pk_bf16_f32 v35, v30, v31\n"
    "s_add_u32 s59, s20, 4194304\n"
    "v_permlane16_swap_b32 v32, v34\n"
    "v_permlane16_swap_b32 v33, v35\n"
    "buffer_store_dwordx4 v[32:35], v1, s[44:47], s59 offen sc1\n"
    "v_add_f32 v36, v24, v25\n"
    "v_mul_f32 v38, v24, v24\n"
    "v_add_f32 v37, v28, v29\n"
    "v_mul_f32 v39, v28, v28\n"
    "v_add_f32 v36, v36, v26\n"
    "v_add_f32 v37, v37, v30\n"
    "v_add_f32 v36, v36, v27\n"
    "v_add_f32 v37, v37, v31\n"
    "v_fmac_f32 v38, v25, v25\n"
    "v_fmac_f32 v39, v29, v29\n"
    "v_fmac_f32 v38, v26, v26\n"
    "v_fmac_f32 v39, v30, v30\n"
    "v_fmac_f32 v38, v27, v27\n"
    "v_fmac_f32 v39, v31, v31\n"
    "v_permlane16_swap_b32 v36, v37\n"
    "s_lshr_b32 s59, s20, 2\n"
    "v_permlane16_swap_b32 v38, v39\n"
    "v_add_f32 v36, v36, v37\n"
    "v_add_f32 v38, v38, v39\n"
    "s_add_u32 s59, s59, 524288\n"
    "v_lshlrev_b32 v37, 2, v0\n"
    "v_permlane32_swap_b32 v36, v38\n"
    "v_add_f32 v36, v36, v38\n"
    "buffer_store_dword v36, v37, s[44:47], s59 offen sc1\n"
    "s_add_u32 s59, s8, 576\n"
    "s_waitcnt vmcnt(0)\n"
    "s_mov_b64 exec, 1\n"
    "buffer_store_dwordx2 v[2:3], off, s[44:47], s59 sc1\n"
    "s_mov_b64 exec, -1\n"
    "s_add_u32 s60, s34, 0\n"
    "buffer_load_dwordx4 v[56:59], v1, s[44:47], s60 offen offset:0 sc1\n"
    "buffer_load_dwordx4 v[60:63], v1, s[44:47], s60 offen offset:1024 sc1\n"
    "v_and_b32 v44, 31, v0\n"
    "v_lshl_add_u32 v40, v44, 1, s63\n"
    "s_mul_i32 s59, s51, 7\n"
    "s_add_u32 s59, s59, s49\n"
    "s_lshl_b32 s58, s59, 6\n"
    "buffer_load_ushort v4, v40, s[28:31], s58 offen\n"
    "v_and_b32 v40, 15, v0\n"
    "v_lshlrev_b32 v40, 3, v40\n"
    "buffer_load_dwordx2 v[6:7], v40, s[36:39], s66 offen\n"
    "v_lshrrev_b32 v40, 5, v0\n"
    "v_lshl_add_u32 v40, v40, 9, s63\n"
    "v_lshl_add_u32 v44, v44, 2, v40\n"
    "s_lshl_b32 s58, s59, 10\n"
    "s_add_u32 s58, s58, 458752\n"
    "buffer_load_dword v40, v44, s[44:47], s58 offen offset:0 sc1\n"
    "buffer_load_dword v41, v44, s[44:47], s58 offen offset:128 sc1\n"
    "buffer_load_dword v42, v44, s[44:47], s58 offen offset:256 sc1\n"
    "buffer_load_dword v43, v44, s[44:47], s58 offen offset:384 sc1\n"
    "s_mov_b32 s62, 0x100000\n"
    "v_mov_b32 v33, 150384\n"
    "s_nop 2\n"
    ".Llw21_%=:\n"
    "ds_read_b32 v32, v33\n"
    "s_waitcnt lgkmcnt(0)\n"
    "v_cmp_ne_u32 vcc, 0x43424140, v32\n"
    "s_cbranch_vccz .Llwd22_%=\n"
    "s_sleep 4\n"
    "s_sub_u32 s62, s62, 1\n"
    "s_cbranch_scc0 .Llw21_%=\n"
    ".Llwd22_%=:\n"
    "s_add_u32 s59, s23, 576\n"
    "s_xor_b32 s58, s99, 4\n"
    "s_lshl3_add_u32 s58, s51, s58\n"
    "s_mul_i32 s58, s58, 28672\n"
    "s_add_u32 s58, s58, 15484928\n"
    "s_sub_u32 m0, s33, 40960\n"
    "s_nop 0\n"
    "buffer_load_dwordx4 v1, s[44:47], s58 offen sc1 lds\n"
    "buffer_load_dwordx4 v1, s[44:47], s58 offen offset:1024 sc1 lds\n"
    "buffer_load_dwordx4 v1, s[44:47], s58 offen offset:2048 sc1 lds\n"
    "buffer_load_dwordx4 v1, s[44:47], s58 offen offset:3072 sc1 lds\n"
    "s_add_u32 m0, m0, 4096\n"
    "s_add_u32 s58, s58, 4096\n"
    "buffer_load_dwordx4 v1, s[44:47], s58 offen sc1 lds\n"
    "buffer_load_dwordx4 v1, s[44:47], s58 offen offset:1024 sc1 lds\n"
    "buffer_load_dwordx4 v1, s[44:47], s58 offen offset:2048 sc1 lds\n"
    "buffer_load_dwordx4 v1, s[44:47], s58 offen offset:3072 sc1 lds\n"
    "s_add_u32 m0, m0, 4096\n"
    "s_add_u32 s58, s58, 4096\n"
    "buffer_load_dwordx4 v1, s[44:47], s58 offen sc1 lds\n"
    ".Lpad3_%=:\n"
    "s_cbranch_execz .Lpad4_%=\n"
    "buffer_load_dwordx4 v1, s[44:47], s58 offen offset:1024 sc1 lds\n"
    "s_sleep 4\n"
    "buffer_load_dwordx2 v[16:17], v19, s[44:47], s59 offen sc1\n"
    "s_waitcnt vmcnt(13)\n"
    "v_add_f32 v40, v40, v41\n"
    "s_waitcnt vmcnt(11)\n"
    "v_add_f32 v42, v42, v43\n"
    "v_add_f32 v40, v40, v42\n"
    "v_mov_b32 v41, v40\n"
    "v_lshlrev_b32 v4, 16, v4\n"
    "v_cmp_gt_u32 vcc, 32, v0\n"
    "v_permlane32_swap_b32 v40, v41\n"
    "v_add_f32 v40, v40, v41\n"
    "v_cndmask_b32 v40, v4, v40, vcc\n"
    "v_cvt_pk_bf16_f32 v4, v40, v40\n"
    "v_and_b32 v41, 0xffff0000, v4\n"
    "v_sub_f32 v40, v40, v41\n"
    "v_cvt_pk_bf16_f32 v5, v40, v40\n"
    "s_add_u32 s58, s22, 4194304\n"
    "s_add_u32 s61, s58, 4096\n"
    "s_lshl_b32 m0, s53, 13\n"
    "s_add_u32 m0, m0, 0\n"
    "s_waitcnt vmcnt(0)\n"
    "v_cmp_ne_u64 vcc, s[56:57], v[16:17]\n"
    "s_cbranch_vccnz .Lpagain24_%=\n"
    ".Lpok23_%=:\n"
    "buffer_load_dwordx4 v1, s[44:47], s58 offen sc1 lds\n"
    "buffer_load_dwordx4 v1, s[44:47], s58 offen offset:1024 sc1 lds\n"
    "buffer_load_dwordx4 v1, s[44:47], s58 offen offset:2048 sc1 lds\n"
    "buffer_load_dwordx4 v1, s[44:47], s58 offen offset:3072 sc1 lds\n"
    "s_add_u32 m0, m0, 4096\n"
    "s_nop 0\n"
    "buffer_load_dwordx4 v1, s[44:47], s61 offen sc1 lds\n"
    "buffer_load_dwordx4 v1, s[44:47], s61 offen offset:1024 sc1 lds\n"
    "buffer_load_dwordx4 v1, s[44:47], s61 offen offset:2048 sc1 lds\n"
    "buffer_load_dwordx4 v1, s[44:47], s61 offen offset:3072 sc1 lds\n"
    "s_lshl_b32 m0, s53, 11\n"
    "s_add_u32 m0, m0, 150784\n"
    "s_lshr_b32 s58, s22, 2\n"
    "s_add_u32 s58, s58, 524288\n"
    "buffer_load_dwordx4 v1, s[44:47], s58 offen sc1 lds\n"
    "buffer_load_dwordx4 v1, s[44:47], s58 offen offset:1024 sc1 lds\n"
    "s_mov_b32 s62, 0x100000\n"
    "v_mov_b32 v33, 150400\n"
    "s_nop 2\n"
    ".Llw28_%=:\n"
    "ds_read_b32 v32, v33\n"
    "s_waitcnt lgkmcnt(0)\n"
    "v_cmp_ne_u32 vcc, 0x43424140, v32\n"
    "s_cbranch_vccz .Llwd29_%=\n"
    "s_sleep 4\n"
    "s_sub_u32 s62, s62, 1\n"
    "s_cbranch_scc0 .Llw28_%=\n"
    ".Llwd29_%=:\n"
    "s_add_u32 s58, s34, 18432\n"
    "s_mov_b32 m0, s33\n"
    "s_nop 0\n"
    "buffer_load_dwordx4 v1, s[44:47], s58 offen sc1 lds\n"
    "buffer_load_dwordx4 v1, s[44:47], s58 offen offset:1024 sc1 lds\n"
    "buffer_load_dwordx4 v1, s[44:47], s58 offen offset:2048 sc1 lds\n"
    "buffer_load_dwordx4 v1, s[44:47], s58 offen offset:3072 sc1 lds\n"
    "s_add_u32 m0, m0, 4096\n"
    "s_add_u32 s58, s58, 4096\n"
    "buffer_load_dwordx4 v1, s[44:47], s58 offen sc1 lds\n"
    "buffer_load_dwordx4 v1, s[44:47], s58 offen offset:1024 sc1 lds\n"
    "buffer_load_dwordx4 v1, s[44:47], s58 offen offset:2048 sc1 lds\n"
    "buffer_load_dwordx4 v1, s[44:47], s58 offen offset:3072 sc1 lds\n"
    "s_add_u32 m0, m0, 4096\n"
    "s_add_u32 s58, s58, 4096\n"
    "buffer_load_dwordx4 v1, s[44:47], s58 offen sc1 lds\n"
    "buffer_load_dwordx4 v1, s[44:47], s58 offen offset:1024 sc1 lds\n"
    "v_bfe_u32 v13, v0, 2, 1\n"
    "v_mul_u32_u24 v13, 2560, v13\n"
    "v_and_b32 v16, 3, v0\n"
    "v_lshl_add_u32 v13, v16, 7, v13\n"
    "s_mov_b32 s16, s9\n"
    "s_lshl_b32 s58, s53, 10\n"
    "s_add_u32 s58, s58, 158976\n"
    "v_lshl_add_u32 v54, v0, 3, s58\n"
    "s_add_u32 s58, s22, 2129920\n"
    "s_add_u32 s61, s58, 4096\n"
    "s_lshl_b32 m0, s53, 13\n"
    "s_add_u32 m0, m0, 114688\n"
    "s_waitcnt vmcnt(0)\n"
    "s_barrier\n"
    "ds_read_b128 v[32:35], v1 offset:0\n"
    "ds_read_b128 v[36:39], v1 offset:1024\n"
    "ds_read_b128 v[40:43], v1 offset:2048\n"
    "s_waitcnt lgkmcnt(2)\n"
    "v_mfma_f32_32x32x16_bf16 v[16:31], v[128:131], v[32:35], 0\n"
    "ds_read_b128 v[44:47], v1 offset:3072\n"
    "s_waitcnt lgkmcnt(2)\n"
    "v_mfma_f32_32x32x16_bf16 v[16:31], v[132:135], v[36:39], v[16:31]\n"
    "ds_read_b128 v[32:35], v1 offset:4096\n"
    "s_waitcnt lgkmcnt(2)\n"
    "v_mfma_f32_32x32x16_bf16 v[16:31], v[136:139], v[40:43], v[16:31]\n"
    "ds_read_b128 v[36:39], v1 offset:5120\n"
    "s_waitcnt lgkmcnt(2)\n"
    "v_mfma_f32_32x32x16_bf16 v[16:31], v[140:143], v[44:47], v[16:31]\n"
    "ds_read_b128 v[40:43], v1 offset:6144\n"
    "s_waitcnt lgkmcnt(2)\n"
    "v_mfma_f32_32x32x16_bf16 v[16:31], v[144:147], v[32:35], v[16:31]\n"
    "ds_read_b128 v[44:47], v1 offset:7168\n"
    "s_waitcnt lgkmcnt(2)\n"
    "v_mfma_f32_32x32x16_bf16 v[16:31], v[148:151], v[36:39], v[16:31]\n"
    "ds_read_b128 v[32:35], v1 offset:8192\n"
    "s_waitcnt lgkmcnt(2)\n"
    "v_mfma_f32_32x32x16_bf16 v[16:31], v[152:155], v[40:43], v[16:31]\n"
    "ds_read_b128 v[36:39], v1 offset:9216\n"
    "s_waitcnt lgkmcnt(2)\n"
    "v_mfma_f32_32x32x16_bf16 v[16:31], v[156:159], v[44:47], v[16:31]\n"
    "ds_read_b128 v[40:43], v1 offset:10240\n"
    "s_waitcnt lgkmcnt(2)\n"
    "v_mfma_f32_32x32x16_bf16 v[16:31], v[160:163], v[32:35], v[16:31]\n"
    "ds_read_b128 v[44:47], v1 offset:11264\n"
    "s_waitcnt lgkmcnt(2)\n"
    "v_mfma_f32_32x32x16_bf16 v[16:31], v[164:167], v[36:39], v[16:31]\n"
    "ds_read_b128 v[32:35], v1 offset:12288\n"
    "s_waitcnt lgkmcnt(2)\n"
    "v_mfma_f32_32x32x16_bf16 v[16:31], v[168:171], v[40:43], v[16:31]\n"
    "ds_read_b128 v[36:39], v1 offset:13312\n"
    ".Lpad4_%=:\n"
    "s_cbranch_execz .Lpad5_%=\n"
    "s_waitcnt lgkmcnt(2)\n"
    "v_mfma_f32_32x32x16_bf16 v[16:31], v[172:175], v[44:47], v[16:31]\n"
    "ds_read_b128 v[40:43], v1 offset:14336\n"
    "s_waitcnt lgkmcnt(2)\n"
    "v_mfma_f32_32x32x16_bf16 v[16:31], v[176:179], v[32:35], v[16:31]\n"
    "ds_read_b128 v[44:47], v1 offset:15360\n"
    "s_waitcnt lgkmcnt(2)\n"
    "v_mfma_f32_32x32x16_bf16 v[16:31], v[180:183], v[36:39], v[16:31]\n"
    "ds_read_b128 v[32:35], v1 offset:16384\n"
    "s_waitcnt lgkmcnt(2)\n"
    "v_mfma_f32_32x32x16_bf16 v[16:31], v[184:187], v[40:43], v[16:31]\n"
    "ds_read_b128 v[36:39], v1 offset:17408\n"
    "s_waitcnt lgkmcnt(2)\n"
    "v_mfma_f32_32x32x16_bf16 v[16:31], v[188:191], v[44:47], v[16:31]\n"
    "ds_read_b128 v[40:43], v1 offset:18432\n"
    "s_waitcnt lgkmcnt(2)\n"
    "v_mfma_f32_32x32x16_bf16 v[16:31], v[192:195], v[32:35], v[16:31]\n"
    "ds_read_b128 v[44:47], v1 offset:19456\n"
    "s_waitcnt lgkmcnt(2)\n"
    "v_mfma_f32_32x32x16_bf16 v[16:31], v[196:199], v[36:39], v[16:31]\n"
    "ds_read_b128 v[32:35], v1 offset:20480\n"
    "s_waitcnt lgkmcnt(2)\n"
    "v_mfma_f32_32x32x16_bf16 v[16:31], v[200:203], v[40:43], v[16:31]\n"
    "ds_read_b128 v[36:39], v1 offset:21504\n"
    "s_waitcnt lgkmcnt(2)\n"
    "v_mfma_f32_32x32x16_bf16 v[16:31], v[204:207], v[44:47], v[16:31]\n"
    "ds_read_b128 v[40:43], v1 offset:22528\n"
    "s_waitcnt lgkmcnt(2)\n"
    "v_mfma_f32_32x32x16_bf16 v[16:31], v[208:211], v[32:35], v[16:31]\n"
    "ds_read_b128 v[44:47], v1 offset:23552\n"
    "s_waitcnt lgkmcnt(2)\n"
    "v_mfma_f32_32x32x16_bf16 v[16:31], v[212:215], v[36:39], v[16:31]\n"
    "ds_read_b128 v[32:35], v1 offset:24576\n"
    "s_waitcnt lgkmcnt(2)\n"
    "v_mfma_f32_32x32x16_bf16 v[16:31], v[216:219], v[40:43], v[16:31]\n"
    "ds_read_b128 v[36:39], v1 offset:25600\n"
    "s_waitcnt lgkmcnt(2)\n"
    "v_mfma_f32_32x32x16_bf16 v[16:31], v[220:223], v[44:47], v[16:31]\n"
    "ds_read_b128 v[40:43], v1 offset:26624\n"
    "s_waitcnt lgkmcnt(2)\n"
    "v_mfma_f32_32x32x16_bf16 v[16:31], v[224:227], v[32:35], v[16:31]\n"
    "ds_read_b128 v[44:47], v1 offset:27648\n"
    "s_waitcnt lgkmcnt(2)\n"
    "v_mfma_f32_32x32x16_bf16 v[16:31], v[228:231], v[36:39], v[16:31]\n"
    "ds_read_b128 v[32:35], v1 offset:28672\n"
    "s_waitcnt lgkmcnt(2)\n"
    "v_mfma_f32_32x32x16_bf16 v[16:31], v[232:235], v[40:43], v[16:31]\n"
    "ds_read_b128 v[36:39], v1 offset:29696\n"
    "s_waitcnt lgkmcnt(2)\n"
    "v_mfma_f32_32x32x16_bf16 v[16:31], v[236:239], v[44:47], v[16:31]\n"
    "ds_read_b128 v[40:43], v1 offset:30720\n"
    "s_waitcnt lgkmcnt(2)\n"
    "v_mfma_f32_32x32x16_bf16 v[16:31], v[240:243], v[32:35], v[16:31]\n"
    "ds_read_b128 v[44:47], v1 offset:31744\n"
    "s_waitcnt lgkmcnt(2)\n"
    "v_mfma_f32_32x32x16_bf16 v[16:31], v[244:247], v[36:39], v[16:31]\n"
    "s_waitcnt lgkmcnt(1)\n"
    "v_mfma_f32_32x32x16_bf16 v[16:31], v[248:251], v[40:43], v[16:31]\n"
    "s_barrier\n"
    "ds_read2_b32 v[50:51], v54 offset0:0 offset1:0\n"
    "ds_read_b32 v12, v54 offset:4\n"
    "s_waitcnt lgkmcnt(2)\n"
    "v_mfma_f32_32x32x16_bf16 v[16:31], v[252:255], v[44:47], v[16:31]\n"
    "s_waitcnt lgkmcnt(1)\n"
    "v_mfma_f32_32x32x8_bf16 v[16:31], v[4:5], v[50:51], v[16:31]\n"
    "s_sleep 4\n"
    "buffer_load_dwordx2 v[48:49], v13, s[44:47], s16 offen sc1\n"
    "buffer_load_dwordx4 v1, s[44:47], s58 offen sc1 lds\n"
    "buffer_load_dwordx4 v1, s[44:47], s58 offen offset:1024 sc1 lds\n"
    "buffer_load_dwordx4 v1, s[44:47], s58 offen offset:2048 sc1 lds\n"
    "buffer_load_dwordx4 v1, s[44:47], s58 offen offset:3072 sc1 lds\n"
    "s_add_u32 m0, m0, 4096\n"
    "s_nop 0\n"
    "buffer_load_dwordx4 v1, s[44:47], s61 offen sc1 lds\n"
    "buffer_load_dwordx4 v1, s[44:47], s61 offen offset:1024 sc1 lds\n"
    "buffer_load_dwordx4 v1, s[44:47], s61 offen offset:2048 sc1 lds\n"
    "buffer_load_dwordx4 v1, s[44:47], s61 offen offset:3072 sc1 lds\n"
    "s_setprio 0\n"
    "s_add_u32 s59, s24, 6291456\n"
    "v_or_b32 v52, s63, v1\n"
    "v_mul_f32 v16, v16, abs(v16)\n"
    "v_mul_f32 v17, v17, abs(v17)\n"
    "v_mul_f32 v18, v18, abs(v18)\n"
    "v_mul_f32 v19, v19, abs(v19)\n"
    "v_mul_f32 v20, v20, abs(v20)\n"
    "v_mul_f32 v21, v21, abs(v21)\n"
    "v_mul_f32 v22, v22, abs(v22)\n"
    "v_mul_f32 v23, v23, abs(v23)\n"
    "v_cvt_pk_bf16_f32 v32, v16, v17\n"
    "v_cvt_pk_bf16_f32 v33, v18, v19\n"
    "v_cvt_pk_bf16_f32 v34, v20, v21\n"
    "v_cvt_pk_bf16_f32 v35, v22, v23\n"
    "v_pk_max_i16 v32, v32, 0\n"
    "v_pk_max_i16 v33, v33, 0\n"
    "v_pk_max_i16 v34, v34, 0\n"
    "v_pk_max_i16 v35, v35, 0\n"
    "buffer_store_dwordx4 v[32:35], v52, s[44:47], s59 offen offset:0 sc1\n"
    "v_mul_f32 v24, v24, abs(v24)\n"
    "v_mul_f32 v25, v25, abs(v25)\n"
    "v_mul_f32 v26, v26, abs(v26)\n"
    "v_mul_f32 v27, v27, abs(v27)\n"
    "v_mul_f32 v28, v28, abs(v28)\n"
    "v_mul_f32 v29, v29, abs(v29)\n"
    "v_mul_f32 v30, v30, abs(v30)\n"
    "v_mul_f32 v31, v31, abs(v31)\n"
    "v_cvt_pk_bf16_f32 v36, v24, v25\n"
    "v_cvt_pk_bf16_f32 v37, v26, v27\n"
    "v_cvt_pk_bf16_f32 v38, v28, v29\n"
    "v_cvt_pk_bf16_f32 v39, v30, v31\n"
    "v_pk_max_i16 v36, v36, 0\n"
    "v_pk_max_i16 v37, v37, 0\n"
    "v_pk_max_i16 v38, v38, 0\n"
    "v_pk_max_i16 v39, v39, 0\n"
    "buffer_store_dwordx4 v[36:39], v52, s[44:47], s59 offen offset:1024 sc1\n"
    "v_lshlrev_b32 v24, 16, v10\n"
    "v_and_b32 v25, 0xffff0000, v10\n"
    "v_lshlrev_b32 v26, 16, v11\n"
    "v_and_b32 v27, 0xffff0000, v11\n"
    ".Lpad5_%=:\n"
    "s_cbranch_execz .Lhopret_%=\n"
    "v_lshlrev_b32 v28, 16, v14\n"
    "v_and_b32 v29, 0xffff0000, v14\n"
    "v_lshlrev_b32 v30, 16, v15\n"
    "v_and_b32 v31, 0xffff0000, v15\n"
    "s_mov_b32 s58, 114688\n"
    "v_bfe_u32 v20, v0, 4, 1\n"
    "v_lshlrev_b32 v20, 10, v20\n"
    "v_lshrrev_b32 v21, 5, v0\n"
    "v_lshl_add_u32 v20, v21, 9, v20\n"
    "v_and_b32 v21, 0xf0, v1\n"
    "v_add3_u32 v20, v20, v21, s58\n"
    "v_bfe_u32 v18, v0, 2, 2\n"
    "v_lshlrev_b32 v18, 10, v18\n"
    "v_bfe_u32 v19, v0, 1, 1\n"
    "v_lshl_add_u32 v18, v19, 9, v18\n"
    "v_and_b32 v19, 0x30, v0\n"
    "v_add3_u32 v18, v19, v18, s27\n"
    "v_and_b32 v19, 1, v0\n"
    "v_lshl_add_u32 v18, v19, 3, v18\n"
    "ds_read_b64 v[8:9], v18\n"
    "ds_read_b64 v[22:23], v18 offset:256\n"
    "s_setprio 2\n"
    "s_waitcnt vmcnt(10)\n"
    "v_cmp_ne_u64 vcc, s[56:57], v[48:49]\n"
    "s_cbranch_vccnz .Lpagain31_%=\n"
    ".Lpok30_%=:\n"
    "s_waitcnt vmcnt(2)\n"
    "s_waitcnt lgkmcnt(0)\n"
    "s_barrier\n"
    "; ---- output projection of tokens 32..63 from the (speculatively filled) LDS image\n"
    "ds_read_b128 v[32:35], v20 offset:0\n"
    "ds_read_b128 v[36:39], v20 offset:256\n"
    "ds_read_b128 v[40:43], v20 offset:2048\n"
    "ds_read_b128 v[44:47], v20 offset:2304\n"
    "ds_read_b128 v[48:51], v20 offset:4096\n"
    "ds_read_b128 v[52:55], v20 offset:4352\n"
    "s_waitcnt lgkmcnt(5)\n"
    "v_mfma_f32_16x16x32_bf16 v[24:27], v[64:67], v[32:35], v[24:27]\n"
    "ds_read_b128 v[32:35], v20 offset:6144\n"
    "s_waitcnt lgkmcnt(5)\n"
    "v_mfma_f32_16x16x32_bf16 v[28:31], v[64:67], v[36:39], v[28:31]\n"
    "ds_read_b128 v[36:39], v20 offset:6400\n"
    "s_waitcnt lgkmcnt(5)\n"
    "v_mfma_f32_16x16x32_bf16 v[24:27], v[68:71], v[40:43], v[24:27]\n"
    "ds_read_b128 v[40:43], v20 offset:8192\n"
    "s_waitcnt lgkmcnt(5)\n"
    "v_mfma_f32_16x16x32_bf16 v[28:31], v[68:71], v[44:47], v[28:31]\n"
    "ds_read_b128 v[44:47], v20 offset:8448\n"
    "s_waitcnt lgkmcnt(5)\n"
    "v_mfma_f32_16x16x32_bf16 v[24:27], v[72:75], v[48:51], v[24:27]\n"
    "ds_read_b128 v[48:51], v20 offset:10240\n"
    "s_waitcnt lgkmcnt(5)\n"
    "v_mfma_f32_16x16x32_bf16 v[28:31], v[72:75], v[52:55], v[28:31]\n"
    "ds_read_b128 v[52:55], v20 offset:10496\n"
    "s_waitcnt lgkmcnt(5)\n"
    "v_mfma_f32_16x16x32_bf16 v[24:27], v[76:79], v[32:35], v[24:27]\n"
    "ds_read_b128 v[32:35], v20 offset:12288\n"
    "s_waitcnt lgkmcnt(5)\n"
    "v_mfma_f32_16x16x32_bf16 v[28:31], v[76:79], v[36:39], v[28:31]\n"
    "ds_read_b128 v[36:39], v20 offset:12544\n"
    "s_waitcnt lgkmcnt(5)\n"
    "v_mfma_f32_16x16x32_bf16 v[24:27], v[80:83], v[40:43], v[24:27]\n"
    "ds_read_b128 v[40:43], v20 offset:14336\n"
    "s_waitcnt lgkmcnt(5)\n"
    "v_mfma_f32_16x16x32_bf16 v[28:31], v[80:83], v[44:47], v[28:31]\n"
    "ds_read_b128 v[44:47], v20 offset:14592\n"
    "s_waitcnt lgkmcnt(5)\n"
    "v_mfma_f32_16x16x32_bf16 v[24:27], v[84:87], v[48:51], v[24:27]\n"
    "ds_read_b128 v[48:51], v20 offset:16384\n"
    "s_waitcnt lgkmcnt(5)\n"
    "v_mfma_f32_16x16x32_bf16 v[28:31], v[84:87], v[52:55], v[28:31]\n"
    "ds_read_b128 v[52:55], v20 offset:16640\n"
    "s_waitcnt lgkmcnt(5)\n"
    "v_mfma_f32_16x16x32_bf16 v[24:27], v[88:91], v[32:35], v[24:27]\n"
    "ds_read_b128 v[32:35], v20 offset:18432\n"
    "s_waitcnt lgkmcnt(5)\n"
    "v_mfma_f32_16x16x32_bf16 v[28:31], v[88:91], v[36:39], v[28:31]\n"
    "ds_read_b128 v[36:39], v20 offset:18688\n"
    "s_waitcnt lgkmcnt(5)\n"
    "v_mfma_f32_16x16x32_bf16 v[24:27], v[92:95], v[40:43], v[24:27]\n"
    "ds_read_b128 v[40:43], v20 offset:20480\n"
    "s_waitcnt lgkmcnt(5)\n"
    "v_mfma_f32_16x16x32_bf16 v[28:31], v[92:95], v[44:47], v[28:31]\n"
    "ds_read_b128 v[44:47], v20 offset:20736\n"
    "s_waitcnt lgkmcnt(5)\n"
    "v_mfma_f32_16x16x32_bf16 v[24:27], v[96:99], v[48:51], v[24:27]\n"
    "ds_read_b128 v[48:51], v20 offset:22528\n"
    "s_waitcnt lgkmcnt(5)\n"
    "v_mfma_f32_16x16x32_bf16 v[28:31], v[96:99], v[52:55], v[28:31]\n"
    "ds_read_b128 v[52:55], v20 offset:22784\n"
    "s_waitcnt lgkmcnt(5)\n"
    "v_mfma_f32_16x16x32_bf16 v[24:27], v[100:103], v[32:35], v[24:27]\n"
    "ds_read_b128 v[32:35], v20 offset:24576\n"
    "s_waitcnt lgkmcnt(5)\n"
    "v_mfma_f32_16x16x32_bf16 v[28:31], v[100:103], v[36:39], v[28:31]\n"
    "ds_read_b128 v[36:39], v20 offset:24832\n"
    "s_waitcnt lgkmcnt(5)\n"
    "v_mfma_f32_16x16x32_bf16 v[24:27], v[104:107], v[40:43], v[24:27]\n"
    "ds_read_b128 v[40:43], v20 offset:26624\n"
    "s_waitcnt lgkmcnt(5)\n"
    "v_mfma_f32_16x16x32_bf16 v[28:31], v[104:107], v[44:47], v[28:31]\n"
    "ds_read_b128 v[44:47], v20 offset:26880\n"
    "s_waitcnt lgkmcnt(5)\n"
    "v_mfma_f32_16x16x32_bf16 v[24:27], v[108:111], v[48:51], v[24:27]\n"
    "ds_read_b128 v[48:51], v20 offset:28672\n"
    "s_waitcnt lgkmcnt(5)\n"
    "v_mfma_f32_16x16x32_bf16 v[28:31], v[108:111], v[52:55], v[28:31]\n"
    "ds_read_b128 v[52:55], v20 offset:28928\n"
    "s_waitcnt lgkmcnt(5)\n"
    "v_mfma_f32_16x16x32_bf16 v[24:27], v[112:115], v[32:35], v[24:27]\n"
    "ds_read_b128 v[32:35], v20 offset:30720\n"
    "s_waitcnt lgkmcnt(5)\n"
    "v_mfma_f32_16x16x32_bf16 v[28:31], v[112:115], v[36:39], v[28:31]\n"
    "ds_read_b128 v[36:39], v20 offset:30976\n"
    "s_waitcnt lgkmcnt(5)\n"
    "v_mfma_f32_16x16x32_bf16 v[24:27], v[116:119], v[40:43], v[24:27]\n"
    "s_waitcnt lgkmcnt(4)\n"
    "v_mfma_f32_16x16x32_bf16 v[28:31], v[116:119], v[44:47], v[28:31]\n"
    "s_waitcnt lgkmcnt(3)\n"
    "v_mfma_f32_16x16x32_bf16 v[24:27], v[120:123], v[48:51], v[24:27]\n"
    ".Lhop1_%=:\n"
    ".Lpad6_%=:\n"
    "s_cbranch_execz .Lpad7_%=\n"
    "s_waitcnt lgkmcnt(2)\n"
    "v_mfma_f32_16x16x32_bf16 v[28:31], v[120:123], v[52:55], v[28:31]\n"
    "s_waitcnt lgkmcnt(1)\n"
    "v_mfma_f32_16x16x32_bf16 v[24:27], v[124:127], v[32:35], v[24:27]\n"
    "s_waitcnt lgkmcnt(0)\n"
    "v_mfma_f32_16x16x32_bf16 v[28:31], v[124:127], v[36:39], v[28:31]\n"
    "s_nop 7\n"
    "v_cmp_u_f32 vcc, v24, v28\n"
    "s_cbranch_vccnz .Loslow35_%=\n"
    ".Locont36_%=:\n"
    "v_cvt_pk_bf16_f32 v32, v24, v25\n"
    "v_cvt_pk_bf16_f32 v33, v26, v27\n"
    "v_cvt_pk_bf16_f32 v34, v28, v29\n"
    "v_cvt_pk_bf16_f32 v35, v30, v31\n"
    "s_add_u32 s59, s20, 4227072\n"
    "v_permlane16_swap_b32 v32, v34\n"
    "v_permlane16_swap_b32 v33, v35\n"
    "buffer_store_dwordx4 v[32:35], v1, s[44:47], s59 offen sc1\n"
    "v_add_f32 v36, v24, v25\n"
    "v_mul_f32 v38, v24, v24\n"
    "v_add_f32 v37, v28, v29\n"
    "v_mul_f32 v39, v28, v28\n"
    "v_add_f32 v36, v36, v26\n"
    "v_add_f32 v37, v37, v30\n"
    "v_add_f32 v36, v36, v27\n"
    "v_add_f32 v37, v37, v31\n"
    "v_fmac_f32 v38, v25, v25\n"
    "v_fmac_f32 v39, v29, v29\n"
    "v_fmac_f32 v38, v26, v26\n"
    "v_fmac_f32 v39, v30, v30\n"
    "v_fmac_f32 v38, v27, v27\n"
    "v_fmac_f32 v39, v31, v31\n"
    "v_permlane16_swap_b32 v36, v37\n"
    "s_lshr_b32 s59, s20, 2\n"
    "v_permlane16_swap_b32 v38, v39\n"
    "v_add_f32 v36, v36, v37\n"
    "v_add_f32 v38, v38, v39\n"
    "s_add_u32 s59, s59, 532480\n"
    "v_lshlrev_b32 v37, 2, v0\n"
    "v_permlane32_swap_b32 v36, v38\n"
    "v_add_f32 v36, v36, v38\n"
    "buffer_store_dword v36, v37, s[44:47], s59 offen sc1\n"
    "s_add_u32 s59, s8, 512\n"
    "s_mov_b64 exec, 1\n"
    "buffer_store_dwordx2 v[2:3], off, s[44:47], s59 sc1\n"
    "s_mov_b64 exec, -1\n"
    "s_add_u32 s59, s10, 64\n"
    "s_waitcnt vmcnt(3)\n"
    "s_mov_b64 exec, 1\n"
    "buffer_store_dwordx2 v[2:3], off, s[44:47], s59 sc1\n"
    "s_mov_b64 exec, -1\n"
    "v_mov_b32 v14, v22\n"
    "v_mov_b32 v15, v23\n"
    "s_add_u32 s16, s11, 64\n"
    "s_waitcnt vmcnt(2)\n"
    "s_barrier\n"
    "s_sleep 6\n"
    "s_add_u32 s59, s21, 32\n"
    "s_mov_b64 exec, 1\n"
    "buffer_store_dwordx2 v[2:3], off, s[44:47], s59 sc1\n"
    "s_mov_b64 exec, -1\n"
    "v_and_b32 v10, 7, v0\n"
    "v_lshlrev_b32 v10, 7, v10\n"
    ".LretryA40_%=:\n"
    "s_lshl_b32 s58, s53, 10\n"
    "s_add_u32 s58, s58, 158976\n"
    "v_lshl_add_u32 v54, v0, 3, s58\n"
    "buffer_load_dwordx2 v[48:49], v10, s[44:47], s16 offen sc1\n"
    "s_setprio 2\n"
    "s_barrier\n"
    "v_add_u32 v55, 114688, v1\n"
    "ds_read_b128 v[32:35], v55 offset:0\n"
    "ds_read_b128 v[36:39], v55 offset:1024\n"
    "ds_read_b128 v[40:43], v55 offset:2048\n"
    "s_waitcnt lgkmcnt(2)\n"
    "v_mfma_f32_32x32x16_bf16 v[16:31], v[128:131], v[32:35], 0\n"
    "ds_read_b128 v[44:47], v55 offset:3072\n"
    "s_waitcnt lgkmcnt(2)\n"
    "v_mfma_f32_32x32x16_bf16 v[16:31], v[132:135], v[36:39], v[16:31]\n"
    "ds_read_b128 v[32:35], v55 offset:4096\n"
    "s_add_u32 s60, s34, 0\n"
    "buffer_load_dwordx4 v[64:67], v1, s[44:47], s60 offen offset:2048 sc1\n"
    "s_waitcnt lgkmcnt(2)\n"
    "v_mfma_f32_32x32x16_bf16 v[16:31], v[136:139], v[40:43], v[16:31]\n"
    "ds_read_b128 v[36:39], v55 offset:5120\n"
    "s_waitcnt lgkmcnt(2)\n"
    "v_mfma_f32_32x32x16_bf16 v[16:31], v[140:143], v[44:47], v[16:31]\n"
    "ds_read_b128 v[40:43], v55 offset:6144\n"
    "buffer_load_dwordx4 v[68:71], v1, s[44:47], s60 offen offset:3072 sc1\n"
    "s_waitcnt lgkmcnt(2)\n"
    "v_mfma_f32_32x32x16_bf16 v[16:31], v[144:147], v[32:35], v[16:31]\n"
    "ds_read_b128 v[44:47], v55 offset:7168\n"
    "s_waitcnt lgkmcnt(2)\n"
    "v_mfma_f32_32x32x16_bf16 v[16:31], v[148:151], v[36:39], v[16:31]\n"
    "ds_read_b128 v[32:35], v55 offset:8192\n"
    "s_add_u32 s60, s34, 4096\n"
    "buffer_load_dwordx4 v[72:75], v1, s[44:47], s60 offen offset:0 sc1\n"
    "s_waitcnt lgkmcnt(2)\n"
    "v_mfma_f32_32x32x16_bf16 v[16:31], v[152:155], v[40:43], v[16:31]\n"
    "ds_read_b128 v[36:39], v55 offset:9216\n"
    "s_waitcnt lgkmcnt(2)\n"
    "v_mfma_f32_32x32x16_bf16 v[16:31], v[156:159], v[44:47], v[16:31]\n"
    "ds_read_b128 v[40:43], v55 offset:10240\n"
    "buffer_load_dwordx4 v[76:79], v1, s[44:47], s60 offen offset:1024 sc1\n"
    "s_waitcnt lgkmcnt(2)\n"
    "v_mfma_f32_32x32x16_bf16 v[16:31], v[160:163], v[32:35], v[16:31]\n"
    "ds_read_b128 v[44:47], v55 offset:11264\n"
    "s_waitcnt lgkmcnt(2)\n"
    "v_mfma_f32_32x32x16_bf16 v[16:31], v[164:167], v[36:39], v[16:31]\n"
    "ds_read_b128 v[32:35], v55 offset:12288\n"
    "buffer_load_dwordx4 v[80:83], v1, s[44:47], s60 offen offset:2048 sc1\n"
    "s_waitcnt lgkmcnt(2)\n"
    "v_mfma_f32_32x32x16_bf16 v[16:31], v[168:171], v[40:43], v[16:31]\n"
    "ds_read_b128 v[36:39], v55 offset:13312\n"
    "s_waitcnt lgkmcnt(2)\n"
    "v_mfma_f32_32x32x16_bf16 v[16:31], v[172:175], v[44:47], v[16:31]\n"
    "ds_read_b128 v[40:43], v55 offset:14336\n"
    "buffer_load_dwordx4 v[84:87], v1, s[44:47], s60 offen offset:3072 sc1\n"
    "s_waitcnt lgkmcnt(2)\n"
    "v_mfma_f32_32x32x16_bf16 v[16:31], v[176:179], v[32:35], v[16:31]\n"
    ".Lpad7_%=:\n"
    "s_cbranch_execz .Lpad8_%=\n"
    "ds_read_b128 v[44:47], v55 offset:15360\n"
    "s_barrier\n"
    "s_waitcnt vmcnt(6)\n"
    "v_cmp_ne_u64 vcc, s[56:57], v[48:49]\n"
    "s_cbranch_vccnz .Lpagain42_%=\n"
    ".Lpok41_%=:\n"
    "s_waitcnt lgkmcnt(2)\n"
    "v_mfma_f32_32x32x16_bf16 v[16:31], v[180:183], v[36:39], v[16:31]\n"
    "ds_read_b128 v[32:35], v55 offset:16384\n"
    "s_add_u32 s60, s34, 8192\n"
    "buffer_load_dwordx4 v[88:91], v1, s[44:47], s60 offen offset:0 sc1\n"
    "s_waitcnt lgkmcnt(2)\n"
    "v_mfma_f32_32x32x16_bf16 v[16:31], v[184:187], v[40:43], v[16:31]\n"
    "ds_read_b128 v[36:39], v55 offset:17408\n"
    "s_waitcnt lgkmcnt(2)\n"
    "v_mfma_f32_32x32x16_bf16 v[16:31], v[188:191], v[44:47], v[16:31]\n"
    "ds_read_b128 v[40:43], v55 offset:18432\n"
    "buffer_load_dwordx4 v[92:95], v1, s[44:47], s60 offen offset:1024 sc1\n"
    "s_waitcnt lgkmcnt(2)\n"
    "v_mfma_f32_32x32x16_bf16 v[16:31], v[192:195], v[32:35], v[16:31]\n"
    "ds_read_b128 v[44:47], v55 offset:19456\n"
    "s_waitcnt lgkmcnt(2)\n"
    "v_mfma_f32_32x32x16_bf16 v[16:31], v[196:199], v[36:39], v[16:31]\n"
    "ds_read_b128 v[32:35], v55 offset:20480\n"
    "buffer_load_dwordx4 v[96:99], v1, s[44:47], s60 offen offset:2048 sc1\n"
    "s_waitcnt lgkmcnt(2)\n"
    "v_mfma_f32_32x32x16_bf16 v[16:31], v[200:203], v[40:43], v[16:31]\n"
    "ds_read_b128 v[36:39], v55 offset:21504\n"
    "s_waitcnt lgkmcnt(2)\n"
    "v_mfma_f32_32x32x16_bf16 v[16:31], v[204:207], v[44:47], v[16:31]\n"
    "ds_read_b128 v[40:43], v55 offset:22528\n"
    "buffer_load_dwordx4 v[100:103], v1, s[44:47], s60 offen offset:3072 sc1\n"
    "s_waitcnt lgkmcnt(2)\n"
    "v_mfma_f32_32x32x16_bf16 v[16:31], v[208:211], v[32:35], v[16:31]\n"
    "ds_read_b128 v[44:47], v55 offset:23552\n"
    "s_waitcnt lgkmcnt(2)\n"
    "v_mfma_f32_32x32x16_bf16 v[16:31], v[212:215], v[36:39], v[16:31]\n"
    "ds_read_b128 v[32:35], v55 offset:24576\n"
    "s_add_u32 s60, s34, 12288\n"
    "buffer_load_dwordx4 v[104:107], v1, s[44:47], s60 offen offset:0 sc1\n"
    "s_waitcnt lgkmcnt(2)\n"
    "v_mfma_f32_32x32x16_bf16 v[16:31], v[216:219], v[40:43], v[16:31]\n"
    "ds_read_b128 v[36:39], v55 offset:25600\n"
    "s_waitcnt lgkmcnt(2)\n"
    "v_mfma_f32_32x32x16_bf16 v[16:31], v[220:223], v[44:47], v[16:31]\n"
    "ds_read_b128 v[40:43], v55 offset:26624\n"
    "buffer_load_dwordx4 v[108:111], v1, s[44:47], s60 offen offset:1024 sc1\n"
    "s_waitcnt lgkmcnt(2)\n"
    "v_mfma_f32_32x32x16_bf16 v[16:31], v[224:227], v[32:35], v[16:31]\n"
    "ds_read_b128 v[44:47], v55 offset:27648\n"
    "s_waitcnt lgkmcnt(2)\n"
    "v_mfma_f32_32x32x16_bf16 v[16:31], v[228:231], v[36:39], v[16:31]\n"
    "ds_read_b128 v[32:35], v55 offset:28672\n"
    "buffer_load_dwordx4 v[112:115], v1, s[44:47], s60 offen offset:2048 sc1\n"
    "s_waitcnt lgkmcnt(2)\n"
    "v_mfma_f32_32x32x16_bf16 v[16:31], v[232:235], v[40:43], v[16:31]\n"
    "ds_read_b128 v[36:39], v55 offset:29696\n"
    "s_waitcnt lgkmcnt(2)\n"
    "v_mfma_f32_32x32x16_bf16 v[16:31], v[236:239], v[44:47], v[16:31]\n"
    "ds_read_b128 v[40:43], v55 offset:30720\n"
    "buffer_load_dwordx4 v[116:119], v1, s[44:47], s60 offen offset:3072 sc1\n"
    "s_waitcnt lgkmcnt(2)\n"
    "v_mfma_f32_32x32x16_bf16 v[16:31], v[240:243], v[32:35], v[16:31]\n"
    "ds_read_b128 v[44:47], v55 offset:31744\n"
    "s_waitcnt lgkmcnt(2)\n"
    "v_mfma_f32_32x32x16_bf16 v[16:31], v[244:247], v[36:39], v[16:31]\n"
    "s_add_u32 s60, s34, 16384\n"
    "buffer_load_dwordx4 v[120:123], v1, s[44:47], s60 offen offset:0 sc1\n"
    "s_waitcnt lgkmcnt(1)\n"
    "v_mfma_f32_32x32x16_bf16 v[16:31], v[248:251], v[40:43], v[16:31]\n"
    "s_barrier\n"
    "ds_read2_b32 v[50:51], v54 offset0:0 offset1:0\n"
    "ds_read_b32 v13, v54 offset:4\n"
    "s_waitcnt lgkmcnt(2)\n"
    "v_mfma_f32_32x32x16_bf16 v[16:31], v[252:255], v[44:47], v[16:31]\n"
    "buffer_load_dwordx4 v[124:127], v1, s[44:47], s60 offen offset:1024 sc1\n"
    "s_waitcnt lgkmcnt(1)\n"
    "v_mfma_f32_32x32x8_bf16 v[16:31], v[4:5], v[50:51], v[16:31]\n"
    "s_setprio 0\n"
    "s_add_u32 s35, s26, 6291456\n"
    "buffer_load_dwordx4 v[128:131], v1, s[44:47], s35 offen offset:0 sc1\n"
    "buffer_load_dwordx4 v[132:135], v1, s[44:47], s35 offen offset:1024 sc1\n"
    "buffer_load_dwordx4 v[136:139], v1, s[44:47], s35 offen offset:2048 sc1\n"
    "buffer_load_dwordx4 v[140:143], v1, s[44:47], s35 offen offset:3072 sc1\n"
    "s_add_u32 s35, s26, 6295552\n"
    "buffer_load_dwordx4 v[144:147], v1, s[44:47], s35 offen offset:0 sc1\n"
    "buffer_load_dwordx4 v[148:151], v1, s[44:47], s35 offen offset:1024 sc1\n"
    "buffer_load_dwordx4 v[152:155], v1, s[44:47], s35 offen offset:2048 sc1\n"
    "buffer_load_dwordx4 v[156:159], v1, s[44:47], s35 offen offset:3072 sc1\n"
    "s_add_u32 s35, s26, 6299648\n"
    "buffer_load_dwordx4 v[160:163], v1, s[44:47], s35 offen offset:0 sc1\n"
    "buffer_load_dwordx4 v[164:167], v1, s[44:47], s35 offen offset:1024 sc1\n"
    "buffer_load_dwordx4 v[168:171], v1, s[44:47], s35 offen offset:2048 sc1\n"
    "buffer_load_dwordx4 v[172:175], v1, s[44:47], s35 offen offset:3072 sc1\n"
    "s_add_u32 s35, s26, 6303744\n"
    "buffer_load_dwordx4 v[176:179], v1, s[44:47], s35 offen offset:0 sc1\n"
    "buffer_load_dwordx4 v[180:183], v1, s[44:47], s35 offen offset:1024 sc1\n"
    "v_cmp_u_f32 vcc, v16, v16\n"
    "s_cbranch_vccnz .Lhslow46_%=\n"
    ".Lhcont47_%=:\n"
    "s_add_u32 s59, s24, 6406144\n"
    "v_or_b32 v52, s63, v1\n"
    "v_mul_f32 v16, v16, abs(v16)\n"
    "v_mul_f32 v17, v17, abs(v17)\n"
    "v_mul_f32 v18, v18, abs(v18)\n"
    "v_mul_f32 v19, v19, abs(v19)\n"
    "v_mul_f32 v20, v20, abs(v20)\n"
    "v_mul_f32 v21, v21, abs(v21)\n"
    "v_mul_f32 v22, v22, abs(v22)\n"
    "v_mul_f32 v23, v23, abs(v23)\n"
    "v_cvt_pk_bf16_f32 v32, v16, v17\n"
    "v_cvt_pk_bf16_f32 v33, v18, v19\n"
    "v_cvt_pk_bf16_f32 v34, v20, v21\n"
    "v_cvt_pk_bf16_f32 v35, v22, v23\n"
    "v_pk_max_i16 v32, v32, 0\n"
    "v_pk_max_i16 v33, v33, 0\n"
    "v_pk_max_i16 v34, v34, 0\n"
    "v_pk_max_i16 v35, v35, 0\n"
    "buffer_store_dwordx4 v[32:35], v52, s[44:47], s59 offen offset:0 sc1\n"
    "v_mul_f32 v24, v24, abs(v24)\n"
    ".Lpad8_%=:\n"
    "s_cbranch_execz .Lpad9_%=\n"
    "v_mul_f32 v25, v25, abs(v25)\n"
    "v_mul_f32 v26, v26, abs(v26)\n"
    "v_mul_f32 v27, v27, abs(v27)\n"
    "v_mul_f32 v28, v28, abs(v28)\n"
    "v_mul_f32 v29, v29, abs(v29)\n"
    "v_mul_f32 v30, v30, abs(v30)\n"
    "v_mul_f32 v31, v31, abs(v31)\n"
    "v_cvt_pk_bf16_f32 v36, v24, v25\n"
    "v_cvt_pk_bf16_f32 v37, v26, v27\n"
    "v_cvt_pk_bf16_f32 v38, v28, v29\n"
    "v_cvt_pk_bf16_f32 v39, v30, v31\n"
    "v_pk_max_i16 v36, v36, 0\n"
    "v_pk_max_i16 v37, v37, 0\n"
    "v_pk_max_i16 v38, v38, 0\n"
    "v_pk_max_i16 v39, v39, 0\n"
    "buffer_store_dwordx4 v[36:39], v52, s[44:47], s59 offen offset:1024 sc1\n"
    "s_mov_b64 exec, 1\n"
    "buffer_store_dwordx2 v[2:3], off, s[44:47], s10 sc1\n"
    "s_mov_b64 exec, -1\n"
    "v_add_u32 v251, s33, v1\n"
    "ds_read_b128 v[184:187], v251 offset:0\n"
    "ds_read_b128 v[188:191], v251 offset:1024\n"
    "ds_read_b128 v[192:195], v251 offset:2048\n"
    "ds_read_b128 v[196:199], v251 offset:3072\n"
    "ds_read_b128 v[200:203], v251 offset:4096\n"
    "ds_read_b128 v[204:207], v251 offset:5120\n"
    "ds_read_b128 v[208:211], v251 offset:6144\n"
    "ds_read_b128 v[212:215], v251 offset:7168\n"
    "ds_read_b128 v[216:219], v251 offset:8192\n"
    "ds_read_b128 v[220:223], v251 offset:9216\n"
    "s_waitcnt vmcnt(16)\n"
    "v_mfma_f32_32x32x16_bf16 v[16:31], v[56:59], v[128:131], 0\n"
    "v_bfe_u32 v251, v0, 2, 2\n"
    "v_lshlrev_b32 v251, 10, v251\n"
    "v_bfe_u32 v252, v0, 1, 1\n"
    "v_lshl_add_u32 v251, v252, 9, v251\n"
    "v_mfma_f32_32x32x16_bf16 v[32:47], v[60:63], v[128:131], 0\n"
    "v_and_b32 v252, 0x30, v0\n"
    "v_add3_u32 v251, v252, v251, s27\n"
    "v_and_b32 v252, 1, v0\n"
    "v_lshl_add_u32 v251, v252, 3, v251\n"
    "s_waitcnt vmcnt(7) lgkmcnt(9)\n"
    "v_mfma_f32_32x32x16_bf16 v[16:31], v[184:187], v[164:167], v[16:31]\n"
    "v_add_u32 v252, 114688, v251\n"
    "ds_read_b64 v[10:11], v252\n"
    "v_lshrrev_b32 v251, 5, v0\n"
    "v_xor_b32 v251, v251, v0\n"
    "s_waitcnt lgkmcnt(9)\n"
    "v_mfma_f32_32x32x16_bf16 v[32:47], v[188:191], v[164:167], v[32:47]\n"
    "v_and_b32 v251, 7, v251\n"
    "v_and_b32 v252, 31, v0\n"
    "v_lshl_add_u32 v252, v252, 8, s33\n"
    "v_lshl_add_u32 v224, v251, 4, v252\n"
    "s_waitcnt vmcnt(6) lgkmcnt(8)\n"
    "v_mfma_f32_32x32x16_bf16 v[16:31], v[192:195], v[168:171], v[16:31]\n"
    "v_xor_b32 v225, 32, v224\n"
    "v_xor_b32 v226, 64, v224\n"
    "v_xor_b32 v227, 0x60, v224\n"
    "v_lshrrev_b32 v251, 4, v0\n"
    "s_waitcnt lgkmcnt(7)\n"
    "v_mfma_f32_32x32x16_bf16 v[32:47], v[196:199], v[168:171], v[32:47]\n"
    "v_lshl_add_u32 v252, s49, 2, v251\n"
    "v_lshlrev_b32 v253, 2, v252\n"
    "ds_bpermute_b32 v13, v253, v13\n"
    "ds_bpermute_b32 v4, v253, v12\n"
    "s_waitcnt vmcnt(5) lgkmcnt(8)\n"
    "v_mfma_f32_32x32x16_bf16 v[16:31], v[200:203], v[172:175], v[16:31]\n"
    "ds_bpermute_b32 v5, v253, v12 offset:64\n"
    "v_and_b32 v253, 7, v252\n"
    "v_xor_b32 v253, v253, v0\n"
    "v_and_b32 v253, 15, v253\n"
    "s_waitcnt lgkmcnt(8)\n"
    "v_mfma_f32_32x32x16_bf16 v[32:47], v[204:207], v[172:175], v[32:47]\n"
    "v_lshlrev_b32 v253, 4, v253\n"
    "v_lshl_add_u32 v230, v252, 8, v253\n"
    "v_add_u32 v231, 114688, v230\n"
    "v_and_b32 v253, 15, v0\n"
    "s_waitcnt vmcnt(4) lgkmcnt(7)\n"
    "v_mfma_f32_32x32x16_bf16 v[16:31], v[208:211], v[176:179], v[16:31]\n"
    "v_lshlrev_b32 v253, 3, v253\n"
    "v_lshl_add_u32 v250, v251, 10, v253\n"
    "v_and_b32 v249, 7, v0\n"
    "v_lshlrev_b32 v249, 7, v249\n"
    "s_waitcnt lgkmcnt(6)\n"
    "v_mfma_f32_32x32x16_bf16 v[32:47], v[212:215], v[176:179], v[32:47]\n"
    "v_add_u32 v228, 32768, v230\n"
    "v_add_u32 v229, 73728, v230\n"
    "v_lshlrev_b32 v251, 16, v6\n"
    "v_and_b32 v252, 0xffff0000, v6\n"
    "s_waitcnt vmcnt(3) lgkmcnt(5)\n"
    "v_mfma_f32_32x32x16_bf16 v[16:31], v[216:219], v[180:183], v[16:31]\n"
    "v_lshlrev_b32 v253, 16, v7\n"
    "v_and_b32 v254, 0xffff0000, v7\n"
    "s_waitcnt lgkmcnt(3)\n"
    "v_lshlrev_b32 v240, 16, v10\n"
    "v_and_b32 v241, 0xffff0000, v10\n"
    "v_mfma_f32_32x32x16_bf16 v[32:47], v[220:223], v[180:183], v[32:47]\n"
    "v_lshlrev_b32 v242, 16, v11\n"
    "v_and_b32 v243, 0xffff0000, v11\n"
    "v_add_f32 v240, v240, v251\n"
    "v_add_f32 v241, v241, v252\n"
    "v_mfma_f32_32x32x16_bf16 v[16:31], v[64:67], v[132:135], v[16:31]\n"
    "v_add_f32 v242, v242, v253\n"
    "v_add_f32 v243, v243, v254\n"
    "v_lshlrev_b32 v232, 16, v8\n"
    "v_and_b32 v233, 0xffff0000, v8\n"
    "v_mfma_f32_32x32x16_bf16 v[32:47], v[68:71], v[132:135], v[32:47]\n"
    "v_lshlrev_b32 v234, 16, v9\n"
    "v_and_b32 v235, 0xffff0000, v9\n"
    "v_add_f32 v232, v232, v251\n"
    "v_add_f32 v233, v233, v252\n"
    "v_mfma_f32_32x32x16_bf16 v[16:31], v[72:75], v[136:139], v[16:31]\n"
    "v_add_f32 v234, v234, v253\n"
    "v_add_f32 v235, v235, v254\n"
    "v_lshlrev_b32 v236, 16, v14\n"
    "v_and_b32 v237, 0xffff0000, v14\n"
    "v_mfma_f32_32x32x16_bf16 v[32:47], v[76:79], v[136:139], v[32:47]\n"
    "v_lshlrev_b32 v238, 16, v15\n"
    ".Lpad9_%=:\n"
    "s_cbranch_execz .Lpad10_%=\n"
    "v_and_b32 v239, 0xffff0000, v15\n"
    "v_add_f32 v236, v236, v251\n"
    "v_add_f32 v237, v237, v252\n"
    "v_mfma_f32_32x32x16_bf16 v[16:31], v[80:83], v[140:143], v[16:31]\n"
    "v_add_f32 v238, v238, v253\n"
    "v_add_f32 v239, v239, v254\n"
    "v_mfma_f32_32x32x16_bf16 v[32:47], v[84:87], v[140:143], v[32:47]\n"
    "v_mfma_f32_32x32x16_bf16 v[16:31], v[88:91], v[144:147], v[16:31]\n"
    "v_mfma_f32_32x32x16_bf16 v[32:47], v[92:95], v[144:147], v[32:47]\n"
    "v_mfma_f32_32x32x16_bf16 v[16:31], v[96:99], v[148:151], v[16:31]\n"
    "v_mfma_f32_32x32x16_bf16 v[32:47], v[100:103], v[148:151], v[32:47]\n"
    "v_mfma_f32_32x32x16_bf16 v[16:31], v[104:107], v[152:155], v[16:31]\n"
    "v_mfma_f32_32x32x16_bf16 v[32:47], v[108:111], v[152:155], v[32:47]\n"
    "v_mfma_f32_32x32x16_bf16 v[16:31], v[112:115], v[156:159], v[16:31]\n"
    "v_mfma_f32_32x32x16_bf16 v[32:47], v[116:119], v[156:159], v[32:47]\n"
    "v_mfma_f32_32x32x16_bf16 v[16:31], v[120:123], v[160:163], v[16:31]\n"
    "v_mfma_f32_32x32x16_bf16 v[32:47], v[124:127], v[160:163], v[32:47]\n"
    "s_setprio 2\n"
    "s_waitcnt vmcnt(1)\n"
    "s_barrier\n"
    "s_nop 7\n"
    "ds_write_b128 v224, v[16:19] offset:0\n"
    "ds_write_b128 v225, v[20:23] offset:0\n"
    "buffer_load_dwordx2 v[244:245], v249, s[44:47], s11 offen sc1\n"
    "s_add_u32 s35, s26, 6406144\n"
    "buffer_load_dwordx4 v[128:131], v1, s[44:47], s35 offen offset:0 sc1\n"
    "buffer_load_dwordx4 v[132:135], v1, s[44:47], s35 offen offset:1024 sc1\n"
    "buffer_load_dwordx4 v[136:139], v1, s[44:47], s35 offen offset:2048 sc1\n"
    "buffer_load_dwordx4 v[140:143], v1, s[44:47], s35 offen offset:3072 sc1\n"
    "s_add_u32 s35, s26, 6410240\n"
    "buffer_load_dwordx4 v[144:147], v1, s[44:47], s35 offen offset:0 sc1\n"
    "buffer_load_dwordx4 v[148:151], v1, s[44:47], s35 offen offset:1024 sc1\n"
    "buffer_load_dwordx4 v[152:155], v1, s[44:47], s35 offen offset:2048 sc1\n"
    "ds_write_b128 v226, v[24:27] offset:0\n"
    "ds_write_b128 v227, v[28:31] offset:0\n"
    "ds_write_b128 v224, v[32:35] offset:128\n"
    "ds_write_b128 v225, v[36:39] offset:128\n"
    "ds_write_b128 v226, v[40:43] offset:128\n"
    "ds_write_b128 v227, v[44:47] offset:128\n"
    "buffer_load_dwordx4 v[156:159], v1, s[44:47], s35 offen offset:3072 sc1\n"
    "s_add_u32 s35, s26, 6414336\n"
    "buffer_load_dwordx4 v[160:163], v1, s[44:47], s35 offen offset:0 sc1\n"
    "buffer_load_dwordx4 v[164:167], v1, s[44:47], s35 offen offset:1024 sc1\n"
    "buffer_load_dwordx4 v[168:171], v1, s[44:47], s35 offen offset:2048 sc1\n"
    "buffer_load_dwordx4 v[172:175], v1, s[44:47], s35 offen offset:3072 sc1\n"
    "s_add_u32 s35, s26, 6418432\n"
    "buffer_load_dwordx4 v[176:179], v1, s[44:47], s35 offen offset:0 sc1\n"
    "buffer_load_dwordx4 v[180:183], v1, s[44:47], s35 offen offset:1024 sc1\n"
    "s_lshl_b32 s58, s53, 13\n"
    "s_add_u32 s58, s58, 0\n"
    "s_sub_u32 s58, s58, s33\n"
    "v_add_u32 v224, s58, v224\n"
    "v_add_u32 v225, s58, v225\n"
    "v_add_u32 v226, s58, v226\n"
    "v_add_u32 v227, s58, v227\n"
    "s_waitcnt vmcnt(14)\n"
    "v_cmp_ne_u64 vcc, s[56:57], v[244:245]\n"
    "s_cbranch_vccnz .Lpagain49_%=\n"
    ".Lpok48_%=:\n"
    "s_setprio 0\n"
    "s_waitcnt vmcnt(13)\n"
    "v_mfma_f32_32x32x16_bf16 v[16:31], v[56:59], v[128:131], 0\n"
    "v_mfma_f32_32x32x16_bf16 v[32:47], v[60:63], v[128:131], 0\n"
    "s_waitcnt vmcnt(12)\n"
    "v_mfma_f32_32x32x16_bf16 v[16:31], v[64:67], v[132:135], v[16:31]\n"
    "v_mfma_f32_32x32x16_bf16 v[32:47], v[68:71], v[132:135], v[32:47]\n"
    "s_waitcnt vmcnt(11)\n"
    "v_mfma_f32_32x32x16_bf16 v[16:31], v[72:75], v[136:139], v[16:31]\n"
    "v_mfma_f32_32x32x16_bf16 v[32:47], v[76:79], v[136:139], v[32:47]\n"
    "s_waitcnt vmcnt(10)\n"
    "v_mfma_f32_32x32x16_bf16 v[16:31], v[80:83], v[140:143], v[16:31]\n"
    "v_mfma_f32_32x32x16_bf16 v[32:47], v[84:87], v[140:143], v[32:47]\n"
    "s_waitcnt vmcnt(9)\n"
    "v_mfma_f32_32x32x16_bf16 v[16:31], v[88:91], v[144:147], v[16:31]\n"
    "v_mfma_f32_32x32x16_bf16 v[32:47], v[92:95], v[144:147], v[32:47]\n"
    "s_waitcnt vmcnt(8)\n"
    "v_mfma_f32_32x32x16_bf16 v[16:31], v[96:99], v[148:151], v[16:31]\n"
    "v_mfma_f32_32x32x16_bf16 v[32:47], v[100:103], v[148:151], v[32:47]\n"
    "s_waitcnt vmcnt(7)\n"
    "v_mfma_f32_32x32x16_bf16 v[16:31], v[104:107], v[152:155], v[16:31]\n"
    "v_mfma_f32_32x32x16_bf16 v[32:47], v[108:111], v[152:155], v[32:47]\n"
    "s_waitcnt vmcnt(6)\n"
    "v_mfma_f32_32x32x16_bf16 v[16:31], v[112:115], v[156:159], v[16:31]\n"
    "v_mfma_f32_32x32x16_bf16 v[32:47], v[116:119], v[156:159], v[32:47]\n"
    "s_waitcnt vmcnt(5)\n"
    "v_mfma_f32_32x32x16_bf16 v[16:31], v[120:123], v[160:163], v[16:31]\n"
    "v_mfma_f32_32x32x16_bf16 v[32:47], v[124:127], v[160:163], v[32:47]\n"
    "s_waitcnt vmcnt(4)\n"
    "v_mfma_f32_32x32x16_bf16 v[16:31], v[184:187], v[164:167], v[16:31]\n"
    "v_mfma_f32_32x32x16_bf16 v[32:47], v[188:191], v[164:167], v[32:47]\n"
    "s_waitcnt vmcnt(3)\n"
    "v_mfma_f32_32x32x16_bf16 v[16:31], v[192:195], v[168:171], v[16:31]\n"
    "v_mfma_f32_32x32x16_bf16 v[32:47], v[196:199], v[168:171], v[32:47]\n"
    "s_waitcnt vmcnt(2)\n"
    "v_mfma_f32_32x32x16_bf16 v[16:31], v[200:203], v[172:175], v[16:31]\n"
    "v_mfma_f32_32x32x16_bf16 v[32:47], v[204:207], v[172:175], v[32:47]\n"
    "s_waitcnt lgkmcnt(0)\n"
    "s_barrier\n"
    "s_waitcnt vmcnt(1)\n"
    "v_mfma_f32_32x32x16_bf16 v[16:31], v[208:211], v[176:179], v[16:31]\n"
    "v_mfma_f32_32x32x16_bf16 v[32:47], v[212:215], v[176:179], v[32:47]\n"
    "s_waitcnt vmcnt(0)\n"
    "v_mfma_f32_32x32x16_bf16 v[16:31], v[216:219], v[180:183], v[16:31]\n"
    "v_mfma_f32_32x32x16_bf16 v[32:47], v[220:223], v[180:183], v[32:47]\n"
    "s_add_u32 s59, s25, 64\n"
    "s_mov_b64 exec, 1\n"
    "buffer_store_dwordx2 v[2:3], off, s[44:47], s59 sc1\n"
    "s_mov_b64 exec, -1\n"
    "s_nop 6\n"
    "v_cmp_u_f32 vcc, v16, v16\n"
    "s_cbranch_vccnz .Lh1slow53_%=\n"
    ".Lh1cont54_%=:\n"
    "ds_write_b128 v224, v[16:19] offset:0\n"
    "ds_write_b128 v225, v[20:23] offset:0\n"
    "ds_write_b128 v226, v[24:27] offset:0\n"
    "ds_write_b128 v227, v[28:31] offset:0\n"
    "ds_write_b128 v224, v[32:35] offset:128\n"
    "ds_write_b128 v225, v[36:39] offset:128\n"
    "ds_write_b128 v226, v[40:43] offset:128\n"
    "ds_write_b128 v227, v[44:47] offset:128\n"
    ".Lpad10_%=:\n"
    "s_cbranch_execz .Lpad11_%=\n"
    "ds_read_b128 v[56:59], v228 offset:0\n"
    "ds_read_b128 v[60:63], v228 offset:10240\n"
    "ds_read_b128 v[64:67], v228 offset:20480\n"
    "ds_read_b128 v[68:71], v228 offset:30720\n"
    "ds_read_b128 v[72:75], v229 offset:0\n"
    "ds_read_b128 v[76:79], v229 offset:10240\n"
    "ds_read_b128 v[80:83], v229 offset:20480\n"
    "ds_read_b128 v[84:87], v229 offset:30720\n"
    "ds_read_b128 v[88:91], v228 offset:4096\n"
    "ds_read_b128 v[92:95], v228 offset:14336\n"
    "ds_read_b128 v[96:99], v228 offset:24576\n"
    "ds_read_b128 v[100:103], v228 offset:34816\n"
    "ds_read_b128 v[104:107], v229 offset:4096\n"
    "ds_read_b128 v[108:111], v229 offset:14336\n"
    "ds_read_b128 v[112:115], v229 offset:24576\n"
    "ds_read_b128 v[116:119], v229 offset:34816\n"
    "s_add_u32 s58, s32, 0\n"
    "s_waitcnt lgkmcnt(14)\n"
    "v_add_f32 v56, v56, v60\n"
    "v_add_f32 v57, v57, v61\n"
    "v_add_f32 v58, v58, v62\n"
    "v_add_f32 v59, v59, v63\n"
    "s_waitcnt lgkmcnt(13)\n"
    "v_add_f32 v56, v56, v64\n"
    "v_add_f32 v57, v57, v65\n"
    "v_add_f32 v58, v58, v66\n"
    "v_add_f32 v59, v59, v67\n"
    "s_waitcnt lgkmcnt(12)\n"
    "v_add_f32 v56, v56, v68\n"
    "v_add_f32 v57, v57, v69\n"
    "v_add_f32 v58, v58, v70\n"
    "v_add_f32 v59, v59, v71\n"
    "s_waitcnt lgkmcnt(11)\n"
    "v_add_f32 v56, v56, v72\n"
    "v_add_f32 v57, v57, v73\n"
    "v_add_f32 v58, v58, v74\n"
    "v_add_f32 v59, v59, v75\n"
    "s_waitcnt lgkmcnt(10)\n"
    "v_add_f32 v56, v56, v76\n"
    "v_add_f32 v57, v57, v77\n"
    "v_add_f32 v58, v58, v78\n"
    "v_add_f32 v59, v59, v79\n"
    "s_waitcnt lgkmcnt(9)\n"
    "v_add_f32 v56, v56, v80\n"
    "v_add_f32 v57, v57, v81\n"
    "v_add_f32 v58, v58, v82\n"
    "v_add_f32 v59, v59, v83\n"
    "s_waitcnt lgkmcnt(8)\n"
    "v_add_f32 v56, v56, v84\n"
    "v_add_f32 v57, v57, v85\n"
    "v_add_f32 v58, v58, v86\n"
    "v_add_f32 v59, v59, v87\n"
    "v_fmac_f32 v232, v56, v4\n"
    "v_fmac_f32 v233, v57, v4\n"
    "v_fmac_f32 v234, v58, v4\n"
    "v_fmac_f32 v235, v59, v4\n"
    "v_cvt_pk_bf16_f32 v232, v232, v233\n"
    "v_cvt_pk_bf16_f32 v233, v234, v235\n"
    "buffer_store_dwordx2 v[232:233], v250, s[40:43], s58 offen sc1\n"
    "s_add_u32 s58, s32, 16384\n"
    "s_waitcnt lgkmcnt(6)\n"
    "v_add_f32 v88, v88, v92\n"
    "v_add_f32 v89, v89, v93\n"
    "v_add_f32 v90, v90, v94\n"
    "v_add_f32 v91, v91, v95\n"
    "s_waitcnt lgkmcnt(5)\n"
    "v_add_f32 v88, v88, v96\n"
    "v_add_f32 v89, v89, v97\n"
    "v_add_f32 v90, v90, v98\n"
    "v_add_f32 v91, v91, v99\n"
    "s_waitcnt lgkmcnt(4)\n"
    "v_add_f32 v88, v88, v100\n"
    "v_add_f32 v89, v89, v101\n"
    "v_add_f32 v90, v90, v102\n"
    "v_add_f32 v91, v91, v103\n"
    "s_waitcnt lgkmcnt(3)\n"
    "v_add_f32 v88, v88, v104\n"
    "v_add_f32 v89, v89, v105\n"
    "v_add_f32 v90, v90, v106\n"
    "v_add_f32 v91, v91, v107\n"
    "s_waitcnt lgkmcnt(2)\n"
    "v_add_f32 v88, v88, v108\n"
    "v_add_f32 v89, v89, v109\n"
    "v_add_f32 v90, v90, v110\n"
    "v_add_f32 v91, v91, v111\n"
    "s_waitcnt lgkmcnt(1)\n"
    "v_add_f32 v88, v88, v112\n"
    "v_add_f32 v89, v89, v113\n"
    "v_add_f32 v90, v90, v114\n"
    "v_add_f32 v91, v91, v115\n"
    "s_waitcnt lgkmcnt(0)\n"
    "v_add_f32 v88, v88, v116\n"
    "v_add_f32 v89, v89, v117\n"
    "v_add_f32 v90, v90, v118\n"
    "v_add_f32 v91, v91, v119\n"
    "v_fmac_f32 v236, v88, v5\n"
    "v_fmac_f32 v237, v89, v5\n"
    "v_fmac_f32 v238, v90, v5\n"
    "v_fmac_f32 v239, v91, v5\n"
    "v_cvt_pk_bf16_f32 v236, v236, v237\n"
    "v_cvt_pk_bf16_f32 v237, v238, v239\n"
    "buffer_store_dwordx2 v[236:237], v250, s[40:43], s58 offen sc1\n"
    "s_barrier\n"
    "ds_read_b128 v[16:19], v230 offset:0\n"
    "ds_read_b128 v[20:23], v230 offset:8192\n"
    "ds_read_b128 v[24:27], v230 offset:16384\n"
    "ds_read_b128 v[28:31], v230 offset:24576\n"
    "ds_read_b128 v[32:35], v231 offset:0\n"
    "ds_read_b128 v[36:39], v231 offset:8192\n"
    "ds_read_b128 v[40:43], v231 offset:16384\n"
    "ds_read_b128 v[44:47], v231 offset:24576\n"
    "s_add_u32 s58, s32, 32768\n"
    "s_waitcnt lgkmcnt(6)\n"
    "v_add_f32 v16, v16, v20\n"
    "v_add_f32 v17, v17, v21\n"
    "v_add_f32 v18, v18, v22\n"
    "v_add_f32 v19, v19, v23\n"
    "s_waitcnt lgkmcnt(5)\n"
    ".Lpad11_%=:\n"
    "s_cbranch_execz .Lpad12_%=\n"
    "v_add_f32 v16, v16, v24\n"
    "v_add_f32 v17, v17, v25\n"
    "v_add_f32 v18, v18, v26\n"
    "v_add_f32 v19, v19, v27\n"
    "s_waitcnt lgkmcnt(4)\n"
    "v_add_f32 v16, v16, v28\n"
    "v_add_f32 v17, v17, v29\n"
    "v_add_f32 v18, v18, v30\n"
    "v_add_f32 v19, v19, v31\n"
    "s_waitcnt lgkmcnt(3)\n"
    "v_add_f32 v16, v16, v32\n"
    "v_add_f32 v17, v17, v33\n"
    "v_add_f32 v18, v18, v34\n"
    "v_add_f32 v19, v19, v35\n"
    "s_waitcnt lgkmcnt(2)\n"
    "v_add_f32 v16, v16, v36\n"
    "v_add_f32 v17, v17, v37\n"
    "v_add_f32 v18, v18, v38\n"
    "v_add_f32 v19, v19, v39\n"
    "s_waitcnt lgkmcnt(1)\n"
    "v_add_f32 v16, v16, v40\n"
    "v_add_f32 v17, v17, v41\n"
    "v_add_f32 v18, v18, v42\n"
    "v_add_f32 v19, v19, v43\n"
    "s_waitcnt lgkmcnt(0)\n"
    "v_add_f32 v16, v16, v44\n"
    "v_add_f32 v17, v17, v45\n"
    "v_add_f32 v18, v18, v46\n"
    "v_add_f32 v19, v19, v47\n"
    "v_fmac_f32 v240, v16, v13\n"
    "v_fmac_f32 v241, v17, v13\n"
    "v_fmac_f32 v242, v18, v13\n"
    "v_fmac_f32 v243, v19, v13\n"
    "v_cvt_pk_bf16_f32 v240, v240, v241\n"
    "v_cvt_pk_bf16_f32 v241, v242, v243\n"
    "buffer_store_dwordx2 v[240:241], v250, s[40:43], s58 offen sc1\n"
    "s_endpgm\n"
    ".LBrest58_%=:\n"
    "; ---- every workgroup has read the epoch word: advance it (workgroup 0, wave 4)\n"
    "buffer_store_dwordx2 v[2:3], v6, s[44:47], 0 offen sc1\n"
    "buffer_load_dwordx4 v[112:115], v109, s[16:19], s8 offen\n"
    "buffer_load_dwordx4 v[116:119], v109, s[16:19], s9 offen\n"
    "s_waitcnt vmcnt(3)\n"
    "s_barrier\n"
    "buffer_load_dwordx4 v[120:123], v109, s[16:19], s10 offen\n"
    "buffer_load_dwordx4 v[124:127], v109, s[16:19], s11 offen\n"
    "s_barrier\n"
    "s_mul_i32 s58, s55, 2560\n"
    "s_add_u32 s58, s58, 1048576\n"
    "s_lshl_b32 s8, s53, 7\n"
    "s_add_u32 s8, s8, s58\n"
    "s_lshl_b32 s10, s49, 7\n"
    "s_add_u32 s10, s10, s58\n"
    "s_add_u32 s10, s10, 1024\n"
    "s_add_u32 s58, s53, s98\n"
    "s_and_b32 s58, s58, 3\n"
    "s_mul_i32 s58, s58, 5120\n"
    "s_add_u32 s9, s58, s23\n"
    "s_mul_i32 s58, s99, 2560\n"
    "s_add_u32 s58, s58, s23\n"
    "s_add_u32 s11, s58, 1024\n"
    "s_mul_i32 s16, s51, 2560\n"
    "s_add_u32 s16, s16, 1050752\n"
    "s_sub_u32 s17, s21, 128\n"
    "; ---- hop over the later code with all lanes off (branches only): fills the instruction cache\n"
    "s_mov_b64 exec, 0\n"
    "s_cmp_eq_u32 s53, 0\n"
    "s_cbranch_scc1 .Lhop0_%=\n"
    "s_cmp_eq_u32 s53, 1\n"
    "s_cbranch_scc1 .Lhop1_%=\n"
    "s_cmp_eq_u32 s53, 2\n"
    "s_cbranch_scc1 .Lhop2_%=\n"
    "s_branch .Lhop3_%=\n"
    ".Lhopret_%=:\n"
    "s_mov_b64 exec, -1\n"
    "s_setprio 2\n"
    "s_waitcnt vmcnt(3)\n"
    "ds_write_b128 v108, v[112:115]\n"
    "ds_read_b64_tr_b16 v[80:81], v110\n"
    "ds_read_b64_tr_b16 v[84:85], v111\n"
    "s_waitcnt vmcnt(2)\n"
    "ds_write_b128 v108, v[116:119]\n"
    "ds_read_b64_tr_b16 v[82:83], v110\n"
    "ds_read_b64_tr_b16 v[86:87], v111\n"
    "s_waitcnt vmcnt(1)\n"
    "ds_write_b128 v108, v[120:123]\n"
    "ds_read_b64_tr_b16 v[88:89], v110\n"
    "ds_read_b64_tr_b16 v[92:93], v111\n"
    "s_waitcnt vmcnt(0)\n"
    "ds_write_b128 v108, v[124:127]\n"
    "ds_read_b64_tr_b16 v[90:91], v110\n"
    "ds_read_b64_tr_b16 v[94:95], v111\n"
    "s_waitcnt lgkmcnt(7)\n"
    "v_mfma_f32_32x32x16_bf16 v[112:127], v[20:23], v[80:83], 0\n"
    "s_waitcnt lgkmcnt(6)\n"
    "v_mfma_f32_32x32x16_bf16 v[112:127], v[20:23], v[84:87], v[112:127]\n"
    "s_waitcnt lgkmcnt(1)\n"
    "v_mfma_f32_32x32x16_bf16 v[112:127], v[20:23], v[88:91], v[112:127]\n"
    "s_waitcnt lgkmcnt(0)\n"
    "v_mfma_f32_32x32x16_bf16 v[112:127], v[20:23], v[92:95], v[112:127]\n"
    "s_setprio 2\n"
    "s_add_u32 s58, s12, s13\n"
    "s_add_u32 s59, s12, s14\n"
    "s_add_u32 s60, s58, s14\n"
    "buffer_store_dwordx4 v[80:83], v1, s[44:47], s12 offen sc1\n"
    "buffer_store_dwordx4 v[84:87], v1, s[44:47], s58 offen sc1\n"
    "buffer_store_dwordx4 v[88:91], v1, s[44:47], s59 offen sc1\n"
    "buffer_store_dwordx4 v[92:95], v1, s[44:47], s60 offen sc1\n"
    "s_nop 3\n"
    "buffer_store_dword v112, v107, s[44:47], 0 offen sc1\n"
    "s_add_u32 s58, s50, 5\n"
    "s_and_b32 s58, s58, 7\n"
    "s_lshl2_add_u32 s58, s58, s53\n"
    "s_lshl_b32 s58, s58, 12\n"
    "s_mul_i32 m0, s53, 4160\n"
    "s_add_u32 m0, m0, 83200\n"
    "s_add_u32 s58, s58, 327680\n"
    "buffer_load_dwordx4 v1, s[44:47], s58 offen sc1 lds\n"
    "s_add_u32 m0, m0, 16\n"
    "s_nop 0\n"
    "buffer_load_dwordx4 v1, s[44:47], s58 offen offset:1024 sc1 lds\n"
    "s_add_u32 m0, m0, 16\n"
    "s_nop 0\n"
    "buffer_load_dwordx4 v1, s[44:47], s58 offen offset:2048 sc1 lds\n"
    "s_add_u32 m0, m0, 16\n"
    "s_nop 0\n"
    "buffer_load_dwordx4 v1, s[44:47], s58 offen offset:3072 sc1 lds\n"
    "s_add_u32 s58, s50, 6\n"
    "s_and_b32 s58, s58, 7\n"
    "s_lshl2_add_u32 s58, s58, s53\n"
    "s_lshl_b32 s58, s58, 12\n"
    "s_mul_i32 m0, s53, 4160\n"
    "s_add_u32 m0, m0, 99840\n"
    "s_add_u32 s58, s58, 327680\n"
    "buffer_load_dwordx4 v1, s[44:47], s58 offen sc1 lds\n"
    "s_add_u32 m0, m0, 16\n"
    "s_nop 0\n"
    "buffer_load_dwordx4 v1, s[44:47], s58 offen offset:1024 sc1 lds\n"
    "s_add_u32 m0, m0, 16\n"
    "s_nop 0\n"
    "buffer_load_dwordx4 v1, s[44:47], s58 offen offset:2048 sc1 lds\n"
    "s_add_u32 m0, m0, 16\n"
    "s_nop 0\n"
    "buffer_load_dwordx4 v1, s[44:47], s58 offen offset:3072 sc1 lds\n"
    "s_waitcnt vmcnt(8)\n"
    "s_mov_b64 exec, 1\n"
    "buffer_store_dwordx2 v[2:3], off, s[44:47], s17 sc1\n"
    "s_mov_b64 exec, -1\n"
    "s_add_u32 s58, s50, 7\n"
    "s_and_b32 s58, s58, 7\n"
    "s_lshl2_add_u32 s58, s58, s53\n"
    "s_lshl_b32 s58, s58, 12\n"
    "s_mul_i32 m0, s53, 4160\n"
    "s_add_u32 m0, m0, 116480\n"
    "s_add_u32 s58, s58, 327680\n"
    "buffer_load_dwordx4 v1, s[44:47], s58 offen sc1 lds\n"
    "s_add_u32 m0, m0, 16\n"
    "s_nop 0\n"
    "buffer_load_dwordx4 v1, s[44:47], s58 offen offset:1024 sc1 lds\n"
    "s_add_u32 m0, m0, 16\n"
    "s_nop 0\n"
    "buffer_load_dwordx4 v1, s[44:47], s58 offen offset:2048 sc1 lds\n"
    "s_add_u32 m0, m0, 16\n"
    "s_nop 0\n"
    "buffer_load_dwordx4 v1, s[44:47], s58 offen offset:3072 sc1 lds\n"
    "s_waitcnt vmcnt(0)\n"
    "s_barrier\n"
    "; ---- poison the blocks that are read speculatively later, touch the lines that are polled later\n"
    "s_add_u32 s58, s20, 2129920\n"
    "s_add_u32 s59, s20, 4227072\n"
    "buffer_store_dwordx4 v[16:19], v1, s[44:47], s58 offen sc1\n"
    "buffer_store_dwordx4 v[16:19], v1, s[44:47], s59 offen sc1\n"
    "s_lshr_b32 s58, s20, 2\n"
    "s_add_u32 s58, s58, 532480\n"
    "buffer_store_dword v16, v25, s[44:47], s58 offen sc1\n"
    "s_add_u32 s60, s24, 6406144\n"
    "s_sub_u32 s61, s60, 8192\n"
    "buffer_store_dwordx4 v[16:19], v24, s[44:47], s60 offen sc1\n"
    "buffer_store_dwordx4 v[16:19], v24, s[44:47], s60 offen offset:1024 sc1\n"
    "buffer_store_dwordx4 v[16:19], v1, s[44:47], s61 offen sc1\n"
    "buffer_store_dwordx4 v[16:19], v1, s[44:47], s61 offen offset:1024 sc1\n"
    "ds_read_b128 v[8:11], v27\n"
    "v_mov_b32 v2, v7\n"
    "s_nop 2\n"
    "s_cmp_eq_u32 s53, 0\n"
    "s_cbranch_scc0 .Lnopoll86_%=\n"
    "s_mov_b32 s62, 0x20000\n"
    ".Lpoll87_%=:\n"
    "buffer_load_dwordx4 v[32:35], v26, s[44:47], s16 offen sc1\n"
    ".Lpchk88_%=:\n"
    "s_waitcnt vmcnt(0)\n"
    "v_cmp_ne_u64 vcc, s[56:57], v[32:33]\n"
    "v_cmp_ne_u64 s[64:65], s[56:57], v[34:35]\n"
    "s_nop 0\n"
    "s_or_b64 vcc, vcc, s[64:65]\n"
    "s_cbranch_scc0 .Lpdone89_%=\n"
    "s_sleep 16\n"
    "s_sub_u32 s62, s62, 1\n"
    "s_cbranch_scc0 .Lpoll87_%=\n"
    ".Lpdone89_%=:\n"
    "v_mov_b32 v32, 0x43424140\n"
    "ds_write_b32 v2, v32 offset:128\n"
    ".Lnopoll86_%=:\n"
    "s_waitcnt lgkmcnt(0)\n"
    "s_mov_b32 s62, 0x100000\n"
    "v_mov_b32 v33, 150464\n"
    "s_nop 2\n"
    ".Llw90_%=:\n"
    "ds_read_b32 v32, v33\n"
    "s_waitcnt lgkmcnt(0)\n"
    "v_cmp_ne_u32 vcc, 0x43424140, v32\n"
    "s_cbranch_vccz .Llwd91_%=\n"
    "s_sleep 2\n"
    "s_sub_u32 s62, s62, 1\n"
    "s_cbranch_scc0 .Llw90_%=\n"
    ".Llwd91_%=:\n"
    "v_and_b32 v40, 31, v0\n"
    "v_lshl_add_u32 v36, v40, 1, s63\n"
    "s_mul_i32 s59, s51, 7\n"
    "s_add_u32 s59, s59, s49\n"
    "s_lshl_b32 s58, s59, 6\n"
    "buffer_load_ushort v4, v36, s[28:31], s58 offen\n"
    "v_and_b32 v36, 15, v0\n"
    "v_lshlrev_b32 v36, 3, v36\n"
    "buffer_load_dwordx2 v[6:7], v36, s[36:39], s66 offen\n"
    "v_lshrrev_b32 v36, 5, v0\n"
    "v_lshl_add_u32 v36, v36, 9, s63\n"
    "v_lshl_add_u32 v40, v40, 2, v36\n"
    "s_lshl_b32 s58, s59, 10\n"
    "s_add_u32 s58, s58, 458752\n"
    "buffer_load_dword v36, v40, s[44:47], s58 offen offset:0 sc1\n"
    "buffer_load_dword v37, v40, s[44:47], s58 offen offset:128 sc1\n"
    "buffer_load_dword v38, v40, s[44:47], s58 offen offset:256 sc1\n"
    "buffer_load_dword v39, v40, s[44:47], s58 offen offset:384 sc1\n"
    "s_and_b32 s58, s53, 1\n"
    "s_mul_i32 s19, s58, 2080\n"
    "s_lshl_b32 s58, s58, 11\n"
    "s_sub_u32 s18, 1024, s58\n"
    "buffer_load_dwordx4 v[128:131], v28, s[44:47], s35 offen offset:0 sc1\n"
    "buffer_load_dwordx4 v[132:135], v28, s[44:47], s35 offen offset:1024 sc1\n"
    "buffer_load_dwordx4 v[136:139], v28, s[44:47], s35 offen offset:2048 sc1\n"
    "buffer_load_dwordx4 v[140:143], v28, s[44:47], s35 offen offset:3072 sc1\n"
    "s_add_u32 s35, s35, 4096\n"
    "buffer_load_dwordx4 v[144:147], v28, s[44:47], s35 offen offset:0 sc1\n"
    "buffer_load_dwordx4 v[148:151], v28, s[44:47], s35 offen offset:1024 sc1\n"
    "buffer_load_dwordx4 v[152:155], v28, s[44:47], s35 offen offset:2048 sc1\n"
    "buffer_load_dwordx4 v[156:159], v28, s[44:47], s35 offen offset:3072 sc1\n"
    "s_add_u32 s35, s35, 4096\n"
    "s_and_b32 s35, s35, 0x7fff\n"
    "buffer_load_dwordx4 v[160:163], v28, s[44:47], s35 offen offset:0 sc1\n"
    "buffer_load_dwordx4 v[164:167], v28, s[44:47], s35 offen offset:1024 sc1\n"
    "buffer_load_dwordx4 v[168:171], v28, s[44:47], s35 offen offset:2048 sc1\n"
    "buffer_load_dwordx4 v[172:175], v28, s[44:47], s35 offen offset:3072 sc1\n"
    "s_add_u32 s35, s35, 4096\n"
    "buffer_load_dwordx4 v[176:179], v28, s[44:47], s35 offen offset:0 sc1\n"
    "buffer_load_dwordx4 v[180:183], v28, s[44:47], s35 offen offset:1024 sc1\n"
    "buffer_load_dwordx4 v[184:187], v28, s[44:47], s35 offen offset:2048 sc1\n"
    "buffer_load_dwordx4 v[188:191], v28, s[44:47], s35 offen offset:3072 sc1\n"
    "s_add_u32 s35, s35, 4096\n"
    "s_and_b32 s35, s35, 0x7fff\n"
    "buffer_load_dwordx4 v[192:195], v28, s[44:47], s35 offen offset:0 sc1\n"
    "buffer_load_dwordx4 v[196:199], v28, s[44:47], s35 offen offset:1024 sc1\n"
    "buffer_load_dwordx4 v[200:203], v28, s[44:47], s35 offen offset:2048 sc1\n"
    "buffer_load_dwordx4 v[204:207], v28, s[44:47], s35 offen offset:3072 sc1\n"
    "s_add_u32 s35, s35, 4096\n"
    "buffer_load_dwordx4 v[208:211], v28, s[44:47], s35 offen offset:0 sc1\n"
    "buffer_load_dwordx4 v[212:215], v28, s[44:47], s35 offen offset:1024 sc1\n"
    "buffer_load_dwordx4 v[216:219], v28, s[44:47], s35 offen offset:2048 sc1\n"
    "buffer_load_dwordx4 v[220:223], v28, s[44:47], s35 offen offset:3072 sc1\n"
    "s_add_u32 s35, s35, 4096\n"
    "s_and_b32 s35, s35, 0x7fff\n"
    "buffer_load_dwordx4 v[224:227], v28, s[44:47], s35 offen offset:0 sc1\n"
    "buffer_load_dwordx4 v[228:231], v28, s[44:47], s35 offen offset:1024 sc1\n"
    "buffer_load_dwordx4 v[232:235], v28, s[44:47], s35 offen offset:2048 sc1\n"
    "buffer_load_dwordx4 v[236:239], v28, s[44:47], s35 offen offset:3072 sc1\n"
    "s_add_u32 s35, s35, 4096\n"
    "buffer_load_dwordx4 v[240:243], v28, s[44:47], s35 offen offset:0 sc1\n"
    "buffer_load_dwordx4 v[244:247], v28, s[44:47], s35 offen offset:1024 sc1\n"
    "buffer_load_dwordx4 v[248:251], v28, s[44:47], s35 offen offset:2048 sc1\n"
    "buffer_load_dwordx4 v[252:255], v28, s[44:47], s35 offen offset:3072 sc1\n"
    "s_add_u32 s60, s34, 8192\n"
    "buffer_load_dwordx4 v[100:103], v1, s[44:47], s60 offen offset:3072 sc1\n"
    "s_add_u32 s60, s34, 12288\n"
    "buffer_load_dwordx4 v[104:107], v1, s[44:47], s60 offen offset:0 sc1\n"
    "buffer_load_dwordx4 v[108:111], v1, s[44:47], s60 offen offset:1024 sc1\n"
    "buffer_load_dwordx4 v[112:115], v1, s[44:47], s60 offen offset:2048 sc1\n"
    "buffer_load_dwordx4 v[116:119], v1, s[44:47], s60 offen offset:3072 sc1\n"
    "s_add_u32 s60, s34, 16384\n"
    "buffer_load_dwordx4 v[120:123], v1, s[44:47], s60 offen offset:0 sc1\n"
    "buffer_load_dwordx4 v[124:127], v1, s[44:47], s60 offen offset:1024 sc1\n"
    "s_setprio 0\n"
    "v_add_u32 v27, s18, v27\n"
    "ds_read_b128 v[80:83], v27\n"
    "s_waitcnt vmcnt(41)\n"
    "v_add_f32 v36, v36, v37\n"
    "s_waitcnt vmcnt(39)\n"
    "v_add_f32 v38, v38, v39\n"
    "v_add_f32 v36, v36, v38\n"
    "v_mov_b32 v37, v36\n"
    "v_lshlrev_b32 v4, 16, v4\n"
    "v_cmp_gt_u32 vcc, 32, v0\n"
    "v_permlane32_swap_b32 v36, v37\n"
    "v_add_f32 v36, v36, v37\n"
    "v_cndmask_b32 v36, v4, v36, vcc\n"
    "v_cvt_pk_bf16_f32 v4, v36, v36\n"
    "v_and_b32 v37, 0xffff0000, v4\n"
    "v_sub_f32 v36, v36, v37\n"
    "v_cvt_pk_bf16_f32 v5, v36, v36\n"
    "v_add_u32 v12, s19, v12\n"
    "v_add_u32 v13, s19, v13\n"
    "v_add_u32 v14, s19, v14\n"
    "v_mov_b32 v48, 0\n"
    "v_mov_b32 v49, 0\n"
    "v_mov_b32 v50, 0\n"
    "v_mov_b32 v51, 0\n"
    "v_mov_b32 v52, 0\n"
    "v_mov_b32 v53, 0\n"
    "v_mov_b32 v54, 0\n"
    "v_mov_b32 v55, 0\n"
    "v_mov_b32 v56, 0\n"
    "v_mov_b32 v57, 0\n"
    "v_mov_b32 v58, 0\n"
    "v_mov_b32 v59, 0\n"
    "v_mov_b32 v60, 0\n"
    "v_mov_b32 v61, 0\n"
    "v_mov_b32 v62, 0\n"
    "v_mov_b32 v63, 0\n"
    "v_mov_b32 v84, 0\n"
    "v_mov_b32 v85, 0\n"
    "v_mov_b32 v86, 0\n"
    "v_mov_b32 v87, 0\n"
    "v_mov_b32 v88, 0\n"
    "v_mov_b32 v89, 0\n"
    "v_mov_b32 v90, 0\n"
    "v_mov_b32 v91, 0\n"
    "v_mov_b32 v92, 0\n"
    "v_mov_b32 v93, 0\n"
    "v_mov_b32 v94, 0\n"
    "v_mov_b32 v95, 0\n"
    "v_mov_b32 v96, 0\n"
    "v_mov_b32 v97, 0\n"
    "v_mov_b32 v98, 0\n"
    "v_mov_b32 v99, 0\n"
    "ds_read_b128 v[64:67], v12 offset:0\n"
    "ds_read_b128 v[68:71], v13 offset:0\n"
    "s_waitcnt lgkmcnt(1)\n"
    "v_mfma_f32_32x32x16_bf16 v[16:31], v[64:67], v[80:83], -4.0\n"
    "ds_read_b128 v[72:75], v13 offset:4160\n"
    "s_nop 10\n"
    "v_exp_f32 v16, v16\n"
    "v_exp_f32 v17, v17\n"
    "v_exp_f32 v18, v18\n"
    "ds_read_b128 v[76:79], v14 offset:0\n"
    "v_exp_f32 v19, v19\n"
    "v_exp_f32 v20, v20\n"
    "v_exp_f32 v21, v21\n"
    "v_mfma_f32_32x32x16_bf16 v[32:47], v[64:67], v[8:11], -4.0\n"
    "ds_read_b128 v[64:67], v12 offset:4160\n"
    "v_exp_f32 v22, v22\n"
    "v_exp_f32 v23, v23\n"
    "v_exp_f32 v24, v24\n"
    "v_exp_f32 v25, v25\n"
    "v_exp_f32 v26, v26\n"
    "s_waitcnt lgkmcnt(3)\n"
    "v_smfmac_f32_32x32x32_bf16 v[84:99], v[68:71], v[16:23], v15\n"
    "v_exp_f32 v27, v27\n"
    "v_exp_f32 v28, v28\n"
    "v_exp_f32 v29, v29\n"
    "v_exp_f32 v30, v30\n"
    "v_exp_f32 v31, v31\n"
    "v_exp_f32 v32, v32\n"
    "v_exp_f32 v33, v33\n"
    "v_exp_f32 v34, v34\n"
    "s_waitcnt lgkmcnt(0)\n"
    "v_smfmac_f32_32x32x32_bf16 v[84:99], v[76:79], v[24:31], v15\n"
    "v_exp_f32 v35, v35\n"
    "v_exp_f32 v36, v36\n"
    "v_exp_f32 v37, v37\n"
    "v_mfma_f32_32x32x16_bf16 v[16:31], v[64:67], v[80:83], -4.0\n"
    "v_exp_f32 v38, v38\n"
    "v_exp_f32 v39, v39\n"
    "v_exp_f32 v40, v40\n"
    "v_exp_f32 v41, v41\n"
    "v_exp_f32 v42, v42\n"
    "v_smfmac_f32_32x32x32_bf16 v[48:63], v[68:71], v[32:39], v15\n"
    "ds_read_b128 v[68:71], v13 offset:8320\n"
    "v_exp_f32 v43, v43\n"
    "v_exp_f32 v44, v44\n"
    "v_exp_f32 v45, v45\n"
    "v_exp_f32 v46, v46\n"
    "v_exp_f32 v47, v47\n"
    "v_exp_f32 v16, v16\n"
    "v_exp_f32 v17, v17\n"
    "v_exp_f32 v18, v18\n"
    "v_smfmac_f32_32x32x32_bf16 v[48:63], v[76:79], v[40:47], v15\n"
    "ds_read_b128 v[76:79], v14 offset:4160\n"
    "v_exp_f32 v19, v19\n"
    "v_exp_f32 v20, v20\n"
    "v_exp_f32 v21, v21\n"
    "v_mfma_f32_32x32x16_bf16 v[32:47], v[64:67], v[8:11], -4.0\n"
    "ds_read_b128 v[64:67], v12 offset:8320\n"
    "v_exp_f32 v22, v22\n"
    "v_exp_f32 v23, v23\n"
    "v_exp_f32 v24, v24\n"
    "v_exp_f32 v25, v25\n"
    "v_exp_f32 v26, v26\n"
    "v_smfmac_f32_32x32x32_bf16 v[84:99], v[72:75], v[16:23], v15\n"
    "v_exp_f32 v27, v27\n"
    "v_exp_f32 v28, v28\n"
    "v_exp_f32 v29, v29\n"
    "v_exp_f32 v30, v30\n"
    "v_exp_f32 v31, v31\n"
    "v_exp_f32 v32, v32\n"
    "v_exp_f32 v33, v33\n"
    "v_exp_f32 v34, v34\n"
    "s_waitcnt lgkmcnt(0)\n"
    "v_smfmac_f32_32x32x32_bf16 v[84:99], v[76:79], v[24:31], v15\n"
    "v_exp_f32 v35, v35\n"
    "v_exp_f32 v36, v36\n"
    "v_exp_f32 v37, v37\n"
    "v_mfma_f32_32x32x16_bf16 v[16:31], v[64:67], v[80:83], -4.0\n"
    "v_exp_f32 v38, v38\n"
    "v_exp_f32 v39, v39\n"
    "v_exp_f32 v40, v40\n"
    "v_exp_f32 v41, v41\n"
    "v_exp_f32 v42, v42\n"
    "v_smfmac_f32_32x32x32_bf16 v[48:63], v[72:75], v[32:39], v15\n"
    "ds_read_b128 v[72:75], v13 offset:12480\n"
    "v_exp_f32 v43, v43\n"
    "v_exp_f32 v44, v44\n"
    "v_exp_f32 v45, v45\n"
    "v_exp_f32 v46, v46\n"
    "v_exp_f32 v47, v47\n"
    "v_exp_f32 v16, v16\n"
    "v_exp_f32 v17, v17\n"
    "v_exp_f32 v18, v18\n"
    "v_smfmac_f32_32x32x32_bf16 v[48:63], v[76:79], v[40:47], v15\n"
    "ds_read_b128 v[76:79], v14 offset:8320\n"
    "v_exp_f32 v19, v19\n"
    "v_exp_f32 v20, v20\n"
    "v_exp_f32 v21, v21\n"
    "v_mfma_f32_32x32x16_bf16 v[32:47], v[64:67], v[8:11], -4.0\n"
    "ds_read_b128 v[64:67], v12 offset:12480\n"
    "v_exp_f32 v22, v22\n"
    "v_exp_f32 v23, v23\n"
    "v_exp_f32 v24, v24\n"
    "v_exp_f32 v25, v25\n"
    "v_exp_f32 v26, v26\n"
    "v_smfmac_f32_32x32x32_bf16 v[84:99], v[68:71], v[16:23], v15\n"
    "v_exp_f32 v27, v27\n"
    "v_exp_f32 v28, v28\n"
    "v_exp_f32 v29, v29\n"
    "v_exp_f32 v30, v30\n"
    "v_exp_f32 v31, v31\n"
    "v_exp_f32 v32, v32\n"
    "v_exp_f32 v33, v33\n"
    "v_exp_f32 v34, v34\n"
    "s_waitcnt lgkmcnt(0)\n"
    "v_smfmac_f32_32x32x32_bf16 v[84:99], v[76:79], v[24:31], v15\n"
    "v_exp_f32 v35, v35\n"
    "v_exp_f32 v36, v36\n"
    "v_exp_f32 v37, v37\n"
    "v_mfma_f32_32x32x16_bf16 v[16:31], v[64:67], v[80:83], -4.0\n"
    "v_exp_f32 v38, v38\n"
    "v_exp_f32 v39, v39\n"
    "v_exp_f32 v40, v40\n"
    "v_exp_f32 v41, v41\n"
    "v_exp_f32 v42, v42\n"
    "v_smfmac_f32_32x32x32_bf16 v[48:63], v[68:71], v[32:39], v15\n"
    "ds_read_b128 v[68:71], v13 offset:16640\n"
    "v_exp_f32 v43, v43\n"
    "v_exp_f32 v44, v44\n"
    "v_exp_f32 v45, v45\n"
    "v_exp_f32 v46, v46\n"
    "v_exp_f32 v47, v47\n"
    "v_exp_f32 v16, v16\n"
    "v_exp_f32 v17, v17\n"
    "v_exp_f32 v18, v18\n"
    "v_smfmac_f32_32x32x32_bf16 v[48:63], v[76:79], v[40:47], v15\n"
    "ds_read_b128 v[76:79], v14 offset:12480\n"
    "v_exp_f32 v19, v19\n"
    "v_exp_f32 v20, v20\n"
    "v_exp_f32 v21, v21\n"
    "v_mfma_f32_32x32x16_bf16 v[32:47], v[64:67], v[8:11], -4.0\n"
    "ds_read_b128 v[64:67], v12 offset:16640\n"
    "v_exp_f32 v22, v22\n"
    "v_exp_f32 v23, v23\n"
    "v_exp_f32 v24, v24\n"
    "v_exp_f32 v25, v25\n"
    "v_exp_f32 v26, v26\n"
    "v_smfmac_f32_32x32x32_bf16 v[84:99], v[72:75], v[16:23], v15\n"
    "v_exp_f32 v27, v27\n"
    "v_exp_f32 v28, v28\n"
    "v_exp_f32 v29, v29\n"
    "v_exp_f32 v30, v30\n"
    "v_exp_f32 v31, v31\n"
    "v_exp_f32 v32, v32\n"
    "v_exp_f32 v33, v33\n"
    "v_exp_f32 v34, v34\n"
    "s_waitcnt lgkmcnt(0)\n"
    "v_smfmac_f32_32x32x32_bf16 v[84:99], v[76:79], v[24:31], v15\n"
    "v_exp_f32 v35, v35\n"
    "v_exp_f32 v36, v36\n"
    "v_exp_f32 v37, v37\n"
    "v_mfma_f32_32x32x16_bf16 v[16:31], v[64:67], v[80:83], -4.0\n"
    "v_exp_f32 v38, v38\n"
    "v_exp_f32 v39, v39\n"
    "v_exp_f32 v40, v40\n"
    "v_exp_f32 v41, v41\n"
    "v_exp_f32 v42, v42\n"
    "v_smfmac_f32_32x32x32_bf16 v[48:63], v[72:75], v[32:39], v15\n"
    "ds_read_b128 v[72:75], v13 offset:20800\n"
    "v_exp_f32 v43, v43\n"
    "v_exp_f32 v44, v44\n"
    "v_exp_f32 v45, v45\n"
    "v_exp_f32 v46, v46\n"
    "v_exp_f32 v47, v47\n"
    "v_exp_f32 v16, v16\n"
    "v_exp_f32 v17, v17\n"
    "v_exp_f32 v18, v18\n"
    "v_smfmac_f32_32x32x32_bf16 v[48:63], v[76:79], v[40:47], v15\n"
    "ds_read_b128 v[76:79], v14 offset:16640\n"
    "v_exp_f32 v19, v19\n"
    "v_exp_f32 v20, v20\n"
    "v_exp_f32 v21, v21\n"
    "v_mfma_f32_32x32x16_bf16 v[32:47], v[64:67], v[8:11], -4.0\n"
    "ds_read_b128 v[64:67], v12 offset:20800\n"
    "v_exp_f32 v22, v22\n"
    "v_exp_f32 v23, v23\n"
    "v_exp_f32 v24, v24\n"
    "v_exp_f32 v25, v25\n"
    "v_exp_f32 v26, v26\n"
    "v_smfmac_f32_32x32x32_bf16 v[84:99], v[68:71], v[16:23], v15\n"
    "v_exp_f32 v27, v27\n"
    "v_exp_f32 v28, v28\n"
    "v_exp_f32 v29, v29\n"
    "v_exp_f32 v30, v30\n"
    "v_exp_f32 v31, v31\n"
    "v_exp_f32 v32, v32\n"
    "v_exp_f32 v33, v33\n"
    "v_exp_f32 v34, v34\n"
    "s_waitcnt lgkmcnt(0)\n"
    "v_smfmac_f32_32x32x32_bf16 v[84:99], v[76:79], v[24:31], v15\n"
    "v_exp_f32 v35, v35\n"
    "v_exp_f32 v36, v36\n"
    "v_exp_f32 v37, v37\n"
    "v_mfma_f32_32x32x16_bf16 v[16:31], v[64:67], v[80:83], -4.0\n"
    "v_exp_f32 v38, v38\n"
    "v_exp_f32 v39, v39\n"
    "v_exp_f32 v40, v40\n"
    "v_exp_f32 v41, v41\n"
    "v_exp_f32 v42, v42\n"
    "v_smfmac_f32_32x32x32_bf16 v[48:63], v[68:71], v[32:39], v15\n"
    "ds_read_b128 v[68:71], v13 offset:24960\n"
    "v_exp_f32 v43, v43\n"
    "v_exp_f32 v44, v44\n"
    "v_exp_f32 v45, v45\n"
    "v_exp_f32 v46, v46\n"
    "v_exp_f32 v47, v47\n"
    "v_exp_f32 v16, v16\n"
    "v_exp_f32 v17, v17\n"
    "v_exp_f32 v18, v18\n"
    "v_smfmac_f32_32x32x32_bf16 v[48:63], v[76:79], v[40:47], v15\n"
    "ds_read_b128 v[76:79], v14 offset:20800\n"
    "v_exp_f32 v19, v19\n"
    "v_exp_f32 v20, v20\n"
    "v_exp_f32 v21, v21\n"
    "v_mfma_f32_32x32x16_bf16 v[32:47], v[64:67], v[8:11], -4.0\n"
    "ds_read_b128 v[64:67], v12 offset:24960\n"
    "v_exp_f32 v22, v22\n"
    "v_exp_f32 v23, v23\n"
    "v_exp_f32 v24, v24\n"
    "v_exp_f32 v25, v25\n"
    "v_exp_f32 v26, v26\n"
    "v_smfmac_f32_32x32x32_bf16 v[84:99], v[72:75], v[16:23], v15\n"
    "v_exp_f32 v27, v27\n"
    "v_exp_f32 v28, v28\n"
    "v_exp_f32 v29, v29\n"
    "v_exp_f32 v30, v30\n"
    "v_exp_f32 v31, v31\n"
    "v_exp_f32 v32, v32\n"
    "v_exp_f32 v33, v33\n"
    "v_exp_f32 v34, v34\n"
    "s_waitcnt lgkmcnt(0)\n"
    "v_smfmac_f32_32x32x32_bf16 v[84:99], v[76:79], v[24:31], v15\n"
    "v_exp_f32 v35, v35\n"
    "v_exp_f32 v36, v36\n"
    "v_exp_f32 v37, v37\n"
    "v_mfma_f32_32x32x16_bf16 v[16:31], v[64:67], v[80:83], -4.0\n"
    "v_exp_f32 v38, v38\n"
    "v_exp_f32 v39, v39\n"
    "v_exp_f32 v40, v40\n"
    "v_exp_f32 v41, v41\n"
    "v_exp_f32 v42, v42\n"
    "v_smfmac_f32_32x32x32_bf16 v[48:63], v[72:75], v[32:39], v15\n"
    "ds_read_b128 v[72:75], v13 offset:29120\n"
    "v_exp_f32 v43, v43\n"
    "v_exp_f32 v44, v44\n"
    "v_exp_f32 v45, v45\n"
    "v_exp_f32 v46, v46\n"
    "v_exp_f32 v47, v47\n"
    "v_exp_f32 v16, v16\n"
    "v_exp_f32 v17, v17\n"
    "v_exp_f32 v18, v18\n"
    "v_smfmac_f32_32x32x32_bf16 v[48:63], v[76:79], v[40:47], v15\n"
    "ds_read_b128 v[76:79], v14 offset:24960\n"
    "v_exp_f32 v19, v19\n"
    "v_exp_f32 v20, v20\n"
    "v_exp_f32 v21, v21\n"
    "v_mfma_f32_32x32x16_bf16 v[32:47], v[64:67], v[8:11], -4.0\n"
    "ds_read_b128 v[64:67], v12 offset:29120\n"
    "v_exp_f32 v22, v22\n"
    "v_exp_f32 v23, v23\n"
    "v_exp_f32 v24, v24\n"
    "v_exp_f32 v25, v25\n"
    "v_exp_f32 v26, v26\n"
    "v_smfmac_f32_32x32x32_bf16 v[84:99], v[68:71], v[16:23], v15\n"
    "v_exp_f32 v27, v27\n"
    "v_exp_f32 v28, v28\n"
    "v_exp_f32 v29, v29\n"
    "v_exp_f32 v30, v30\n"
    "v_exp_f32 v31, v31\n"
    "v_exp_f32 v32, v32\n"
    "v_exp_f32 v33, v33\n"
    "v_exp_f32 v34, v34\n"
    "s_waitcnt lgkmcnt(0)\n"
    "v_smfmac_f32_32x32x32_bf16 v[84:99], v[76:79], v[24:31], v15\n"
    "v_exp_f32 v35, v35\n"
    "v_exp_f32 v36, v36\n"
    "v_exp_f32 v37, v37\n"
    "v_mfma_f32_32x32x16_bf16 v[16:31], v[64:67], v[80:83], -4.0\n"
    "v_exp_f32 v38, v38\n"
    "v_exp_f32 v39, v39\n"
    "v_exp_f32 v40, v40\n"
    "v_exp_f32 v41, v41\n"
    "v_exp_f32 v42, v42\n"
    "v_smfmac_f32_32x32x32_bf16 v[48:63], v[68:71], v[32:39], v15\n"
    "ds_read_b128 v[68:71], v13 offset:33280\n"
    "v_exp_f32 v43, v43\n"
    "v_exp_f32 v44, v44\n"
    "v_exp_f32 v45, v45\n"
    "v_exp_f32 v46, v46\n"
    "v_exp_f32 v47, v47\n"
    "v_exp_f32 v16, v16\n"
    "v_exp_f32 v17, v17\n"
    "v_exp_f32 v18, v18\n"
    "v_smfmac_f32_32x32x32_bf16 v[48:63], v[76:79], v[40:47], v15\n"
    "ds_read_b128 v[76:79], v14 offset:29120\n"
    "v_exp_f32 v19, v19\n"
    "v_exp_f32 v20, v20\n"
    "v_exp_f32 v21, v21\n"
    "v_mfma_f32_32x32x16_bf16 v[32:47], v[64:67], v[8:11], -4.0\n"
    "ds_read_b128 v[64:67], v12 offset:33280\n"
    "v_exp_f32 v22, v22\n"
    "v_exp_f32 v23, v23\n"
    "v_exp_f32 v24, v24\n"
    "v_exp_f32 v25, v25\n"
    "v_exp_f32 v26, v26\n"
    "v_smfmac_f32_32x32x32_bf16 v[84:99], v[72:75], v[16:23], v15\n"
    "v_exp_f32 v27, v27\n"
    "v_exp_f32 v28, v28\n"
    "v_exp_f32 v29, v29\n"
    "v_exp_f32 v30, v30\n"
    "v_exp_f32 v31, v31\n"
    "v_exp_f32 v32, v32\n"
    "v_exp_f32 v33, v33\n"
    "v_exp_f32 v34, v34\n"
    "s_waitcnt lgkmcnt(0)\n"
    "v_smfmac_f32_32x32x32_bf16 v[84:99], v[76:79], v[24:31], v15\n"
    "v_exp_f32 v35, v35\n"
    "v_exp_f32 v36, v36\n"
    "v_exp_f32 v37, v37\n"
    "v_mfma_f32_32x32x16_bf16 v[16:31], v[64:67], v[80:83], -4.0\n"
    "v_exp_f32 v38, v38\n"
    "v_exp_f32 v39, v39\n"
    "v_exp_f32 v40, v40\n"
    "v_exp_f32 v41, v41\n"
    "v_exp_f32 v42, v42\n"
    "v_smfmac_f32_32x32x32_bf16 v[48:63], v[72:75], v[32:39], v15\n"
    "ds_read_b128 v[72:75], v13 offset:37440\n"
    "v_exp_f32 v43, v43\n"
    "v_exp_f32 v44, v44\n"
    "v_exp_f32 v45, v45\n"
    "v_exp_f32 v46, v46\n"
    "v_exp_f32 v47, v47\n"
    "s_waitcnt vmcnt(0)\n"
    "s_setprio 1\n"
    "ds_write_b8 v2, v2 offset:32\n"
    "v_exp_f32 v16, v16\n"
    "v_exp_f32 v17, v17\n"
    "v_exp_f32 v18, v18\n"
    "v_smfmac_f32_32x32x32_bf16 v[48:63], v[76:79], v[40:47], v15\n"
    "ds_read_b128 v[76:79], v14 offset:33280\n"
    "v_exp_f32 v19, v19\n"
    "v_exp_f32 v20, v20\n"
    "v_exp_f32 v21, v21\n"
    "v_mfma_f32_32x32x16_bf16 v[32:47], v[64:67], v[8:11], -4.0\n"
    "ds_read_b128 v[64:67], v12 offset:37440\n"
    "v_exp_f32 v22, v22\n"
    "v_exp_f32 v23, v23\n"
    "v_exp_f32 v24, v24\n"
    "v_exp_f32 v25, v25\n"
    "v_exp_f32 v26, v26\n"
    "v_smfmac_f32_32x32x32_bf16 v[84:99], v[68:71], v[16:23], v15\n"
    "v_exp_f32 v27, v27\n"
    "v_exp_f32 v28, v28\n"
    "v_exp_f32 v29, v29\n"
    "v_exp_f32 v30, v30\n"
    "v_exp_f32 v31, v31\n"
    "v_exp_f32 v32, v32\n"
    "v_exp_f32 v33, v33\n"
    "v_exp_f32 v34, v34\n"
    "s_waitcnt lgkmcnt(0)\n"
    "v_smfmac_f32_32x32x32_bf16 v[84:99], v[76:79], v[24:31], v15\n"
    "v_exp_f32 v35, v35\n"
    "v_exp_f32 v36, v36\n"
    "v_exp_f32 v37, v37\n"
    "v_mfma_f32_32x32x16_bf16 v[16:31], v[64:67], v[80:83], -4.0\n"
    "v_exp_f32 v38, v38\n"
    "v_exp_f32 v39, v39\n"
    "v_exp_f32 v40, v40\n"
    "v_exp_f32 v41, v41\n"
    "v_exp_f32 v42, v42\n"
    "v_smfmac_f32_32x32x32_bf16 v[48:63], v[68:71], v[32:39], v15\n"
    "ds_read_b128 v[68:71], v13 offset:41600\n"
    "v_exp_f32 v43, v43\n"
    "v_exp_f32 v44, v44\n"
    "v_exp_f32 v45, v45\n"
    "v_exp_f32 v46, v46\n"
    "v_exp_f32 v47, v47\n"
    "v_exp_f32 v16, v16\n"
    "v_exp_f32 v17, v17\n"
    "v_exp_f32 v18, v18\n"
    "v_smfmac_f32_32x32x32_bf16 v[48:63], v[76:79], v[40:47], v15\n"
    "ds_read_b128 v[76:79], v14 offset:37440\n"
    "v_exp_f32 v19, v19\n"
    "v_exp_f32 v20, v20\n"
    "v_exp_f32 v21, v21\n"
    "v_mfma_f32_32x32x16_bf16 v[32:47], v[64:67], v[8:11], -4.0\n"
    "ds_read_b128 v[64:67], v12 offset:41600\n"
    "v_exp_f32 v22, v22\n"
    "v_exp_f32 v23, v23\n"
    "v_exp_f32 v24, v24\n"
    "v_exp_f32 v25, v25\n"
    "v_exp_f32 v26, v26\n"
    "v_smfmac_f32_32x32x32_bf16 v[84:99], v[72:75], v[16:23], v15\n"
    "v_exp_f32 v27, v27\n"
    "v_exp_f32 v28, v28\n"
    "v_exp_f32 v29, v29\n"
    "v_exp_f32 v30, v30\n"
    "v_exp_f32 v31, v31\n"
    "v_exp_f32 v32, v32\n"
    "v_exp_f32 v33, v33\n"
    "v_exp_f32 v34, v34\n"
    "s_waitcnt lgkmcnt(0)\n"
    "v_smfmac_f32_32x32x32_bf16 v[84:99], v[76:79], v[24:31], v15\n"
    "v_exp_f32 v35, v35\n"
    "v_exp_f32 v36, v36\n"
    "v_exp_f32 v37, v37\n"
    "v_mfma_f32_32x32x16_bf16 v[16:31], v[64:67], v[80:83], -4.0\n"
    "v_exp_f32 v38, v38\n"
    "v_exp_f32 v39, v39\n"
    "v_exp_f32 v40, v40\n"
    "v_exp_f32 v41, v41\n"
    "v_exp_f32 v42, v42\n"
    "v_smfmac_f32_32x32x32_bf16 v[48:63], v[72:75], v[32:39], v15\n"
    "ds_read_b128 v[72:75], v13 offset:45760\n"
    "v_exp_f32 v43, v43\n"
    "v_exp_f32 v44, v44\n"
    "v_exp_f32 v45, v45\n"
    "v_exp_f32 v46, v46\n"
    "v_exp_f32 v47, v47\n"
    "v_exp_f32 v16, v16\n"
    "v_exp_f32 v17, v17\n"
    "v_exp_f32 v18, v18\n"
    "v_smfmac_f32_32x32x32_bf16 v[48:63], v[76:79], v[40:47], v15\n"
    "ds_read_b128 v[76:79], v14 offset:41600\n"
    "v_exp_f32 v19, v19\n"
    "v_exp_f32 v20, v20\n"
    "v_exp_f32 v21, v21\n"
    "v_mfma_f32_32x32x16_bf16 v[32:47], v[64:67], v[8:11], -4.0\n"
    "ds_read_b128 v[64:67], v12 offset:45760\n"
    "v_exp_f32 v22, v22\n"
    "v_exp_f32 v23, v23\n"
    "v_exp_f32 v24, v24\n"
    "v_exp_f32 v25, v25\n"
    "v_exp_f32 v26, v26\n"
    "v_smfmac_f32_32x32x32_bf16 v[84:99], v[68:71], v[16:23], v15\n"
    "v_exp_f32 v27, v27\n"
    "v_exp_f32 v28, v28\n"
    "v_exp_f32 v29, v29\n"
    "v_exp_f32 v30, v30\n"
    "v_exp_f32 v31, v31\n"
    "v_exp_f32 v32, v32\n"
    "v_exp_f32 v33, v33\n"
    "v_exp_f32 v34, v34\n"
    "s_waitcnt lgkmcnt(0)\n"
    "v_smfmac_f32_32x32x32_bf16 v[84:99], v[76:79], v[24:31], v15\n"
    "v_exp_f32 v35, v35\n"
    "v_exp_f32 v36, v36\n"
    "v_exp_f32 v37, v37\n"
    "v_mfma_f32_32x32x16_bf16 v[16:31], v[64:67], v[80:83], -4.0\n"
    "v_exp_f32 v38, v38\n"
    "v_exp_f32 v39, v39\n"
    "v_exp_f32 v40, v40\n"
    "v_exp_f32 v41, v41\n"
    "v_exp_f32 v42, v42\n"
    "v_smfmac_f32_32x32x32_bf16 v[48:63], v[68:71], v[32:39], v15\n"
    "ds_read_b128 v[68:71], v13 offset:49920\n"
    "v_exp_f32 v43, v43\n"
    "v_exp_f32 v44, v44\n"
    "v_exp_f32 v45, v45\n"
    "v_exp_f32 v46, v46\n"
    "v_exp_f32 v47, v47\n"
    "v_exp_f32 v16, v16\n"
    "v_exp_f32 v17, v17\n"
    "v_exp_f32 v18, v18\n"
    "v_smfmac_f32_32x32x32_bf16 v[48:63], v[76:79], v[40:47], v15\n"
    "ds_read_b128 v[76:79], v14 offset:45760\n"
    "v_exp_f32 v19, v19\n"
    "v_exp_f32 v20, v20\n"
    "v_exp_f32 v21, v21\n"
    "v_mfma_f32_32x32x16_bf16 v[32:47], v[64:67], v[8:11], -4.0\n"
    "ds_read_b128 v[64:67], v12 offset:49920\n"
    "v_exp_f32 v22, v22\n"
    "v_exp_f32 v23, v23\n"
    "v_exp_f32 v24, v24\n"
    "v_exp_f32 v25, v25\n"
    "v_exp_f32 v26, v26\n"
    "v_smfmac_f32_32x32x32_bf16 v[84:99], v[72:75], v[16:23], v15\n"
    "v_exp_f32 v27, v27\n"
    "v_exp_f32 v28, v28\n"
    "v_exp_f32 v29, v29\n"
    "v_exp_f32 v30, v30\n"
    "v_exp_f32 v31, v31\n"
    "v_exp_f32 v32, v32\n"
    "v_exp_f32 v33, v33\n"
    "v_exp_f32 v34, v34\n"
    "s_waitcnt lgkmcnt(0)\n"
    "v_smfmac_f32_32x32x32_bf16 v[84:99], v[76:79], v[24:31], v15\n"
    "v_exp_f32 v35, v35\n"
    "v_exp_f32 v36, v36\n"
    "v_exp_f32 v37, v37\n"
    "v_mfma_f32_32x32x16_bf16 v[16:31], v[64:67], v[80:83], -4.0\n"
    "v_exp_f32 v38, v38\n"
    "v_exp_f32 v39, v39\n"
    "v_exp_f32 v40, v40\n"
    "v_exp_f32 v41, v41\n"
    "v_exp_f32 v42, v42\n"
    "v_smfmac_f32_32x32x32_bf16 v[48:63], v[72:75], v[32:39], v15\n"
    "ds_read_b128 v[72:75], v13 offset:54080\n"
    "v_exp_f32 v43, v43\n"
    "v_exp_f32 v44, v44\n"
    "v_exp_f32 v45, v45\n"
    "v_exp_f32 v46, v46\n"
    "v_exp_f32 v47, v47\n"
    "v_exp_f32 v16, v16\n"
    "v_exp_f32 v17, v17\n"
    "v_exp_f32 v18, v18\n"
    "v_smfmac_f32_32x32x32_bf16 v[48:63], v[76:79], v[40:47], v15\n"
    "ds_read_b128 v[76:79], v14 offset:49920\n"
    "v_exp_f32 v19, v19\n"
    "v_exp_f32 v20, v20\n"
    "v_exp_f32 v21, v21\n"
    "v_mfma_f32_32x32x16_bf16 v[32:47], v[64:67], v[8:11], -4.0\n"
    "ds_read_b128 v[64:67], v12 offset:54080\n"
    "v_exp_f32 v22, v22\n"
    "v_exp_f32 v23, v23\n"
    "v_exp_f32 v24, v24\n"
    "v_exp_f32 v25, v25\n"
    "v_exp_f32 v26, v26\n"
    "v_smfmac_f32_32x32x32_bf16 v[84:99], v[68:71], v[16:23], v15\n"
    "v_exp_f32 v27, v27\n"
    "v_exp_f32 v28, v28\n"
    "v_exp_f32 v29, v29\n"
    "v_exp_f32 v30, v30\n"
    "v_exp_f32 v31, v31\n"
    "v_exp_f32 v32, v32\n"
    "v_exp_f32 v33, v33\n"
    "v_exp_f32 v34, v34\n"
    "s_waitcnt lgkmcnt(0)\n"
    "v_smfmac_f32_32x32x32_bf16 v[84:99], v[76:79], v[24:31], v15\n"
    "v_exp_f32 v35, v35\n"
    "v_exp_f32 v36, v36\n"
    "v_exp_f32 v37, v37\n"
    "v_mfma_f32_32x32x16_bf16 v[16:31], v[64:67], v[80:83], -4.0\n"
    "v_exp_f32 v38, v38\n"
    "v_exp_f32 v39, v39\n"
    "v_exp_f32 v40, v40\n"
    "v_exp_f32 v41, v41\n"
    "v_exp_f32 v42, v42\n"
    "v_smfmac_f32_32x32x32_bf16 v[48:63], v[68:71], v[32:39], v15\n"
    "ds_read_b128 v[68:71], v13 offset:58240\n"
    "v_exp_f32 v43, v43\n"
    "v_exp_f32 v44, v44\n"
    "v_exp_f32 v45, v45\n"
    "v_exp_f32 v46, v46\n"
    "v_exp_f32 v47, v47\n"
    "v_exp_f32 v16, v16\n"
    "v_exp_f32 v17, v17\n"
    "v_exp_f32 v18, v18\n"
    "v_smfmac_f32_32x32x32_bf16 v[48:63], v[76:79], v[40:47], v15\n"
    "ds_read_b128 v[76:79], v14 offset:54080\n"
    "v_exp_f32 v19, v19\n"
    "v_exp_f32 v20, v20\n"
    "v_exp_f32 v21, v21\n"
    "v_mfma_f32_32x32x16_bf16 v[32:47], v[64:67], v[8:11], -4.0\n"
    "ds_read_b128 v[64:67], v12 offset:58240\n"
    "v_exp_f32 v22, v22\n"
    "v_exp_f32 v23, v23\n"
    "v_exp_f32 v24, v24\n"
    "v_exp_f32 v25, v25\n"
    "v_exp_f32 v26, v26\n"
    "v_smfmac_f32_32x32x32_bf16 v[84:99], v[72:75], v[16:23], v15\n"
    "v_exp_f32 v27, v27\n"
    "v_exp_f32 v28, v28\n"
    "v_exp_f32 v29, v29\n"
    "v_exp_f32 v30, v30\n"
    "v_exp_f32 v31, v31\n"
    "v_exp_f32 v32, v32\n"
    "v_exp_f32 v33, v33\n"
    "v_exp_f32 v34, v34\n"
    "s_waitcnt lgkmcnt(0)\n"
    "v_smfmac_f32_32x32x32_bf16 v[84:99], v[76:79], v[24:31], v15\n"
    "v_exp_f32 v35, v35\n"
    "v_exp_f32 v36, v36\n"
    "v_exp_f32 v37, v37\n"
    "v_mfma_f32_32x32x16_bf16 v[16:31], v[64:67], v[80:83], -4.0\n"
    "v_exp_f32 v38, v38\n"
    "v_exp_f32 v39, v39\n"
    "v_exp_f32 v40, v40\n"
    "v_exp_f32 v41, v41\n"
    "v_exp_f32 v42, v42\n"
    "v_smfmac_f32_32x32x32_bf16 v[48:63], v[72:75], v[32:39], v15\n"
    "ds_read_b128 v[72:75], v13 offset:62400\n"
    "v_exp_f32 v43, v43\n"
    "v_exp_f32 v44, v44\n"
    "v_exp_f32 v45, v45\n"
    "v_exp_f32 v46, v46\n"
    "v_exp_f32 v47, v47\n"
    "v_exp_f32 v16, v16\n"
    "v_exp_f32 v17, v17\n"
    "v_exp_f32 v18, v18\n"
    "v_smfmac_f32_32x32x32_bf16 v[48:63], v[76:79], v[40:47], v15\n"
    "ds_read_b128 v[76:79], v14 offset:58240\n"
    "v_exp_f32 v19, v19\n"
    "v_exp_f32 v20, v20\n"
    "v_exp_f32 v21, v21\n"
    "v_mfma_f32_32x32x16_bf16 v[32:47], v[64:67], v[8:11], -4.0\n"
    "ds_read_b128 v[64:67], v12 offset:62400\n"
    "v_exp_f32 v22, v22\n"
    "v_exp_f32 v23, v23\n"
    "v_exp_f32 v24, v24\n"
    "v_exp_f32 v25, v25\n"
    "v_exp_f32 v26, v26\n"
    "v_smfmac_f32_32x32x32_bf16 v[84:99], v[68:71], v[16:23], v15\n"
    "v_exp_f32 v27, v27\n"
    "v_exp_f32 v28, v28\n"
    "v_exp_f32 v29, v29\n"
    "v_exp_f32 v30, v30\n"
    "v_exp_f32 v31, v31\n"
    "v_exp_f32 v32, v32\n"
    "v_exp_f32 v33, v33\n"
    "v_exp_f32 v34, v34\n"
    "s_waitcnt lgkmcnt(0)\n"
    "v_smfmac_f32_32x32x32_bf16 v[84:99], v[76:79], v[24:31], v15\n"
    "v_exp_f32 v35, v35\n"
    "v_exp_f32 v36, v36\n"
    "v_exp_f32 v37, v37\n"
    "v_mfma_f32_32x32x16_bf16 v[16:31], v[64:67], v[80:83], -4.0\n"
    "v_exp_f32 v38, v38\n"
    "v_exp_f32 v39, v39\n"
    "v_exp_f32 v40, v40\n"
    "v_exp_f32 v41, v41\n"
    "v_exp_f32 v42, v42\n"
    "v_smfmac_f32_32x32x32_bf16 v[48:63], v[68:71], v[32:39], v15\n"
    "v_add_u32 v13, 66560, v13\n"
    "ds_read_b128 v[68:71], v13 offset:0\n"
    "v_exp_f32 v43, v43\n"
    "v_exp_f32 v44, v44\n"
    "v_exp_f32 v45, v45\n"
    "v_exp_f32 v46, v46\n"
    "v_exp_f32 v47, v47\n"
    "v_exp_f32 v16, v16\n"
    "v_exp_f32 v17, v17\n"
    "v_exp_f32 v18, v18\n"
    "v_smfmac_f32_32x32x32_bf16 v[48:63], v[76:79], v[40:47], v15\n"
    "ds_read_b128 v[76:79], v14 offset:62400\n"
    "v_exp_f32 v19, v19\n"
    "v_exp_f32 v20, v20\n"
    "v_exp_f32 v21, v21\n"
    "v_mfma_f32_32x32x16_bf16 v[32:47], v[64:67], v[8:11], -4.0\n"
    "v_add_u32 v12, 66560, v12\n"
    "ds_read_b128 v[64:67], v12 offset:0\n"
    "v_exp_f32 v22, v22\n"
    "v_exp_f32 v23, v23\n"
    "v_exp_f32 v24, v24\n"
    "v_exp_f32 v25, v25\n"
    "v_exp_f32 v26, v26\n"
    "v_smfmac_f32_32x32x32_bf16 v[84:99], v[72:75], v[16:23], v15\n"
    "v_exp_f32 v27, v27\n"
    "v_exp_f32 v28, v28\n"
    "v_exp_f32 v29, v29\n"
    "v_exp_f32 v30, v30\n"
    "v_exp_f32 v31, v31\n"
    "v_exp_f32 v32, v32\n"
    "v_exp_f32 v33, v33\n"
    "v_exp_f32 v34, v34\n"
    "s_waitcnt lgkmcnt(0)\n"
    "v_smfmac_f32_32x32x32_bf16 v[84:99], v[76:79], v[24:31], v15\n"
    "v_exp_f32 v35, v35\n"
    "v_exp_f32 v36, v36\n"
    "v_exp_f32 v37, v37\n"
    "v_mfma_f32_32x32x16_bf16 v[16:31], v[64:67], v[80:83], -4.0\n"
    "v_exp_f32 v38, v38\n"
    "v_exp_f32 v39, v39\n"
    "v_exp_f32 v40, v40\n"
    "v_exp_f32 v41, v41\n"
    "v_exp_f32 v42, v42\n"
    "v_smfmac_f32_32x32x32_bf16 v[48:63], v[72:75], v[32:39], v15\n"
    "ds_read_b128 v[72:75], v13 offset:4160\n"
    "v_exp_f32 v43, v43\n"
    "v_exp_f32 v44, v44\n"
    "v_exp_f32 v45, v45\n"
    "v_exp_f32 v46, v46\n"
    "v_exp_f32 v47, v47\n"
    "v_exp_f32 v16, v16\n"
    "v_exp_f32 v17, v17\n"
    "v_exp_f32 v18, v18\n"
    "v_smfmac_f32_32x32x32_bf16 v[48:63], v[76:79], v[40:47], v15\n"
    "v_add_u32 v14, 66560, v14\n"
    "ds_read_b128 v[76:79], v14 offset:0\n"
    "v_exp_f32 v19, v19\n"
    "v_exp_f32 v20, v20\n"
    "v_exp_f32 v21, v21\n"
    "v_mfma_f32_32x32x16_bf16 v[32:47], v[64:67], v[8:11], -4.0\n"
    "ds_read_b128 v[64:67], v12 offset:4160\n"
    "v_exp_f32 v22, v22\n"
    "v_exp_f32 v23, v23\n"
    "v_exp_f32 v24, v24\n"
    "v_exp_f32 v25, v25\n"
    "v_exp_f32 v26, v26\n"
    "v_smfmac_f32_32x32x32_bf16 v[84:99], v[68:71], v[16:23], v15\n"
    "v_exp_f32 v27, v27\n"
    "v_exp_f32 v28, v28\n"
    "v_exp_f32 v29, v29\n"
    "v_exp_f32 v30, v30\n"
    "v_exp_f32 v31, v31\n"
    "v_exp_f32 v32, v32\n"
    "v_exp_f32 v33, v33\n"
    "v_exp_f32 v34, v34\n"
    "s_waitcnt lgkmcnt(0)\n"
    "v_smfmac_f32_32x32x32_bf16 v[84:99], v[76:79], v[24:31], v15\n"
    "v_exp_f32 v35, v35\n"
    "v_exp_f32 v36, v36\n"
    "v_exp_f32 v37, v37\n"
    "v_mfma_f32_32x32x16_bf16 v[16:31], v[64:67], v[80:83], -4.0\n"
    "v_exp_f32 v38, v38\n"
    "v_exp_f32 v39, v39\n"
    "v_exp_f32 v40, v40\n"
    "v_exp_f32 v41, v41\n"
    "v_exp_f32 v42, v42\n"
    "v_smfmac_f32_32x32x32_bf16 v[48:63], v[68:71], v[32:39], v15\n"
    "ds_read_b128 v[68:71], v13 offset:8320\n"
    "v_exp_f32 v43, v43\n"
    "v_exp_f32 v44, v44\n"
    "v_exp_f32 v45, v45\n"
    "v_exp_f32 v46, v46\n"
    "v_exp_f32 v47, v47\n"
    "v_exp_f32 v16, v16\n"
    "v_exp_f32 v17, v17\n"
    "v_exp_f32 v18, v18\n"
    "v_smfmac_f32_32x32x32_bf16 v[48:63], v[76:79], v[40:47], v15\n"
    "ds_read_b128 v[76:79], v14 offset:4160\n"
    "v_exp_f32 v19, v19\n"
    "v_exp_f32 v20, v20\n"
    "v_exp_f32 v21, v21\n"
    "v_mfma_f32_32x32x16_bf16 v[32:47], v[64:67], v[8:11], -4.0\n"
    "ds_read_b128 v[64:67], v12 offset:8320\n"
    "v_exp_f32 v22, v22\n"
    "v_exp_f32 v23, v23\n"
    "v_exp_f32 v24, v24\n"
    "v_exp_f32 v25, v25\n"
    "v_exp_f32 v26, v26\n"
    "v_smfmac_f32_32x32x32_bf16 v[84:99], v[72:75], v[16:23], v15\n"
    "v_exp_f32 v27, v27\n"
    "v_exp_f32 v28, v28\n"
    "v_exp_f32 v29, v29\n"
    "v_exp_f32 v30, v30\n"
    "v_exp_f32 v31, v31\n"
    "v_exp_f32 v32, v32\n"
    "v_exp_f32 v33, v33\n"
    "v_exp_f32 v34, v34\n"
    "s_waitcnt lgkmcnt(0)\n"
    "v_smfmac_f32_32x32x32_bf16 v[84:99], v[76:79], v[24:31], v15\n"
    "v_exp_f32 v35, v35\n"
    "v_exp_f32 v36, v36\n"
    "v_exp_f32 v37, v37\n"
    "v_mfma_f32_32x32x16_bf16 v[16:31], v[64:67], v[80:83], -4.0\n"
    "v_exp_f32 v38, v38\n"
    "v_exp_f32 v39, v39\n"
    "v_exp_f32 v40, v40\n"
    "v_exp_f32 v41, v41\n"
    "v_exp_f32 v42, v42\n"
    "v_smfmac_f32_32x32x32_bf16 v[48:63], v[72:75], v[32:39], v15\n"
    "ds_read_b128 v[72:75], v13 offset:12480\n"
    "v_exp_f32 v43, v43\n"
    "v_exp_f32 v44, v44\n"
    "v_exp_f32 v45, v45\n"
    "v_exp_f32 v46, v46\n"
    "v_exp_f32 v47, v47\n"
    "ds_write_b8 v2, v2 offset:48\n"
    "v_exp_f32 v16, v16\n"
    "v_exp_f32 v17, v17\n"
    "v_exp_f32 v18, v18\n"
    "v_smfmac_f32_32x32x32_bf16 v[48:63], v[76:79], v[40:47], v15\n"
    "ds_read_b128 v[76:79], v14 offset:8320\n"
    "v_exp_f32 v19, v19\n"
    "v_exp_f32 v20, v20\n"
    "v_exp_f32 v21, v21\n"
    "v_mfma_f32_32x32x16_bf16 v[32:47], v[64:67], v[8:11], -4.0\n"
    "ds_read_b128 v[64:67], v12 offset:12480\n"
    "v_exp_f32 v22, v22\n"
    "v_exp_f32 v23, v23\n"
    "v_exp_f32 v24, v24\n"
    "v_exp_f32 v25, v25\n"
    "v_exp_f32 v26, v26\n"
    "v_smfmac_f32_32x32x32_bf16 v[84:99], v[68:71], v[16:23], v15\n"
    "v_exp_f32 v27, v27\n"
    "v_exp_f32 v28, v28\n"
    "v_exp_f32 v29, v29\n"
    "v_exp_f32 v30, v30\n"
    "v_exp_f32 v31, v31\n"
    "v_exp_f32 v32, v32\n"
    "v_exp_f32 v33, v33\n"
    "v_exp_f32 v34, v34\n"
    "s_waitcnt lgkmcnt(0)\n"
    "v_smfmac_f32_32x32x32_bf16 v[84:99], v[76:79], v[24:31], v15\n"
    "v_exp_f32 v35, v35\n"
    "v_exp_f32 v36, v36\n"
    "v_exp_f32 v37, v37\n"
    "v_mfma_f32_32x32x16_bf16 v[16:31], v[64:67], v[80:83], -4.0\n"
    "v_exp_f32 v38, v38\n"
    "v_exp_f32 v39, v39\n"
    "v_exp_f32 v40, v40\n"
    "v_exp_f32 v41, v41\n"
    "v_exp_f32 v42, v42\n"
    "v_smfmac_f32_32x32x32_bf16 v[48:63], v[68:71], v[32:39], v15\n"
    "ds_read_b128 v[68:71], v13 offset:16640\n"
    "v_exp_f32 v43, v43\n"
    "v_exp_f32 v44, v44\n"
    "v_exp_f32 v45, v45\n"
    "v_exp_f32 v46, v46\n"
    "v_exp_f32 v47, v47\n"
    "v_exp_f32 v16, v16\n"
    "v_exp_f32 v17, v17\n"
    "v_exp_f32 v18, v18\n"
    "v_smfmac_f32_32x32x32_bf16 v[48:63], v[76:79], v[40:47], v15\n"
    "ds_read_b128 v[76:79], v14 offset:12480\n"
    "v_exp_f32 v19, v19\n"
    "v_exp_f32 v20, v20\n"
    "v_exp_f32 v21, v21\n"
    "v_mfma_f32_32x32x16_bf16 v[32:47], v[64:67], v[8:11], -4.0\n"
    "ds_read_b128 v[64:67], v12 offset:16640\n"
    "v_exp_f32 v22, v22\n"
    "v_exp_f32 v23, v23\n"
    "v_exp_f32 v24, v24\n"
    "v_exp_f32 v25, v25\n"
    "v_exp_f32 v26, v26\n"
    "v_smfmac_f32_32x32x32_bf16 v[84:99], v[72:75], v[16:23], v15\n"
    "v_exp_f32 v27, v27\n"
    "v_exp_f32 v28, v28\n"
    "v_exp_f32 v29, v29\n"
    "v_exp_f32 v30, v30\n"
    "v_exp_f32 v31, v31\n"
    "v_exp_f32 v32, v32\n"
    "v_exp_f32 v33, v33\n"
    "v_exp_f32 v34, v34\n"
    "s_waitcnt lgkmcnt(0)\n"
    "v_smfmac_f32_32x32x32_bf16 v[84:99], v[76:79], v[24:31], v15\n"
    "v_exp_f32 v35, v35\n"
    "v_exp_f32 v36, v36\n"
    "v_exp_f32 v37, v37\n"
    "v_mfma_f32_32x32x16_bf16 v[16:31], v[64:67], v[80:83], -4.0\n"
    "v_exp_f32 v38, v38\n"
    "v_exp_f32 v39, v39\n"
    "v_exp_f32 v40, v40\n"
    "v_exp_f32 v41, v41\n"
    "v_exp_f32 v42, v42\n"
    "v_smfmac_f32_32x32x32_bf16 v[48:63], v[72:75], v[32:39], v15\n"
    "ds_read_b128 v[72:75], v13 offset:20800\n"
    "v_exp_f32 v43, v43\n"
    "v_exp_f32 v44, v44\n"
    "v_exp_f32 v45, v45\n"
    "v_exp_f32 v46, v46\n"
    "v_exp_f32 v47, v47\n"
    "v_exp_f32 v16, v16\n"
    "v_exp_f32 v17, v17\n"
    "v_exp_f32 v18, v18\n"
    "v_smfmac_f32_32x32x32_bf16 v[48:63], v[76:79], v[40:47], v15\n"
    "ds_read_b128 v[76:79], v14 offset:16640\n"
    "v_exp_f32 v19, v19\n"
    "v_exp_f32 v20, v20\n"
    "v_exp_f32 v21, v21\n"
    "v_mfma_f32_32x32x16_bf16 v[32:47], v[64:67], v[8:11], -4.0\n"
    "ds_read_b128 v[64:67], v12 offset:20800\n"
    "v_exp_f32 v22, v22\n"
    "v_exp_f32 v23, v23\n"
    "v_exp_f32 v24, v24\n"
    "v_exp_f32 v25, v25\n"
    "v_exp_f32 v26, v26\n"
    "v_smfmac_f32_32x32x32_bf16 v[84:99], v[68:71], v[16:23], v15\n"
    "v_exp_f32 v27, v27\n"
    "v_exp_f32 v28, v28\n"
    "v_exp_f32 v29, v29\n"
    "v_exp_f32 v30, v30\n"
    "v_exp_f32 v31, v31\n"
    "v_exp_f32 v32, v32\n"
    "v_exp_f32 v33, v33\n"
    "v_exp_f32 v34, v34\n"
    "s_waitcnt lgkmcnt(0)\n"
    "v_smfmac_f32_32x32x32_bf16 v[84:99], v[76:79], v[24:31], v15\n"
    "v_exp_f32 v35, v35\n"
    "v_exp_f32 v36, v36\n"
    "v_exp_f32 v37, v37\n"
    "v_mfma_f32_32x32x16_bf16 v[16:31], v[64:67], v[80:83], -4.0\n"
    "v_exp_f32 v38, v38\n"
    "v_exp_f32 v39, v39\n"
    "v_exp_f32 v40, v40\n"
    "v_exp_f32 v41, v41\n"
    "v_exp_f32 v42, v42\n"
    "v_smfmac_f32_32x32x32_bf16 v[48:63], v[68:71], v[32:39], v15\n"
    "ds_read_b128 v[68:71], v13 offset:24960\n"
    "v_exp_f32 v43, v43\n"
    "v_exp_f32 v44, v44\n"
    "v_exp_f32 v45, v45\n"
    "v_exp_f32 v46, v46\n"
    "v_exp_f32 v47, v47\n"
    "v_exp_f32 v16, v16\n"
    "v_exp_f32 v17, v17\n"
    "v_exp_f32 v18, v18\n"
    "v_smfmac_f32_32x32x32_bf16 v[48:63], v[76:79], v[40:47], v15\n"
    "ds_read_b128 v[76:79], v14 offset:20800\n"
    "v_exp_f32 v19, v19\n"
    "v_exp_f32 v20, v20\n"
    "v_exp_f32 v21, v21\n"
    "v_mfma_f32_32x32x16_bf16 v[32:47], v[64:67], v[8:11], -4.0\n"
    "ds_read_b128 v[64:67], v12 offset:24960\n"
    "v_exp_f32 v22, v22\n"
    "v_exp_f32 v23, v23\n"
    "v_exp_f32 v24, v24\n"
    "v_exp_f32 v25, v25\n"
    "v_exp_f32 v26, v26\n"
    "v_smfmac_f32_32x32x32_bf16 v[84:99], v[72:75], v[16:23], v15\n"
    "v_exp_f32 v27, v27\n"
    "v_exp_f32 v28, v28\n"
    "v_exp_f32 v29, v29\n"
    "v_exp_f32 v30, v30\n"
    "v_exp_f32 v31, v31\n"
    "v_exp_f32 v32, v32\n"
    "v_exp_f32 v33, v33\n"
    "v_exp_f32 v34, v34\n"
    "s_waitcnt lgkmcnt(0)\n"
    "v_smfmac_f32_32x32x32_bf16 v[84:99], v[76:79], v[24:31], v15\n"
    "v_exp_f32 v35, v35\n"
    "v_exp_f32 v36, v36\n"
    "v_exp_f32 v37, v37\n"
    "v_mfma_f32_32x32x16_bf16 v[16:31], v[64:67], v[80:83], -4.0\n"
    "v_exp_f32 v38, v38\n"
    "v_exp_f32 v39, v39\n"
    "v_exp_f32 v40, v40\n"
    "v_exp_f32 v41, v41\n"
    "v_exp_f32 v42, v42\n"
    "v_smfmac_f32_32x32x32_bf16 v[48:63], v[72:75], v[32:39], v15\n"
    "ds_read_b128 v[72:75], v13 offset:29120\n"
    "v_exp_f32 v43, v43\n"
    "v_exp_f32 v44, v44\n"
    "v_exp_f32 v45, v45\n"
    "v_exp_f32 v46, v46\n"
    "v_exp_f32 v47, v47\n"
    "v_exp_f32 v16, v16\n"
    "v_exp_f32 v17, v17\n"
    "v_exp_f32 v18, v18\n"
    "v_smfmac_f32_32x32x32_bf16 v[48:63], v[76:79], v[40:47], v15\n"
    "ds_read_b128 v[76:79], v14 offset:24960\n"
    "v_exp_f32 v19, v19\n"
    "v_exp_f32 v20, v20\n"
    "v_exp_f32 v21, v21\n"
    "v_mfma_f32_32x32x16_bf16 v[32:47], v[64:67], v[8:11], -4.0\n"
    "ds_read_b128 v[64:67], v12 offset:29120\n"
    "v_exp_f32 v22, v22\n"
    "v_exp_f32 v23, v23\n"
    "v_exp_f32 v24, v24\n"
    "v_exp_f32 v25, v25\n"
    "v_exp_f32 v26, v26\n"
    "v_smfmac_f32_32x32x32_bf16 v[84:99], v[68:71], v[16:23], v15\n"
    "v_exp_f32 v27, v27\n"
    "v_exp_f32 v28, v28\n"
    "v_exp_f32 v29, v29\n"
    "v_exp_f32 v30, v30\n"
    "v_exp_f32 v31, v31\n"
    "v_exp_f32 v32, v32\n"
    "v_exp_f32 v33, v33\n"
    "v_exp_f32 v34, v34\n"
    "s_waitcnt lgkmcnt(0)\n"
    "v_smfmac_f32_32x32x32_bf16 v[84:99], v[76:79], v[24:31], v15\n"
    "v_exp_f32 v35, v35\n"
    "v_exp_f32 v36, v36\n"
    "v_exp_f32 v37, v37\n"
    "v_mfma_f32_32x32x16_bf16 v[16:31], v[64:67], v[80:83], -4.0\n"
    "v_exp_f32 v38, v38\n"
    "v_exp_f32 v39, v39\n"
    "v_exp_f32 v40, v40\n"
    "v_exp_f32 v41, v41\n"
    "v_exp_f32 v42, v42\n"
    "v_smfmac_f32_32x32x32_bf16 v[48:63], v[68:71], v[32:39], v15\n"
    "ds_read_b128 v[68:71], v13 offset:33280\n"
    "v_exp_f32 v43, v43\n"
    "v_exp_f32 v44, v44\n"
    "v_exp_f32 v45, v45\n"
    "v_exp_f32 v46, v46\n"
    "v_exp_f32 v47, v47\n"
    "v_exp_f32 v16, v16\n"
    "v_exp_f32 v17, v17\n"
    "v_exp_f32 v18, v18\n"
    "v_smfmac_f32_32x32x32_bf16 v[48:63], v[76:79], v[40:47], v15\n"
    "ds_read_b128 v[76:79], v14 offset:29120\n"
    "v_exp_f32 v19, v19\n"
    "v_exp_f32 v20, v20\n"
    "v_exp_f32 v21, v21\n"
    "v_mfma_f32_32x32x16_bf16 v[32:47], v[64:67], v[8:11], -4.0\n"
    "ds_read_b128 v[64:67], v12 offset:33280\n"
    "v_exp_f32 v22, v22\n"
    "v_exp_f32 v23, v23\n"
    "v_exp_f32 v24, v24\n"
    "v_exp_f32 v25, v25\n"
    "v_exp_f32 v26, v26\n"
    "v_smfmac_f32_32x32x32_bf16 v[84:99], v[72:75], v[16:23], v15\n"
    "v_exp_f32 v27, v27\n"
    "v_exp_f32 v28, v28\n"
    "v_exp_f32 v29, v29\n"
    "v_exp_f32 v30, v30\n"
    "v_exp_f32 v31, v31\n"
    "v_exp_f32 v32, v32\n"
    "v_exp_f32 v33, v33\n"
    "v_exp_f32 v34, v34\n"
    "s_waitcnt lgkmcnt(0)\n"
    "v_smfmac_f32_32x32x32_bf16 v[84:99], v[76:79], v[24:31], v15\n"
    "v_exp_f32 v35, v35\n"
    "v_exp_f32 v36, v36\n"
    "v_exp_f32 v37, v37\n"
    "v_mfma_f32_32x32x16_bf16 v[16:31], v[64:67], v[80:83], -4.0\n"
    "v_exp_f32 v38, v38\n"
    "v_exp_f32 v39, v39\n"
    "v_exp_f32 v40, v40\n"
    "v_exp_f32 v41, v41\n"
    "v_exp_f32 v42, v42\n"
    "v_smfmac_f32_32x32x32_bf16 v[48:63], v[72:75], v[32:39], v15\n"
    "ds_read_b128 v[72:75], v13 offset:37440\n"
    "v_exp_f32 v43, v43\n"
    "v_exp_f32 v44, v44\n"
    "v_exp_f32 v45, v45\n"
    "v_exp_f32 v46, v46\n"
    "v_exp_f32 v47, v47\n"
    "v_exp_f32 v16, v16\n"
    "v_exp_f32 v17, v17\n"
    "v_exp_f32 v18, v18\n"
    "v_smfmac_f32_32x32x32_bf16 v[48:63], v[76:79], v[40:47], v15\n"
    "ds_read_b128 v[76:79], v14 offset:33280\n"
    "v_exp_f32 v19, v19\n"
    "v_exp_f32 v20, v20\n"
    "v_exp_f32 v21, v21\n"
    "v_mfma_f32_32x32x16_bf16 v[32:47], v[64:67], v[8:11], -4.0\n"
    "ds_read_b128 v[64:67], v12 offset:37440\n"
    "v_exp_f32 v22, v22\n"
    "v_exp_f32 v23, v23\n"
    "v_exp_f32 v24, v24\n"
    "v_exp_f32 v25, v25\n"
    "v_exp_f32 v26, v26\n"
    "v_smfmac_f32_32x32x32_bf16 v[84:99], v[68:71], v[16:23], v15\n"
    "v_exp_f32 v27, v27\n"
    "v_exp_f32 v28, v28\n"
    "v_exp_f32 v29, v29\n"
    "v_exp_f32 v30, v30\n"
    "v_exp_f32 v31, v31\n"
    "v_exp_f32 v32, v32\n"
    "v_exp_f32 v33, v33\n"
    "v_exp_f32 v34, v34\n"
    "s_waitcnt lgkmcnt(0)\n"
    "v_smfmac_f32_32x32x32_bf16 v[84:99], v[76:79], v[24:31], v15\n"
    "v_exp_f32 v35, v35\n"
    "v_exp_f32 v36, v36\n"
    "v_exp_f32 v37, v37\n"
    "v_mfma_f32_32x32x16_bf16 v[16:31], v[64:67], v[80:83], -4.0\n"
    "v_exp_f32 v38, v38\n"
    "v_exp_f32 v39, v39\n"
    "v_exp_f32 v40, v40\n"
    "v_exp_f32 v41, v41\n"
    "v_exp_f32 v42, v42\n"
    "v_smfmac_f32_32x32x32_bf16 v[48:63], v[68:71], v[32:39], v15\n"
    "ds_read_b128 v[68:71], v13 offset:41600\n"
    "v_exp_f32 v43, v43\n"
    "v_exp_f32 v44, v44\n"
    "v_exp_f32 v45, v45\n"
    "v_exp_f32 v46, v46\n"
    "v_exp_f32 v47, v47\n"
    "v_exp_f32 v16, v16\n"
    "v_exp_f32 v17, v17\n"
    "v_exp_f32 v18, v18\n"
    "v_smfmac_f32_32x32x32_bf16 v[48:63], v[76:79], v[40:47], v15\n"
    "ds_read_b128 v[76:79], v14 offset:37440\n"
    "v_exp_f32 v19, v19\n"
    "v_exp_f32 v20, v20\n"
    "v_exp_f32 v21, v21\n"
    "v_mfma_f32_32x32x16_bf16 v[32:47], v[64:67], v[8:11], -4.0\n"
    "ds_read_b128 v[64:67], v12 offset:41600\n"
    "v_exp_f32 v22, v22\n"
    "v_exp_f32 v23, v23\n"
    "v_exp_f32 v24, v24\n"
    "v_exp_f32 v25, v25\n"
    "v_exp_f32 v26, v26\n"
    "v_smfmac_f32_32x32x32_bf16 v[84:99], v[72:75], v[16:23], v15\n"
    "v_exp_f32 v27, v27\n"
    "v_exp_f32 v28, v28\n"
    "v_exp_f32 v29, v29\n"
    "v_exp_f32 v30, v30\n"
    "v_exp_f32 v31, v31\n"
    "v_exp_f32 v32, v32\n"
    "v_exp_f32 v33, v33\n"
    "v_exp_f32 v34, v34\n"
    "s_waitcnt lgkmcnt(0)\n"
    "v_smfmac_f32_32x32x32_bf16 v[84:99], v[76:79], v[24:31], v15\n"
    "v_exp_f32 v35, v35\n"
    "v_exp_f32 v36, v36\n"
    "v_exp_f32 v37, v37\n"
    "v_mfma_f32_32x32x16_bf16 v[16:31], v[64:67], v[80:83], -4.0\n"
    "v_exp_f32 v38, v38\n"
    "v_exp_f32 v39, v39\n"
    "v_exp_f32 v40, v40\n"
    "v_exp_f32 v41, v41\n"
    "v_exp_f32 v42, v42\n"
    "v_smfmac_f32_32x32x32_bf16 v[48:63], v[72:75], v[32:39], v15\n"
    "ds_read_b128 v[72:75], v13 offset:45760\n"
    "v_exp_f32 v43, v43\n"
    "v_exp_f32 v44, v44\n"
    "v_exp_f32 v45, v45\n"
    "v_exp_f32 v46, v46\n"
    "v_exp_f32 v47, v47\n"
    "v_exp_f32 v16, v16\n"
    "v_exp_f32 v17, v17\n"
    "v_exp_f32 v18, v18\n"
    "v_smfmac_f32_32x32x32_bf16 v[48:63], v[76:79], v[40:47], v15\n"
    "ds_read_b128 v[76:79], v14 offset:41600\n"
    "v_exp_f32 v19, v19\n"
    "v_exp_f32 v20, v20\n"
    "v_exp_f32 v21, v21\n"
    "v_mfma_f32_32x32x16_bf16 v[32:47], v[64:67], v[8:11], -4.0\n"
    "ds_read_b128 v[64:67], v12 offset:45760\n"
    "v_exp_f32 v22, v22\n"
    "v_exp_f32 v23, v23\n"
    "v_exp_f32 v24, v24\n"
    "v_exp_f32 v25, v25\n"
    "v_exp_f32 v26, v26\n"
    "v_smfmac_f32_32x32x32_bf16 v[84:99], v[68:71], v[16:23], v15\n"
    "v_exp_f32 v27, v27\n"
    "v_exp_f32 v28, v28\n"
    "v_exp_f32 v29, v29\n"
    "v_exp_f32 v30, v30\n"
    "v_exp_f32 v31, v31\n"
    "v_exp_f32 v32, v32\n"
    "v_exp_f32 v33, v33\n"
    "v_exp_f32 v34, v34\n"
    "s_waitcnt lgkmcnt(0)\n"
    "v_smfmac_f32_32x32x32_bf16 v[84:99], v[76:79], v[24:31], v15\n"
    "v_exp_f32 v35, v35\n"
    "v_exp_f32 v36, v36\n"
    "v_exp_f32 v37, v37\n"
    "v_mfma_f32_32x32x16_bf16 v[16:31], v[64:67], v[80:83], -4.0\n"
    "v_exp_f32 v38, v38\n"
    "v_exp_f32 v39, v39\n"
    "v_exp_f32 v40, v40\n"
    "v_exp_f32 v41, v41\n"
    "v_exp_f32 v42, v42\n"
    "v_smfmac_f32_32x32x32_bf16 v[48:63], v[68:71], v[32:39], v15\n"
    "ds_read_b128 v[68:71], v13 offset:49920\n"
    "v_exp_f32 v43, v43\n"
    "v_exp_f32 v44, v44\n"
    "v_exp_f32 v45, v45\n"
    "v_exp_f32 v46, v46\n"
    "v_exp_f32 v47, v47\n"
    "v_exp_f32 v16, v16\n"
    "v_exp_f32 v17, v17\n"
    "v_exp_f32 v18, v18\n"
    "v_smfmac_f32_32x32x32_bf16 v[48:63], v[76:79], v[40:47], v15\n"
    "ds_read_b128 v[76:79], v14 offset:45760\n"
    "v_exp_f32 v19, v19\n"
    "v_exp_f32 v20, v20\n"
    "v_exp_f32 v21, v21\n"
    "v_mfma_f32_32x32x16_bf16 v[32:47], v[64:67], v[8:11], -4.0\n"
    "ds_read_b128 v[64:67], v12 offset:49920\n"
    "v_exp_f32 v22, v22\n"
    "v_exp_f32 v23, v23\n"
    "v_exp_f32 v24, v24\n"
    "v_exp_f32 v25, v25\n"
    "v_exp_f32 v26, v26\n"
    "v_smfmac_f32_32x32x32_bf16 v[84:99], v[72:75], v[16:23], v15\n"
    "v_exp_f32 v27, v27\n"
    "v_exp_f32 v28, v28\n"
    "v_exp_f32 v29, v29\n"
    "v_exp_f32 v30, v30\n"
    "v_exp_f32 v31, v31\n"
    "v_exp_f32 v32, v32\n"
    "v_exp_f32 v33, v33\n"
    "v_exp_f32 v34, v34\n"
    "s_waitcnt lgkmcnt(0)\n"
    "v_smfmac_f32_32x32x32_bf16 v[84:99], v[76:79], v[24:31], v15\n"
    "v_exp_f32 v35, v35\n"
    "v_exp_f32 v36, v36\n"
    "v_exp_f32 v37, v37\n"
    "v_mfma_f32_32x32x16_bf16 v[16:31], v[64:67], v[80:83], -4.0\n"
    "v_exp_f32 v38, v38\n"
    "v_exp_f32 v39, v39\n"
    "v_exp_f32 v40, v40\n"
    "v_exp_f32 v41, v41\n"
    "v_exp_f32 v42, v42\n"
    "v_smfmac_f32_32x32x32_bf16 v[48:63], v[72:75], v[32:39], v15\n"
    "ds_read_b128 v[72:75], v13 offset:54080\n"
    "v_exp_f32 v43, v43\n"
    "v_exp_f32 v44, v44\n"
    "v_exp_f32 v45, v45\n"
    "v_exp_f32 v46, v46\n"
    "v_exp_f32 v47, v47\n"
    "ds_write_b8 v2, v2 offset:64\n"
    "v_exp_f32 v16, v16\n"
    "v_exp_f32 v17, v17\n"
    "v_exp_f32 v18, v18\n"
    "v_smfmac_f32_32x32x32_bf16 v[48:63], v[76:79], v[40:47], v15\n"
    "ds_read_b128 v[76:79], v14 offset:49920\n"
    "v_exp_f32 v19, v19\n"
    "v_exp_f32 v20, v20\n"
    "v_exp_f32 v21, v21\n"
    "v_mfma_f32_32x32x16_bf16 v[32:47], v[64:67], v[8:11], -4.0\n"
    "ds_read_b128 v[64:67], v12 offset:54080\n"
    "v_exp_f32 v22, v22\n"
    "v_exp_f32 v23, v23\n"
    "v_exp_f32 v24, v24\n"
    "v_exp_f32 v25, v25\n"
    "v_exp_f32 v26, v26\n"
    "v_smfmac_f32_32x32x32_bf16 v[84:99], v[68:71], v[16:23], v15\n"
    "v_exp_f32 v27, v27\n"
    "v_exp_f32 v28, v28\n"
    "v_exp_f32 v29, v29\n"
    "v_exp_f32 v30, v30\n"
    "v_exp_f32 v31, v31\n"
    "v_exp_f32 v32, v32\n"
    "v_exp_f32 v33, v33\n"
    "v_exp_f32 v34, v34\n"
    "s_waitcnt lgkmcnt(0)\n"
    "v_smfmac_f32_32x32x32_bf16 v[84:99], v[76:79], v[24:31], v15\n"
    "v_exp_f32 v35, v35\n"
    "v_exp_f32 v36, v36\n"
    "v_exp_f32 v37, v37\n"
    "v_mfma_f32_32x32x16_bf16 v[16:31], v[64:67], v[80:83], -4.0\n"
    "v_exp_f32 v38, v38\n"
    "v_exp_f32 v39, v39\n"
    "v_exp_f32 v40, v40\n"
    "v_exp_f32 v41, v41\n"
    "v_exp_f32 v42, v42\n"
    "v_smfmac_f32_32x32x32_bf16 v[48:63], v[68:71], v[32:39], v15\n"
    "ds_read_b128 v[68:71], v13 offset:58240\n"
    "v_exp_f32 v43, v43\n"
    "v_exp_f32 v44, v44\n"
    "v_exp_f32 v45, v45\n"
    "v_exp_f32 v46, v46\n"
    "v_exp_f32 v47, v47\n"
    "v_exp_f32 v16, v16\n"
    "v_exp_f32 v17, v17\n"
    "v_exp_f32 v18, v18\n"
    "v_smfmac_f32_32x32x32_bf16 v[48:63], v[76:79], v[40:47], v15\n"
    "ds_read_b128 v[76:79], v14 offset:54080\n"
    "v_exp_f32 v19, v19\n"
    "v_exp_f32 v20, v20\n"
    "v_exp_f32 v21, v21\n"
    "v_mfma_f32_32x32x16_bf16 v[32:47], v[64:67], v[8:11], -4.0\n"
    "ds_read_b128 v[64:67], v12 offset:58240\n"
    "v_exp_f32 v22, v22\n"
    "v_exp_f32 v23, v23\n"
    "v_exp_f32 v24, v24\n"
    "v_exp_f32 v25, v25\n"
    "v_exp_f32 v26, v26\n"
    "v_smfmac_f32_32x32x32_bf16 v[84:99], v[72:75], v[16:23], v15\n"
    "v_exp_f32 v27, v27\n"
    "v_exp_f32 v28, v28\n"
    "v_exp_f32 v29, v29\n"
    "v_exp_f32 v30, v30\n"
    "v_exp_f32 v31, v31\n"
    "v_exp_f32 v32, v32\n"
    "v_exp_f32 v33, v33\n"
    "v_exp_f32 v34, v34\n"
    "s_waitcnt lgkmcnt(0)\n"
    "v_smfmac_f32_32x32x32_bf16 v[84:99], v[76:79], v[24:31], v15\n"
    "v_exp_f32 v35, v35\n"
    "v_exp_f32 v36, v36\n"
    "v_exp_f32 v37, v37\n"
    "v_mfma_f32_32x32x16_bf16 v[16:31], v[64:67], v[80:83], -4.0\n"
    "v_exp_f32 v38, v38\n"
    "v_exp_f32 v39, v39\n"
    "v_exp_f32 v40, v40\n"
    "v_exp_f32 v41, v41\n"
    "v_exp_f32 v42, v42\n"
    "v_smfmac_f32_32x32x32_bf16 v[48:63], v[72:75], v[32:39], v15\n"
    "ds_read_b128 v[72:75], v13 offset:62400\n"
    "v_exp_f32 v43, v43\n"
    "v_exp_f32 v44, v44\n"
    "v_exp_f32 v45, v45\n"
    "v_exp_f32 v46, v46\n"
    "v_exp_f32 v47, v47\n"
    "v_exp_f32 v16, v16\n"
    "v_exp_f32 v17, v17\n"
    "v_exp_f32 v18, v18\n"
    "v_smfmac_f32_32x32x32_bf16 v[48:63], v[76:79], v[40:47], v15\n"
    "ds_read_b128 v[76:79], v14 offset:58240\n"
    "v_exp_f32 v19, v19\n"
    "v_exp_f32 v20, v20\n"
    "v_exp_f32 v21, v21\n"
    "v_mfma_f32_32x32x16_bf16 v[32:47], v[64:67], v[8:11], -4.0\n"
    "ds_read_b128 v[64:67], v12 offset:62400\n"
    "v_exp_f32 v22, v22\n"
    "v_exp_f32 v23, v23\n"
    "v_exp_f32 v24, v24\n"
    "v_exp_f32 v25, v25\n"
    "v_exp_f32 v26, v26\n"
    "v_smfmac_f32_32x32x32_bf16 v[84:99], v[68:71], v[16:23], v15\n"
    "v_exp_f32 v27, v27\n"
    "v_exp_f32 v28, v28\n"
    "v_exp_f32 v29, v29\n"
    "v_exp_f32 v30, v30\n"
    "v_exp_f32 v31, v31\n"
    "v_exp_f32 v32, v32\n"
    "v_exp_f32 v33, v33\n"
    "v_exp_f32 v34, v34\n"
    "s_waitcnt lgkmcnt(0)\n"
    "v_smfmac_f32_32x32x32_bf16 v[84:99], v[76:79], v[24:31], v15\n"
    "v_exp_f32 v35, v35\n"
    "v_exp_f32 v36, v36\n"
    "v_exp_f32 v37, v37\n"
    "v_mfma_f32_32x32x16_bf16 v[16:31], v[64:67], v[80:83], -4.0\n"
    "v_exp_f32 v38, v38\n"
    "v_exp_f32 v39, v39\n"
    "v_exp_f32 v40, v40\n"
    "v_exp_f32 v41, v41\n"
    "v_exp_f32 v42, v42\n"
    "v_smfmac_f32_32x32x32_bf16 v[48:63], v[68:71], v[32:39], v15\n"
    "v_exp_f32 v43, v43\n"
    "v_exp_f32 v44, v44\n"
    "v_exp_f32 v45, v45\n"
    "v_exp_f32 v46, v46\n"
    "v_exp_f32 v47, v47\n"
    "v_mov_b32 v2, s56\n"
    "v_mov_b32 v3, s57\n"
    "v_exp_f32 v16, v16\n"
    "v_exp_f32 v17, v17\n"
    "v_exp_f32 v18, v18\n"
    "v_smfmac_f32_32x32x32_bf16 v[48:63], v[76:79], v[40:47], v15\n"
    "ds_read_b128 v[76:79], v14 offset:62400\n"
    "v_exp_f32 v19, v19\n"
    "v_exp_f32 v20, v20\n"
    "v_exp_f32 v21, v21\n"
    "v_mfma_f32_32x32x16_bf16 v[32:47], v[64:67], v[8:11], -4.0\n"
    "v_exp_f32 v22, v22\n"
    "v_exp_f32 v23, v23\n"
    "v_exp_f32 v24, v24\n"
    "v_exp_f32 v25, v25\n"
    "v_exp_f32 v26, v26\n"
    "v_smfmac_f32_32x32x32_bf16 v[84:99], v[72:75], v[16:23], v15\n"
    "v_exp_f32 v27, v27\n"
    "v_exp_f32 v28, v28\n"
    "v_exp_f32 v29, v29\n"
    "v_exp_f32 v30, v30\n"
    "v_exp_f32 v31, v31\n"
    "s_nop 0\n"
    "v_exp_f32 v32, v32\n"
    "v_exp_f32 v33, v33\n"
    "v_exp_f32 v34, v34\n"
    "s_waitcnt lgkmcnt(0)\n"
    "v_smfmac_f32_32x32x32_bf16 v[84:99], v[76:79], v[24:31], v15\n"
    "v_exp_f32 v35, v35\n"
    "v_exp_f32 v36, v36\n"
    "v_exp_f32 v37, v37\n"
    "s_xor_b32 s58, s53, 1\n"
    "s_lshl_b32 s58, s58, 10\n"
    "s_lshl1_add_u32 s59, s58, 141312\n"
    "s_add_u32 s58, s58, 159744\n"
    "v_add_u32 v12, s59, v1\n"
    "v_lshl_add_u32 v13, v0, 2, s58\n"
    "v_exp_f32 v38, v38\n"
    "v_exp_f32 v39, v39\n"
    "v_exp_f32 v40, v40\n"
    "v_exp_f32 v41, v41\n"
    "v_exp_f32 v42, v42\n"
    "v_smfmac_f32_32x32x32_bf16 v[48:63], v[72:75], v[32:39], v15\n"
    "ds_write_b128 v12, v[84:87]\n"
    "v_exp_f32 v43, v43\n"
    "v_exp_f32 v44, v44\n"
    "ds_write_b128 v12, v[88:91] offset:1024\n"
    "v_exp_f32 v45, v45\n"
    "v_exp_f32 v46, v46\n"
    "ds_write_b32 v13, v92\n"
    "v_exp_f32 v47, v47\n"
    "s_lshl_b32 s58, s18, 1\n"
    "v_subrev_u32 v13, s18, v13\n"
    "v_smfmac_f32_32x32x32_bf16 v[48:63], v[76:79], v[40:47], v15\n"
    "v_subrev_u32 v12, s58, v12\n"
    "s_waitcnt lgkmcnt(0)\n"
    "s_barrier\n"
    "ds_read_b32 v72, v13\n"
    "ds_read_b128 v[64:67], v12\n"
    "ds_read_b128 v[68:71], v12 offset:1024\n"
    "v_lshlrev_b32 v73, 2, v0\n"
    "v_add_u32 v73, 150784, v73\n"
    "ds_read2st64_b32 v[80:81], v73 offset0:0 offset1:1\n"
    "ds_read2st64_b32 v[82:83], v73 offset0:2 offset1:3\n"
    "ds_read2st64_b32 v[84:85], v73 offset0:4 offset1:5\n"
    "ds_read2st64_b32 v[86:87], v73 offset0:6 offset1:7\n"
    "ds_read2st64_b32 v[88:89], v73 offset0:8 offset1:9\n"
    "ds_read2st64_b32 v[90:91], v73 offset0:10 offset1:11\n"
    "ds_read2st64_b32 v[92:93], v73 offset0:12 offset1:13\n"
    "ds_read2st64_b32 v[94:95], v73 offset0:14 offset1:15\n"
    "ds_read2st64_b32 v[96:97], v73 offset0:16 offset1:17\n"
    "ds_read2st64_b32 v[98:99], v73 offset0:18 offset1:19\n"
    "s_waitcnt lgkmcnt(12)\n"
    "v_add_f32 v56, v56, v72\n"
    "s_waitcnt lgkmcnt(11)\n"
    "v_add_f32 v48, v48, v64\n"
    "v_add_f32 v49, v49, v65\n"
    "v_add_f32 v50, v50, v66\n"
    "v_add_f32 v51, v51, v67\n"
    "s_waitcnt lgkmcnt(10)\n"
    "v_add_f32 v52, v52, v68\n"
    "v_add_f32 v53, v53, v69\n"
    "v_add_f32 v54, v54, v70\n"
    "v_add_f32 v55, v55, v71\n"
    ".Lpad12_%=:\n"
    "s_cbranch_execz .Lhopret_%=\n"
    "; ---- safety check of the un-shifted exponentials: row sum finite and far from the f32 range ends\n"
    "v_mov_b32 v16, v56\n"
    "v_mov_b32 v17, v56\n"
    "s_nop 1\n"
    "v_permlane32_swap_b32 v16, v17\n"
    "v_max_f32 v17, 0x21800000, v16\n"
    "v_min_f32 v17, 0x59000000, v17\n"
    "v_cmp_neq_f32 vcc, v17, v16\n"
    "s_cbranch_vccnz .Lfb2_%=\n"
    ".LretB4_%=:\n"
    "v_rcp_f32 v16, v16\n"
    "s_add_u32 s59, s20, 2129920\n"
    "v_mul_f32 v48, v16, v48\n"
    "v_mul_f32 v49, v16, v49\n"
    "v_mul_f32 v50, v16, v50\n"
    "v_mul_f32 v51, v16, v51\n"
    "v_mul_f32 v52, v16, v52\n"
    "v_mul_f32 v53, v16, v53\n"
    "v_mul_f32 v54, v16, v54\n"
    "v_mul_f32 v55, v16, v55\n"
    "v_cvt_pk_bf16_f32 v48, v48, v49\n"
    "v_cvt_pk_bf16_f32 v49, v50, v51\n"
    "v_cvt_pk_bf16_f32 v50, v52, v53\n"
    "v_cvt_pk_bf16_f32 v51, v54, v55\n"
    "buffer_store_dwordx4 v[48:51], v1, s[44:47], s59 offen sc1\n"
    "s_mov_b64 exec, 1\n"
    "buffer_store_dwordx2 v[2:3], off, s[44:47], s8 sc1\n"
    "s_mov_b64 exec, -1\n"
    "s_waitcnt lgkmcnt(8)\n"
    "v_add_f32 v80, v80, v82\n"
    "v_add_f32 v81, v81, v83\n"
    "ds_read2st64_b32 v[82:83], v73 offset0:20 offset1:21\n"
    "s_waitcnt lgkmcnt(8)\n"
    "v_add_f32 v80, v80, v84\n"
    "v_add_f32 v81, v81, v85\n"
    "ds_read2st64_b32 v[84:85], v73 offset0:22 offset1:23\n"
    "s_waitcnt lgkmcnt(8)\n"
    "v_add_f32 v80, v80, v86\n"
    "v_add_f32 v81, v81, v87\n"
    "ds_read2st64_b32 v[86:87], v73 offset0:24 offset1:25\n"
    "s_waitcnt lgkmcnt(8)\n"
    "v_add_f32 v80, v80, v88\n"
    "v_add_f32 v81, v81, v89\n"
    "ds_read2st64_b32 v[88:89], v73 offset0:26 offset1:27\n"
    "s_waitcnt lgkmcnt(8)\n"
    "v_add_f32 v80, v80, v90\n"
    "v_add_f32 v81, v81, v91\n"
    "ds_read2st64_b32 v[90:91], v73 offset0:28 offset1:29\n"
    "s_waitcnt lgkmcnt(8)\n"
    "v_add_f32 v80, v80, v92\n"
    "v_add_f32 v81, v81, v93\n"
    "ds_read2st64_b32 v[92:93], v73 offset0:30 offset1:31\n"
    "s_add_u32 s60, s34, 0\n"
    "buffer_load_dwordx4 v[56:59], v1, s[44:47], s60 offen offset:0 sc1\n"
    "buffer_load_dwordx4 v[60:63], v1, s[44:47], s60 offen offset:1024 sc1\n"
    "buffer_load_dwordx4 v[64:67], v1, s[44:47], s60 offen offset:2048 sc1\n"
    "buffer_load_dwordx4 v[68:71], v1, s[44:47], s60 offen offset:3072 sc1\n"
    "s_add_u32 s60, s34, 4096\n"
    "buffer_load_dwordx4 v[72:75], v1, s[44:47], s60 offen offset:0 sc1\n"
    "buffer_load_dwordx4 v[76:79], v1, s[44:47], s60 offen offset:1024 sc1\n"
    "s_waitcnt lgkmcnt(8)\n"
    "v_add_f32 v80, v80, v94\n"
    "v_add_f32 v81, v81, v95\n"
    "s_waitcnt lgkmcnt(7)\n"
    "v_add_f32 v80, v80, v96\n"
    "v_add_f32 v81, v81, v97\n"
    "s_waitcnt lgkmcnt(6)\n"
    "v_add_f32 v80, v80, v98\n"
    "v_add_f32 v81, v81, v99\n"
    "s_waitcnt lgkmcnt(5)\n"
    "v_add_f32 v80, v80, v82\n"
    "v_add_f32 v81, v81, v83\n"
    "s_waitcnt lgkmcnt(4)\n"
    "v_add_f32 v80, v80, v84\n"
    "v_add_f32 v81, v81, v85\n"
    "s_waitcnt lgkmcnt(3)\n"
    "v_add_f32 v80, v80, v86\n"
    "v_add_f32 v81, v81, v87\n"
    "s_waitcnt lgkmcnt(2)\n"
    "v_add_f32 v80, v80, v88\n"
    "v_add_f32 v81, v81, v89\n"
    "s_waitcnt lgkmcnt(1)\n"
    "v_add_f32 v80, v80, v90\n"
    "v_add_f32 v81, v81, v91\n"
    "s_waitcnt lgkmcnt(0)\n"
    "v_add_f32 v80, v80, v92\n"
    "v_add_f32 v81, v81, v93\n"
    "v_add_f32 v80, v80, v81\n"
    "v_mul_f32 v80, 0x3b000000, v80\n"
    "v_mov_b32 v81, v80\n"
    "s_nop 1\n"
    "v_permlane32_swap_b32 v80, v81\n"
    "v_fma_f32 v81, -v80, v80, v81\n"
    "v_cmp_gt_f32 vcc, 0, v81\n"
    "v_sub_f32 v80, 0, v80\n"
    "s_nop 0\n"
    "v_cndmask_b32 v81, v81, 0, vcc\n"
    "v_cmp_gt_u32 vcc, 32, v0\n"
    "v_add_f32 v81, 0x3727c5ac, v81\n"
    "v_sqrt_f32 v82, v81\n"
    "v_rcp_f32 v12, v81\n"
    "v_cndmask_b32 v80, v82, v80, vcc\n"
    "v_cvt_pk_bf16_f32 v81, v80, v80\n"
    "v_and_b32 v81, 0xffff0000, v81\n"
    "v_sub_f32 v80, v80, v81\n"
    "v_cvt_pk_bf16_f32 v50, v81, v80\n"
    "v_cvt_pk_bf16_f32 v51, v81, v80\n"
    "v_and_b32 v10, 7, v0\n"
    "v_lshlrev_b32 v10, 7, v10\n"
    "s_lshl_b32 s58, s53, 10\n"
    "s_add_u32 s58, s58, 158976\n"
    "v_lshl_add_u32 v54, v0, 3, s58\n"
    "ds_write2_b32 v54, v50, v12 offset0:0 offset1:1\n"
    "s_waitcnt vmcnt(7)\n"
    "s_waitcnt lgkmcnt(0)\n"
    "s_barrier\n"
    "ds_read_b128 v[32:35], v1 offset:0\n"
    "ds_read_b128 v[36:39], v1 offset:1024\n"
    "ds_read_b128 v[40:43], v1 offset:2048\n"
    "v_mfma_f32_32x32x8_bf16 v[16:31], v[4:5], v[50:51], 0\n"
    ".Lhop2_%=:\n"
    ".Lpad13_%=:\n"
    "s_cbranch_execz .Lpad14_%=\n"
    "s_waitcnt lgkmcnt(2)\n"
    "v_mfma_f32_32x32x16_bf16 v[16:31], v[128:131], v[32:35], v[16:31]\n"
    "ds_read_b128 v[44:47], v1 offset:3072\n"
    "s_waitcnt lgkmcnt(2)\n"
    "v_mfma_f32_32x32x16_bf16 v[16:31], v[132:135], v[36:39], v[16:31]\n"
    "ds_read_b128 v[32:35], v1 offset:4096\n"
    "s_waitcnt lgkmcnt(2)\n"
    "v_mfma_f32_32x32x16_bf16 v[16:31], v[136:139], v[40:43], v[16:31]\n"
    "ds_read_b128 v[36:39], v1 offset:5120\n"
    "s_waitcnt lgkmcnt(2)\n"
    "v_mfma_f32_32x32x16_bf16 v[16:31], v[140:143], v[44:47], v[16:31]\n"
    "ds_read_b128 v[40:43], v1 offset:6144\n"
    "s_waitcnt lgkmcnt(2)\n"
    "v_mfma_f32_32x32x16_bf16 v[16:31], v[144:147], v[32:35], v[16:31]\n"
    "ds_read_b128 v[44:47], v1 offset:7168\n"
    "s_waitcnt lgkmcnt(2)\n"
    "v_mfma_f32_32x32x16_bf16 v[16:31], v[148:151], v[36:39], v[16:31]\n"
    "ds_read_b128 v[32:35], v1 offset:8192\n"
    "s_waitcnt lgkmcnt(2)\n"
    "v_mfma_f32_32x32x16_bf16 v[16:31], v[152:155], v[40:43], v[16:31]\n"
    "ds_read_b128 v[36:39], v1 offset:9216\n"
    "s_waitcnt lgkmcnt(2)\n"
    "v_mfma_f32_32x32x16_bf16 v[16:31], v[156:159], v[44:47], v[16:31]\n"
    "ds_read_b128 v[40:43], v1 offset:10240\n"
    "s_waitcnt lgkmcnt(2)\n"
    "v_mfma_f32_32x32x16_bf16 v[16:31], v[160:163], v[32:35], v[16:31]\n"
    "ds_read_b128 v[44:47], v1 offset:11264\n"
    "s_waitcnt lgkmcnt(2)\n"
    "v_mfma_f32_32x32x16_bf16 v[16:31], v[164:167], v[36:39], v[16:31]\n"
    "ds_read_b128 v[32:35], v1 offset:12288\n"
    "s_waitcnt lgkmcnt(2)\n"
    "v_mfma_f32_32x32x16_bf16 v[16:31], v[168:171], v[40:43], v[16:31]\n"
    "ds_read_b128 v[36:39], v1 offset:13312\n"
    "s_waitcnt lgkmcnt(2)\n"
    "v_mfma_f32_32x32x16_bf16 v[16:31], v[172:175], v[44:47], v[16:31]\n"
    "ds_read_b128 v[40:43], v1 offset:14336\n"
    "s_waitcnt lgkmcnt(2)\n"
    "v_mfma_f32_32x32x16_bf16 v[16:31], v[176:179], v[32:35], v[16:31]\n"
    "ds_read_b128 v[44:47], v1 offset:15360\n"
    "s_waitcnt lgkmcnt(2)\n"
    "v_mfma_f32_32x32x16_bf16 v[16:31], v[180:183], v[36:39], v[16:31]\n"
    "ds_read_b128 v[32:35], v1 offset:16384\n"
    "s_waitcnt lgkmcnt(2)\n"
    "v_mfma_f32_32x32x16_bf16 v[16:31], v[184:187], v[40:43], v[16:31]\n"
    "ds_read_b128 v[36:39], v1 offset:17408\n"
    "s_waitcnt lgkmcnt(2)\n"
    "v_mfma_f32_32x32x16_bf16 v[16:31], v[188:191], v[44:47], v[16:31]\n"
    "ds_read_b128 v[40:43], v1 offset:18432\n"
    "s_waitcnt lgkmcnt(2)\n"
    "v_mfma_f32_32x32x16_bf16 v[16:31], v[192:195], v[32:35], v[16:31]\n"
    "ds_read_b128 v[44:47], v1 offset:19456\n"
    "s_waitcnt lgkmcnt(2)\n"
    "v_mfma_f32_32x32x16_bf16 v[16:31], v[196:199], v[36:39], v[16:31]\n"
    "ds_read_b128 v[32:35], v1 offset:20480\n"
    "s_waitcnt lgkmcnt(2)\n"
    "v_mfma_f32_32x32x16_bf16 v[16:31], v[200:203], v[40:43], v[16:31]\n"
    "ds_read_b128 v[36:39], v1 offset:21504\n"
    "s_waitcnt lgkmcnt(2)\n"
    "v_mfma_f32_32x32x16_bf16 v[16:31], v[204:207], v[44:47], v[16:31]\n"
    "ds_read_b128 v[40:43], v1 offset:22528\n"
    "s_waitcnt lgkmcnt(2)\n"
    "v_mfma_f32_32x32x16_bf16 v[16:31], v[208:211], v[32:35], v[16:31]\n"
    "ds_read_b128 v[44:47], v1 offset:23552\n"
    "s_waitcnt lgkmcnt(2)\n"
    "v_mfma_f32_32x32x16_bf16 v[16:31], v[212:215], v[36:39], v[16:31]\n"
    "ds_read_b128 v[32:35], v1 offset:24576\n"
    "s_waitcnt lgkmcnt(2)\n"
    "v_mfma_f32_32x32x16_bf16 v[16:31], v[216:219], v[40:43], v[16:31]\n"
    "ds_read_b128 v[36:39], v1 offset:25600\n"
    "s_waitcnt lgkmcnt(2)\n"
    "v_mfma_f32_32x32x16_bf16 v[16:31], v[220:223], v[44:47], v[16:31]\n"
    "ds_read_b128 v[40:43], v1 offset:26624\n"
    "s_waitcnt lgkmcnt(2)\n"
    "v_mfma_f32_32x32x16_bf16 v[16:31], v[224:227], v[32:35], v[16:31]\n"
    "ds_read_b128 v[44:47], v1 offset:27648\n"
    "s_waitcnt lgkmcnt(2)\n"
    "v_mfma_f32_32x32x16_bf16 v[16:31], v[228:231], v[36:39], v[16:31]\n"
    "ds_read_b128 v[32:35], v1 offset:28672\n"
    "s_waitcnt lgkmcnt(2)\n"
    "v_mfma_f32_32x32x16_bf16 v[16:31], v[232:235], v[40:43], v[16:31]\n"
    "ds_read_b128 v[36:39], v1 offset:29696\n"
    "s_waitcnt lgkmcnt(2)\n"
    "v_mfma_f32_32x32x16_bf16 v[16:31], v[236:239], v[44:47], v[16:31]\n"
    "ds_read_b128 v[40:43], v1 offset:30720\n"
    "s_waitcnt lgkmcnt(2)\n"
    "v_mfma_f32_32x32x16_bf16 v[16:31], v[240:243], v[32:35], v[16:31]\n"
    "ds_read_b128 v[44:47], v1 offset:31744\n"
    "s_waitcnt lgkmcnt(2)\n"
    "v_mfma_f32_32x32x16_bf16 v[16:31], v[244:247], v[36:39], v[16:31]\n"
    "s_waitcnt lgkmcnt(1)\n"
    "v_mfma_f32_32x32x16_bf16 v[16:31], v[248:251], v[40:43], v[16:31]\n"
    "s_waitcnt lgkmcnt(0)\n"
    "v_mfma_f32_32x32x16_bf16 v[16:31], v[252:255], v[44:47], v[16:31]\n"
    "s_add_u32 s59, s21, 0\n"
    "s_mov_b64 exec, 1\n"
    "buffer_store_dwordx2 v[2:3], off, s[44:47], s59 sc1\n"
    "s_mov_b64 exec, -1\n"
    "s_barrier\n"
    "s_add_u32 s60, s34, 4096\n"
    "buffer_load_dwordx4 v[80:83], v1, s[44:47], s60 offen offset:2048 sc1\n"
    "buffer_load_dwordx4 v[84:87], v1, s[44:47], s60 offen offset:3072 sc1\n"
    "s_add_u32 s60, s34, 8192\n"
    "buffer_load_dwordx4 v[88:91], v1, s[44:47], s60 offen offset:0 sc1\n"
    "buffer_load_dwordx4 v[92:95], v1, s[44:47], s60 offen offset:1024 sc1\n"
    "buffer_load_dwordx4 v[96:99], v1, s[44:47], s60 offen offset:2048 sc1\n"
    "s_add_u32 s59, s24, 6291456\n"
    "v_or_b32 v52, s63, v1\n"
    "v_mul_f32 v16, v16, abs(v16)\n"
    "v_mul_f32 v17, v17, abs(v17)\n"
    "v_mul_f32 v18, v18, abs(v18)\n"
    "v_mul_f32 v19, v19, abs(v19)\n"
    "v_mul_f32 v20, v20, abs(v20)\n"
    "v_mul_f32 v21, v21, abs(v21)\n"
    "v_mul_f32 v22, v22, abs(v22)\n"
    "v_mul_f32 v23, v23, abs(v23)\n"
    "v_cvt_pk_bf16_f32 v32, v16, v17\n"
    "v_cvt_pk_bf16_f32 v33, v18, v19\n"
    "v_cvt_pk_bf16_f32 v34, v20, v21\n"
    ".Lpad14_%=:\n"
    "s_cbranch_execz .Lpad15_%=\n"
    "v_cvt_pk_bf16_f32 v35, v22, v23\n"
    "v_pk_max_i16 v32, v32, 0\n"
    "v_pk_max_i16 v33, v33, 0\n"
    "v_pk_max_i16 v34, v34, 0\n"
    "v_pk_max_i16 v35, v35, 0\n"
    "buffer_store_dwordx4 v[32:35], v52, s[44:47], s59 offen offset:0 sc1\n"
    "v_mul_f32 v24, v24, abs(v24)\n"
    "v_mul_f32 v25, v25, abs(v25)\n"
    "v_mul_f32 v26, v26, abs(v26)\n"
    "v_mul_f32 v27, v27, abs(v27)\n"
    "v_mul_f32 v28, v28, abs(v28)\n"
    "v_mul_f32 v29, v29, abs(v29)\n"
    "v_mul_f32 v30, v30, abs(v30)\n"
    "v_mul_f32 v31, v31, abs(v31)\n"
    "v_cvt_pk_bf16_f32 v36, v24, v25\n"
    "v_cvt_pk_bf16_f32 v37, v26, v27\n"
    "v_cvt_pk_bf16_f32 v38, v28, v29\n"
    "v_cvt_pk_bf16_f32 v39, v30, v31\n"
    "v_pk_max_i16 v36, v36, 0\n"
    "v_pk_max_i16 v37, v37, 0\n"
    "v_pk_max_i16 v38, v38, 0\n"
    "v_pk_max_i16 v39, v39, 0\n"
    "buffer_store_dwordx4 v[36:39], v52, s[44:47], s59 offen offset:1024 sc1\n"
    "s_lshl_b32 s58, s53, 7\n"
    "s_add_u32 s16, s23, s58\n"
    "s_add_u32 s16, s16, 512\n"
    "s_add_u32 s17, s11, 64\n"
    "v_mul_u32_u24 v13, 20, v10\n"
    "v_and_b32 v11, 0x300, v1\n"
    "v_lshlrev_b32 v11, 2, v11\n"
    "v_and_b32 v12, 0xf0, v1\n"
    "s_lshl_b32 s58, s67, 13\n"
    "s_lshl_b32 s61, s53, 8\n"
    "s_add_u32 s58, s58, s61\n"
    "s_add_u32 s61, s58, 532480\n"
    "v_add3_u32 v12, v11, v12, s61\n"
    "s_lshl_b32 s58, s58, 2\n"
    "s_add_u32 s58, s58, 4227072\n"
    "v_add_u32 v11, s58, v1\n"
    "s_lshl_b32 m0, s53, 11\n"
    "s_add_u32 m0, m0, 150784\n"
    "s_lshl_b32 s58, s53, 10\n"
    "s_add_u32 s58, s58, 114688\n"
    "s_lshl_b32 s61, s98, 13\n"
    "s_movk_i32 s60, 3072\n"
    "s_add_u32 s59, s10, 64\n"
    "s_waitcnt vmcnt(0)\n"
    "s_mov_b64 exec, 1\n"
    "buffer_store_dwordx2 v[2:3], off, s[44:47], s59 sc1\n"
    "s_mov_b64 exec, -1\n"
    "s_barrier\n"
    "s_sleep 5\n"
    "buffer_load_dwordx2 v[14:15], v13, s[44:47], s16 offen sc1\n"
    "s_mov_b32 s18, m0\n"
    "s_add_u32 m0, s58, 0\n"
    "s_nop 0\n"
    "buffer_load_dwordx4 v11, s[44:47], s61 offen sc1 lds\n"
    "s_add_u32 s61, s61, 4096\n"
    "s_add_u32 m0, s58, 4096\n"
    "s_nop 0\n"
    "buffer_load_dwordx4 v11, s[44:47], s61 offen sc1 lds\n"
    "s_add_u32 s61, s61, 4096\n"
    "s_and_b32 s61, s61, 0x7fff\n"
    "s_add_u32 m0, s58, 8192\n"
    "s_nop 0\n"
    "buffer_load_dwordx4 v11, s[44:47], s61 offen sc1 lds\n"
    "s_add_u32 s61, s61, 4096\n"
    "s_add_u32 m0, s58, 12288\n"
    "s_nop 0\n"
    "buffer_load_dwordx4 v11, s[44:47], s61 offen sc1 lds\n"
    "s_mov_b32 m0, s18\n"
    "s_nop 0\n"
    "buffer_load_dwordx4 v12, s[44:47], 0 offen sc1 lds\n"
    "buffer_load_dwordx4 v12, s[44:47], s60 offen offset:1024 sc1 lds\n"
    "s_add_u32 s61, s61, 4096\n"
    "s_and_b32 s61, s61, 0x7fff\n"
    "s_add_u32 m0, s58, 16384\n"
    "s_nop 0\n"
    "buffer_load_dwordx4 v11, s[44:47], s61 offen sc1 lds\n"
    "s_add_u32 s61, s61, 4096\n"
    "s_add_u32 m0, s58, 20480\n"
    "s_nop 0\n"
    "buffer_load_dwordx4 v11, s[44:47], s61 offen sc1 lds\n"
    "s_add_u32 s61, s61, 4096\n"
    "s_and_b32 s61, s61, 0x7fff\n"
    "s_add_u32 m0, s58, 24576\n"
    "s_nop 0\n"
    "buffer_load_dwordx4 v11, s[44:47], s61 offen sc1 lds\n"
    "s_add_u32 s61, s61, 4096\n"
    "s_add_u32 m0, s58, 28672\n"
    "s_nop 0\n"
    "buffer_load_dwordx4 v11, s[44:47], s61 offen sc1 lds\n"
    "buffer_load_dwordx2 v[8:9], v10, s[44:47], s17 offen sc1\n"
    "s_waitcnt vmcnt(11)\n"
    "v_cmp_ne_u64 vcc, s[56:57], v[14:15]\n"
    "s_cbranch_vccnz .Lpagain93_%=\n"
    ".Lpok92_%=:\n"
    "s_waitcnt vmcnt(5)\n"
    ".LretryB97_%=:\n"
    "s_barrier\n"
    "v_lshlrev_b32 v54, 2, v0\n"
    "v_add_u32 v54, 150784, v54\n"
    "ds_read2st64_b32 v[48:49], v54 offset0:0 offset1:1\n"
    "ds_read2st64_b32 v[16:17], v54 offset0:2 offset1:3\n"
    "ds_read2st64_b32 v[18:19], v54 offset0:4 offset1:5\n"
    "ds_read2st64_b32 v[20:21], v54 offset0:6 offset1:7\n"
    "ds_read2st64_b32 v[22:23], v54 offset0:8 offset1:9\n"
    "ds_read2st64_b32 v[24:25], v54 offset0:10 offset1:11\n"
    "ds_read2st64_b32 v[26:27], v54 offset0:12 offset1:13\n"
    "ds_read2st64_b32 v[28:29], v54 offset0:14 offset1:15\n"
    "ds_read2st64_b32 v[30:31], v54 offset0:16 offset1:17\n"
    "ds_read2st64_b32 v[32:33], v54 offset0:18 offset1:19\n"
    "ds_read2st64_b32 v[34:35], v54 offset0:20 offset1:21\n"
    "ds_read2st64_b32 v[36:37], v54 offset0:22 offset1:23\n"
    "ds_read2st64_b32 v[38:39], v54 offset0:24 offset1:25\n"
    "ds_read2st64_b32 v[40:41], v54 offset0:26 offset1:27\n"
    "ds_read2st64_b32 v[42:43], v54 offset0:28 offset1:29\n"
    "ds_read2st64_b32 v[44:45], v54 offset0:30 offset1:31\n"
    "s_waitcnt vmcnt(1)\n"
    "s_barrier\n"
    ".Lpad15_%=:\n"
    "s_cbranch_execz .Lpad16_%=\n"
    "s_waitcnt lgkmcnt(14)\n"
    "v_add_f32 v48, v48, v16\n"
    "v_add_f32 v49, v49, v17\n"
    "s_waitcnt lgkmcnt(13)\n"
    "v_add_f32 v48, v48, v18\n"
    "v_add_f32 v49, v49, v19\n"
    "s_waitcnt lgkmcnt(12)\n"
    "v_add_f32 v48, v48, v20\n"
    "v_add_f32 v49, v49, v21\n"
    "s_waitcnt lgkmcnt(11)\n"
    "v_add_f32 v48, v48, v22\n"
    "v_add_f32 v49, v49, v23\n"
    "s_waitcnt lgkmcnt(10)\n"
    "v_add_f32 v48, v48, v24\n"
    "v_add_f32 v49, v49, v25\n"
    "s_waitcnt lgkmcnt(9)\n"
    "v_add_f32 v48, v48, v26\n"
    "v_add_f32 v49, v49, v27\n"
    "s_waitcnt lgkmcnt(8)\n"
    "v_add_f32 v48, v48, v28\n"
    "v_add_f32 v49, v49, v29\n"
    "s_waitcnt lgkmcnt(7)\n"
    "v_add_f32 v48, v48, v30\n"
    "v_add_f32 v49, v49, v31\n"
    "s_waitcnt lgkmcnt(6)\n"
    "v_add_f32 v48, v48, v32\n"
    "v_add_f32 v49, v49, v33\n"
    "s_waitcnt lgkmcnt(5)\n"
    "v_add_f32 v48, v48, v34\n"
    "v_add_f32 v49, v49, v35\n"
    "s_waitcnt lgkmcnt(4)\n"
    "v_add_f32 v48, v48, v36\n"
    "v_add_f32 v49, v49, v37\n"
    "s_waitcnt lgkmcnt(3)\n"
    "v_add_f32 v48, v48, v38\n"
    "v_add_f32 v49, v49, v39\n"
    "s_waitcnt lgkmcnt(2)\n"
    "v_add_f32 v48, v48, v40\n"
    "v_add_f32 v49, v49, v41\n"
    "s_waitcnt lgkmcnt(1)\n"
    "v_add_f32 v48, v48, v42\n"
    "v_add_f32 v49, v49, v43\n"
    "s_waitcnt lgkmcnt(0)\n"
    "v_add_f32 v48, v48, v44\n"
    "v_add_f32 v49, v49, v45\n"
    "v_add_f32 v48, v48, v49\n"
    "v_mul_f32 v48, 0x3b000000, v48\n"
    "v_mov_b32 v49, v48\n"
    "s_nop 1\n"
    "v_permlane32_swap_b32 v48, v49\n"
    "v_fma_f32 v49, -v48, v48, v49\n"
    "v_cmp_gt_f32 vcc, 0, v49\n"
    "v_sub_f32 v48, 0, v48\n"
    "s_nop 0\n"
    "v_cndmask_b32 v49, v49, 0, vcc\n"
    "v_cmp_gt_u32 vcc, 32, v0\n"
    "v_add_f32 v49, 0x3727c5ac, v49\n"
    "v_sqrt_f32 v50, v49\n"
    "v_rcp_f32 v13, v49\n"
    "v_cndmask_b32 v48, v50, v48, vcc\n"
    "v_cvt_pk_bf16_f32 v49, v48, v48\n"
    "v_and_b32 v49, 0xffff0000, v49\n"
    "v_sub_f32 v48, v48, v49\n"
    "v_cvt_pk_bf16_f32 v50, v49, v48\n"
    "v_cvt_pk_bf16_f32 v51, v49, v48\n"
    "s_lshl_b32 s58, s53, 10\n"
    "s_add_u32 s58, s58, 158976\n"
    "v_lshl_add_u32 v54, v0, 3, s58\n"
    "ds_write2_b32 v54, v50, v13 offset0:0 offset1:1\n"
    "s_waitcnt vmcnt(0)\n"
    "v_cmp_ne_u64 vcc, s[56:57], v[8:9]\n"
    "s_cbranch_vccnz .Lpagain99_%=\n"
    ".Lpok98_%=:\n"
    "s_waitcnt lgkmcnt(0)\n"
    "s_barrier\n"
    "v_add_u32 v55, 114688, v1\n"
    "ds_read_b128 v[32:35], v55 offset:0\n"
    "ds_read_b128 v[36:39], v55 offset:1024\n"
    "ds_read_b128 v[40:43], v55 offset:2048\n"
    "v_mfma_f32_32x32x8_bf16 v[16:31], v[4:5], v[50:51], 0\n"
    "s_waitcnt lgkmcnt(2)\n"
    "v_mfma_f32_32x32x16_bf16 v[16:31], v[128:131], v[32:35], v[16:31]\n"
    "ds_read_b128 v[44:47], v55 offset:3072\n"
    "s_waitcnt lgkmcnt(2)\n"
    "v_mfma_f32_32x32x16_bf16 v[16:31], v[132:135], v[36:39], v[16:31]\n"
    "ds_read_b128 v[32:35], v55 offset:4096\n"
    "s_waitcnt lgkmcnt(2)\n"
    "v_mfma_f32_32x32x16_bf16 v[16:31], v[136:139], v[40:43], v[16:31]\n"
    "ds_read_b128 v[36:39], v55 offset:5120\n"
    "s_waitcnt lgkmcnt(2)\n"
    "v_mfma_f32_32x32x16_bf16 v[16:31], v[140:143], v[44:47], v[16:31]\n"
    "ds_read_b128 v[40:43], v55 offset:6144\n"
    "s_waitcnt lgkmcnt(2)\n"
    "v_mfma_f32_32x32x16_bf16 v[16:31], v[144:147], v[32:35], v[16:31]\n"
    "ds_read_b128 v[44:47], v55 offset:7168\n"
    "s_waitcnt lgkmcnt(2)\n"
    "v_mfma_f32_32x32x16_bf16 v[16:31], v[148:151], v[36:39], v[16:31]\n"
    "ds_read_b128 v[32:35], v55 offset:8192\n"
    "s_waitcnt lgkmcnt(2)\n"
    "v_mfma_f32_32x32x16_bf16 v[16:31], v[152:155], v[40:43], v[16:31]\n"
    "ds_read_b128 v[36:39], v55 offset:9216\n"
    "s_waitcnt lgkmcnt(2)\n"
    "v_mfma_f32_32x32x16_bf16 v[16:31], v[156:159], v[44:47], v[16:31]\n"
    "ds_read_b128 v[40:43], v55 offset:10240\n"
    "s_waitcnt lgkmcnt(2)\n"
    "v_mfma_f32_32x32x16_bf16 v[16:31], v[160:163], v[32:35], v[16:31]\n"
    "ds_read_b128 v[44:47], v55 offset:11264\n"
    "s_waitcnt lgkmcnt(2)\n"
    "v_mfma_f32_32x32x16_bf16 v[16:31], v[164:167], v[36:39], v[16:31]\n"
    "ds_read_b128 v[32:35], v55 offset:12288\n"
    "s_waitcnt lgkmcnt(2)\n"
    "v_mfma_f32_32x32x16_bf16 v[16:31], v[168:171], v[40:43], v[16:31]\n"
    "ds_read_b128 v[36:39], v55 offset:13312\n"
    "s_waitcnt lgkmcnt(2)\n"
    "v_mfma_f32_32x32x16_bf16 v[16:31], v[172:175], v[44:47], v[16:31]\n"
    "ds_read_b128 v[40:43], v55 offset:14336\n"
    "s_waitcnt lgkmcnt(2)\n"
    "v_mfma_f32_32x32x16_bf16 v[16:31], v[176:179], v[32:35], v[16:31]\n"
    "ds_read_b128 v[44:47], v55 offset:15360\n"
    ".Lpad16_%=:\n"
    "s_cbranch_execz .Lpad17_%=\n"
    "s_waitcnt lgkmcnt(2)\n"
    "v_mfma_f32_32x32x16_bf16 v[16:31], v[180:183], v[36:39], v[16:31]\n"
    "ds_read_b128 v[32:35], v55 offset:16384\n"
    "s_waitcnt lgkmcnt(2)\n"
    "v_mfma_f32_32x32x16_bf16 v[16:31], v[184:187], v[40:43], v[16:31]\n"
    "ds_read_b128 v[36:39], v55 offset:17408\n"
    "s_waitcnt lgkmcnt(2)\n"
    "v_mfma_f32_32x32x16_bf16 v[16:31], v[188:191], v[44:47], v[16:31]\n"
    "ds_read_b128 v[40:43], v55 offset:18432\n"
    "s_waitcnt lgkmcnt(2)\n"
    "v_mfma_f32_32x32x16_bf16 v[16:31], v[192:195], v[32:35], v[16:31]\n"
    "ds_read_b128 v[44:47], v55 offset:19456\n"
    "s_waitcnt lgkmcnt(2)\n"
    "v_mfma_f32_32x32x16_bf16 v[16:31], v[196:199], v[36:39], v[16:31]\n"
    "ds_read_b128 v[32:35], v55 offset:20480\n"
    "s_waitcnt lgkmcnt(2)\n"
    "v_mfma_f32_32x32x16_bf16 v[16:31], v[200:203], v[40:43], v[16:31]\n"
    "ds_read_b128 v[36:39], v55 offset:21504\n"
    "s_waitcnt lgkmcnt(2)\n"
    "v_mfma_f32_32x32x16_bf16 v[16:31], v[204:207], v[44:47], v[16:31]\n"
    "ds_read_b128 v[40:43], v55 offset:22528\n"
    "s_waitcnt lgkmcnt(2)\n"
    "v_mfma_f32_32x32x16_bf16 v[16:31], v[208:211], v[32:35], v[16:31]\n"
    "ds_read_b128 v[44:47], v55 offset:23552\n"
    "s_waitcnt lgkmcnt(2)\n"
    "v_mfma_f32_32x32x16_bf16 v[16:31], v[212:215], v[36:39], v[16:31]\n"
    "ds_read_b128 v[32:35], v55 offset:24576\n"
    "s_waitcnt lgkmcnt(2)\n"
    "v_mfma_f32_32x32x16_bf16 v[16:31], v[216:219], v[40:43], v[16:31]\n"
    "ds_read_b128 v[36:39], v55 offset:25600\n"
    "s_waitcnt lgkmcnt(2)\n"
    "v_mfma_f32_32x32x16_bf16 v[16:31], v[220:223], v[44:47], v[16:31]\n"
    "ds_read_b128 v[40:43], v55 offset:26624\n"
    "s_waitcnt lgkmcnt(2)\n"
    "v_mfma_f32_32x32x16_bf16 v[16:31], v[224:227], v[32:35], v[16:31]\n"
    "ds_read_b128 v[44:47], v55 offset:27648\n"
    "s_waitcnt lgkmcnt(2)\n"
    "v_mfma_f32_32x32x16_bf16 v[16:31], v[228:231], v[36:39], v[16:31]\n"
    "ds_read_b128 v[32:35], v55 offset:28672\n"
    "s_waitcnt lgkmcnt(2)\n"
    "v_mfma_f32_32x32x16_bf16 v[16:31], v[232:235], v[40:43], v[16:31]\n"
    "ds_read_b128 v[36:39], v55 offset:29696\n"
    "s_waitcnt lgkmcnt(2)\n"
    "v_mfma_f32_32x32x16_bf16 v[16:31], v[236:239], v[44:47], v[16:31]\n"
    "ds_read_b128 v[40:43], v55 offset:30720\n"
    "s_waitcnt lgkmcnt(2)\n"
    "v_mfma_f32_32x32x16_bf16 v[16:31], v[240:243], v[32:35], v[16:31]\n"
    "ds_read_b128 v[44:47], v55 offset:31744\n"
    "s_waitcnt lgkmcnt(2)\n"
    "v_mfma_f32_32x32x16_bf16 v[16:31], v[244:247], v[36:39], v[16:31]\n"
    "s_waitcnt lgkmcnt(1)\n"
    "v_mfma_f32_32x32x16_bf16 v[16:31], v[248:251], v[40:43], v[16:31]\n"
    "s_waitcnt lgkmcnt(0)\n"
    "v_mfma_f32_32x32x16_bf16 v[16:31], v[252:255], v[44:47], v[16:31]\n"
    "s_nop 11\n"
    "v_cmp_u_f32 vcc, v16, v16\n"
    "s_cbranch_vccnz .Lhslow103_%=\n"
    ".Lhcont104_%=:\n"
    "s_add_u32 s59, s24, 6406144\n"
    "v_or_b32 v52, s63, v1\n"
    "v_mul_f32 v16, v16, abs(v16)\n"
    "v_mul_f32 v17, v17, abs(v17)\n"
    "v_mul_f32 v18, v18, abs(v18)\n"
    "v_mul_f32 v19, v19, abs(v19)\n"
    "v_mul_f32 v20, v20, abs(v20)\n"
    "v_mul_f32 v21, v21, abs(v21)\n"
    "v_mul_f32 v22, v22, abs(v22)\n"
    "v_mul_f32 v23, v23, abs(v23)\n"
    "v_cvt_pk_bf16_f32 v32, v16, v17\n"
    "v_cvt_pk_bf16_f32 v33, v18, v19\n"
    "v_cvt_pk_bf16_f32 v34, v20, v21\n"
    "v_cvt_pk_bf16_f32 v35, v22, v23\n"
    "v_pk_max_i16 v32, v32, 0\n"
    "v_pk_max_i16 v33, v33, 0\n"
    "v_pk_max_i16 v34, v34, 0\n"
    "v_pk_max_i16 v35, v35, 0\n"
    "buffer_store_dwordx4 v[32:35], v52, s[44:47], s59 offen offset:0 sc1\n"
    "v_mul_f32 v24, v24, abs(v24)\n"
    "v_mul_f32 v25, v25, abs(v25)\n"
    "v_mul_f32 v26, v26, abs(v26)\n"
    "v_mul_f32 v27, v27, abs(v27)\n"
    "v_mul_f32 v28, v28, abs(v28)\n"
    "v_mul_f32 v29, v29, abs(v29)\n"
    "v_mul_f32 v30, v30, abs(v30)\n"
    "v_mul_f32 v31, v31, abs(v31)\n"
    "v_cvt_pk_bf16_f32 v36, v24, v25\n"
    "v_cvt_pk_bf16_f32 v37, v26, v27\n"
    "v_cvt_pk_bf16_f32 v38, v28, v29\n"
    "v_cvt_pk_bf16_f32 v39, v30, v31\n"
    "v_pk_max_i16 v36, v36, 0\n"
    "v_pk_max_i16 v37, v37, 0\n"
    "v_pk_max_i16 v38, v38, 0\n"
    "v_pk_max_i16 v39, v39, 0\n"
    "buffer_store_dwordx4 v[36:39], v52, s[44:47], s59 offen offset:1024 sc1\n"
    "s_mov_b64 exec, 1\n"
    "buffer_store_dwordx2 v[2:3], off, s[44:47], s10 sc1\n"
    "s_mov_b64 exec, -1\n"
    "s_setprio 0\n"
    "s_add_u32 s35, s26, 6291456\n"
    "buffer_load_dwordx4 v[128:131], v1, s[44:47], s35 offen offset:0 sc1\n"
    "buffer_load_dwordx4 v[132:135], v1, s[44:47], s35 offen offset:1024 sc1\n"
    "buffer_load_dwordx4 v[136:139], v1, s[44:47], s35 offen offset:2048 sc1\n"
    "buffer_load_dwordx4 v[140:143], v1, s[44:47], s35 offen offset:3072 sc1\n"
    "s_add_u32 s35, s26, 6295552\n"
    "buffer_load_dwordx4 v[144:147], v1, s[44:47], s35 offen offset:0 sc1\n"
    "buffer_load_dwordx4 v[148:151], v1, s[44:47], s35 offen offset:1024 sc1\n"
    "buffer_load_dwordx4 v[152:155], v1, s[44:47], s35 offen offset:2048 sc1\n"
    "buffer_load_dwordx4 v[156:159], v1, s[44:47], s35 offen offset:3072 sc1\n"
    "s_add_u32 s35, s26, 6299648\n"
    "buffer_load_dwordx4 v[160:163], v1, s[44:47], s35 offen offset:0 sc1\n"
    "buffer_load_dwordx4 v[164:167], v1, s[44:47], s35 offen offset:1024 sc1\n"
    "buffer_load_dwordx4 v[168:171], v1, s[44:47], s35 offen offset:2048 sc1\n"
    "buffer_load_dwordx4 v[172:175], v1, s[44:47], s35 offen offset:3072 sc1\n"
    "s_add_u32 s35, s26, 6303744\n"
    "buffer_load_dwordx4 v[176:179], v1, s[44:47], s35 offen offset:0 sc1\n"
    "buffer_load_dwordx4 v[180:183], v1, s[44:47], s35 offen offset:1024 sc1\n"
    "v_add_u32 v251, s33, v1\n"
    "ds_read_b128 v[184:187], v251 offset:0\n"
    "ds_read_b128 v[188:191], v251 offset:1024\n"
    ".Lpad17_%=:\n"
    "s_cbranch_execz .Lpad18_%=\n"
    "ds_read_b128 v[192:195], v251 offset:2048\n"
    "ds_read_b128 v[196:199], v251 offset:3072\n"
    "ds_read_b128 v[200:203], v251 offset:4096\n"
    "ds_read_b128 v[204:207], v251 offset:5120\n"
    "ds_read_b128 v[208:211], v251 offset:6144\n"
    "ds_read_b128 v[212:215], v251 offset:7168\n"
    "ds_read_b128 v[216:219], v251 offset:8192\n"
    "ds_read_b128 v[220:223], v251 offset:9216\n"
    "v_bfe_u32 v251, v0, 2, 2\n"
    "v_lshlrev_b32 v251, 10, v251\n"
    "v_bfe_u32 v252, v0, 1, 1\n"
    "v_lshl_add_u32 v251, v252, 9, v251\n"
    "v_and_b32 v252, 0x30, v0\n"
    "v_add3_u32 v251, v252, v251, s27\n"
    "v_and_b32 v252, 1, v0\n"
    "v_lshl_add_u32 v251, v252, 3, v251\n"
    "v_add_u32 v252, 114688, v251\n"
    "ds_read_b64 v[10:11], v252\n"
    "v_lshrrev_b32 v251, 5, v0\n"
    "v_xor_b32 v251, v251, v0\n"
    "v_and_b32 v251, 7, v251\n"
    "v_and_b32 v252, 31, v0\n"
    "v_lshl_add_u32 v252, v252, 8, s33\n"
    "v_lshl_add_u32 v224, v251, 4, v252\n"
    "v_xor_b32 v225, 32, v224\n"
    "v_xor_b32 v226, 64, v224\n"
    "v_xor_b32 v227, 0x60, v224\n"
    "v_lshrrev_b32 v251, 4, v0\n"
    "v_lshl_add_u32 v252, s49, 2, v251\n"
    "v_lshlrev_b32 v253, 2, v252\n"
    "ds_bpermute_b32 v13, v253, v13\n"
    "v_and_b32 v253, 7, v252\n"
    "v_xor_b32 v253, v253, v0\n"
    "v_and_b32 v253, 15, v253\n"
    "v_lshlrev_b32 v253, 4, v253\n"
    "v_lshl_add_u32 v230, v252, 8, v253\n"
    "v_add_u32 v231, 114688, v230\n"
    "v_and_b32 v253, 15, v0\n"
    "v_lshlrev_b32 v253, 3, v253\n"
    "v_lshl_add_u32 v250, v251, 10, v253\n"
    "v_and_b32 v249, 7, v0\n"
    "v_lshlrev_b32 v249, 7, v249\n"
    "v_lshlrev_b32 v251, 16, v6\n"
    "v_and_b32 v252, 0xffff0000, v6\n"
    "v_lshlrev_b32 v253, 16, v7\n"
    "v_and_b32 v254, 0xffff0000, v7\n"
    "s_waitcnt lgkmcnt(1)\n"
    "v_lshlrev_b32 v240, 16, v10\n"
    "v_and_b32 v241, 0xffff0000, v10\n"
    "v_lshlrev_b32 v242, 16, v11\n"
    "v_and_b32 v243, 0xffff0000, v11\n"
    "v_add_f32 v240, v240, v251\n"
    "v_add_f32 v241, v241, v252\n"
    "v_add_f32 v242, v242, v253\n"
    "v_add_f32 v243, v243, v254\n"
    "s_waitcnt vmcnt(15)\n"
    "s_barrier\n"
    "s_setprio 1\n"
    "s_waitcnt vmcnt(13)\n"
    "v_mfma_f32_32x32x16_bf16 v[16:31], v[56:59], v[128:131], 0\n"
    "v_mfma_f32_32x32x16_bf16 v[32:47], v[60:63], v[128:131], 0\n"
    "s_waitcnt vmcnt(12)\n"
    "v_mfma_f32_32x32x16_bf16 v[16:31], v[64:67], v[132:135], v[16:31]\n"
    "v_mfma_f32_32x32x16_bf16 v[32:47], v[68:71], v[132:135], v[32:47]\n"
    "s_waitcnt vmcnt(11)\n"
    "v_mfma_f32_32x32x16_bf16 v[16:31], v[72:75], v[136:139], v[16:31]\n"
    "v_mfma_f32_32x32x16_bf16 v[32:47], v[76:79], v[136:139], v[32:47]\n"
    "s_waitcnt vmcnt(10)\n"
    "v_mfma_f32_32x32x16_bf16 v[16:31], v[80:83], v[140:143], v[16:31]\n"
    "v_mfma_f32_32x32x16_bf16 v[32:47], v[84:87], v[140:143], v[32:47]\n"
    "s_waitcnt vmcnt(9)\n"
    "v_mfma_f32_32x32x16_bf16 v[16:31], v[88:91], v[144:147], v[16:31]\n"
    "v_mfma_f32_32x32x16_bf16 v[32:47], v[92:95], v[144:147], v[32:47]\n"
    "s_waitcnt vmcnt(8)\n"
    "v_mfma_f32_32x32x16_bf16 v[16:31], v[96:99], v[148:151], v[16:31]\n"
    "v_mfma_f32_32x32x16_bf16 v[32:47], v[100:103], v[148:151], v[32:47]\n"
    "s_waitcnt vmcnt(7)\n"
    "v_mfma_f32_32x32x16_bf16 v[16:31], v[104:107], v[152:155], v[16:31]\n"
    "v_mfma_f32_32x32x16_bf16 v[32:47], v[108:111], v[152:155], v[32:47]\n"
    "s_waitcnt vmcnt(6)\n"
    "v_mfma_f32_32x32x16_bf16 v[16:31], v[112:115], v[156:159], v[16:31]\n"
    "v_mfma_f32_32x32x16_bf16 v[32:47], v[116:119], v[156:159], v[32:47]\n"
    "s_waitcnt vmcnt(5)\n"
    "v_mfma_f32_32x32x16_bf16 v[16:31], v[120:123], v[160:163], v[16:31]\n"
    "v_mfma_f32_32x32x16_bf16 v[32:47], v[124:127], v[160:163], v[32:47]\n"
    "s_waitcnt vmcnt(4)\n"
    "v_mfma_f32_32x32x16_bf16 v[16:31], v[184:187], v[164:167], v[16:31]\n"
    "v_mfma_f32_32x32x16_bf16 v[32:47], v[188:191], v[164:167], v[32:47]\n"
    "s_waitcnt vmcnt(3)\n"
    "v_mfma_f32_32x32x16_bf16 v[16:31], v[192:195], v[168:171], v[16:31]\n"
    "v_mfma_f32_32x32x16_bf16 v[32:47], v[196:199], v[168:171], v[32:47]\n"
    "s_waitcnt vmcnt(2)\n"
    "v_mfma_f32_32x32x16_bf16 v[16:31], v[200:203], v[172:175], v[16:31]\n"
    "v_mfma_f32_32x32x16_bf16 v[32:47], v[204:207], v[172:175], v[32:47]\n"
    "s_waitcnt vmcnt(1)\n"
    "v_mfma_f32_32x32x16_bf16 v[16:31], v[208:211], v[176:179], v[16:31]\n"
    "v_mfma_f32_32x32x16_bf16 v[32:47], v[212:215], v[176:179], v[32:47]\n"
    "s_waitcnt vmcnt(0)\n"
    "v_mfma_f32_32x32x16_bf16 v[16:31], v[216:219], v[180:183], v[16:31]\n"
    "v_mfma_f32_32x32x16_bf16 v[32:47], v[220:223], v[180:183], v[32:47]\n"
    "buffer_load_dwordx2 v[244:245], v249, s[44:47], s11 offen sc1\n"
    "s_add_u32 s35, s26, 6406144\n"
    "buffer_load_dwordx4 v[128:131], v1, s[44:47], s35 offen offset:0 sc1\n"
    "buffer_load_dwordx4 v[132:135], v1, s[44:47], s35 offen offset:1024 sc1\n"
    "s_nop 6\n"
    "ds_write_b128 v224, v[16:19] offset:0\n"
    "buffer_load_dwordx4 v[136:139], v1, s[44:47], s35 offen offset:2048 sc1\n"
    "ds_write_b128 v225, v[20:23] offset:0\n"
    "buffer_load_dwordx4 v[140:143], v1, s[44:47], s35 offen offset:3072 sc1\n"
    "ds_write_b128 v226, v[24:27] offset:0\n"
    "s_add_u32 s35, s26, 6410240\n"
    "buffer_load_dwordx4 v[144:147], v1, s[44:47], s35 offen offset:0 sc1\n"
    "ds_write_b128 v227, v[28:31] offset:0\n"
    "buffer_load_dwordx4 v[148:151], v1, s[44:47], s35 offen offset:1024 sc1\n"
    "ds_write_b128 v224, v[32:35] offset:128\n"
    "buffer_load_dwordx4 v[152:155], v1, s[44:47], s35 offen offset:2048 sc1\n"
    "ds_write_b128 v225, v[36:39] offset:128\n"
    "buffer_load_dwordx4 v[156:159], v1, s[44:47], s35 offen offset:3072 sc1\n"
    ".Lpad18_%=:\n"
    "s_cbranch_execz .Lpad19_%=\n"
    "ds_write_b128 v226, v[40:43] offset:128\n"
    "s_add_u32 s35, s26, 6414336\n"
    "buffer_load_dwordx4 v[160:163], v1, s[44:47], s35 offen offset:0 sc1\n"
    "ds_write_b128 v227, v[44:47] offset:128\n"
    "buffer_load_dwordx4 v[164:167], v1, s[44:47], s35 offen offset:1024 sc1\n"
    "s_lshl_b32 s58, s53, 13\n"
    "s_add_u32 s58, s58, 114688\n"
    "s_sub_u32 s58, s58, s33\n"
    "v_add_u32 v224, s58, v224\n"
    "v_add_u32 v225, s58, v225\n"
    "v_add_u32 v226, s58, v226\n"
    "v_add_u32 v227, s58, v227\n"
    "s_setprio 0\n"
    "s_waitcnt lgkmcnt(0)\n"
    "s_barrier\n"
    "s_waitcnt vmcnt(10)\n"
    "v_cmp_ne_u64 vcc, s[56:57], v[244:245]\n"
    "s_cbranch_vccnz .Lpagain109_%=\n"
    ".Lpok108_%=:\n"
    "s_waitcnt vmcnt(9)\n"
    "v_mfma_f32_32x32x16_bf16 v[16:31], v[56:59], v[128:131], 0\n"
    "v_mfma_f32_32x32x16_bf16 v[32:47], v[60:63], v[128:131], 0\n"
    "buffer_load_dwordx4 v[168:171], v1, s[44:47], s35 offen offset:2048 sc1\n"
    "s_waitcnt vmcnt(9)\n"
    "v_mfma_f32_32x32x16_bf16 v[16:31], v[64:67], v[132:135], v[16:31]\n"
    "v_mfma_f32_32x32x16_bf16 v[32:47], v[68:71], v[132:135], v[32:47]\n"
    "buffer_load_dwordx4 v[172:175], v1, s[44:47], s35 offen offset:3072 sc1\n"
    "s_waitcnt vmcnt(9)\n"
    "v_mfma_f32_32x32x16_bf16 v[16:31], v[72:75], v[136:139], v[16:31]\n"
    "v_mfma_f32_32x32x16_bf16 v[32:47], v[76:79], v[136:139], v[32:47]\n"
    "s_add_u32 s35, s26, 6418432\n"
    "buffer_load_dwordx4 v[176:179], v1, s[44:47], s35 offen offset:0 sc1\n"
    "s_waitcnt vmcnt(9)\n"
    "v_mfma_f32_32x32x16_bf16 v[16:31], v[80:83], v[140:143], v[16:31]\n"
    "v_mfma_f32_32x32x16_bf16 v[32:47], v[84:87], v[140:143], v[32:47]\n"
    "buffer_load_dwordx4 v[180:183], v1, s[44:47], s35 offen offset:1024 sc1\n"
    "s_waitcnt vmcnt(9)\n"
    "v_mfma_f32_32x32x16_bf16 v[16:31], v[88:91], v[144:147], v[16:31]\n"
    "v_mfma_f32_32x32x16_bf16 v[32:47], v[92:95], v[144:147], v[32:47]\n"
    "s_add_u32 s59, s25, 64\n"
    "s_mov_b64 exec, 1\n"
    "buffer_store_dwordx2 v[2:3], off, s[44:47], s59 sc1\n"
    "s_mov_b64 exec, -1\n"
    "s_waitcnt vmcnt(9)\n"
    "v_mfma_f32_32x32x16_bf16 v[16:31], v[96:99], v[148:151], v[16:31]\n"
    "v_mfma_f32_32x32x16_bf16 v[32:47], v[100:103], v[148:151], v[32:47]\n"
    "s_waitcnt vmcnt(8)\n"
    "v_mfma_f32_32x32x16_bf16 v[16:31], v[104:107], v[152:155], v[16:31]\n"
    "v_mfma_f32_32x32x16_bf16 v[32:47], v[108:111], v[152:155], v[32:47]\n"
    "s_waitcnt vmcnt(7)\n"
    "v_mfma_f32_32x32x16_bf16 v[16:31], v[112:115], v[156:159], v[16:31]\n"
    "s_waitcnt vmcnt(6)\n"
    "v_mfma_f32_32x32x16_bf16 v[16:31], v[120:123], v[160:163], v[16:31]\n"
    "s_waitcnt vmcnt(5)\n"
    "v_mfma_f32_32x32x16_bf16 v[16:31], v[184:187], v[164:167], v[16:31]\n"
    "s_waitcnt vmcnt(4)\n"
    "v_mfma_f32_32x32x16_bf16 v[16:31], v[192:195], v[168:171], v[16:31]\n"
    "s_waitcnt vmcnt(3)\n"
    "v_mfma_f32_32x32x16_bf16 v[16:31], v[200:203], v[172:175], v[16:31]\n"
    "s_waitcnt vmcnt(2)\n"
    "v_mfma_f32_32x32x16_bf16 v[16:31], v[208:211], v[176:179], v[16:31]\n"
    "s_waitcnt vmcnt(1)\n"
    "v_mfma_f32_32x32x16_bf16 v[16:31], v[216:219], v[180:183], v[16:31]\n"
    "v_mfma_f32_32x32x16_bf16 v[32:47], v[116:119], v[156:159], v[32:47]\n"
    "v_mfma_f32_32x32x16_bf16 v[32:47], v[124:127], v[160:163], v[32:47]\n"
    "v_mfma_f32_32x32x16_bf16 v[32:47], v[188:191], v[164:167], v[32:47]\n"
    "v_mfma_f32_32x32x16_bf16 v[32:47], v[196:199], v[168:171], v[32:47]\n"
    "s_nop 7\n"
    "ds_write_b128 v224, v[16:19] offset:0\n"
    "v_mfma_f32_32x32x16_bf16 v[32:47], v[204:207], v[172:175], v[32:47]\n"
    "ds_write_b128 v225, v[20:23] offset:0\n"
    "v_mfma_f32_32x32x16_bf16 v[32:47], v[212:215], v[176:179], v[32:47]\n"
    "ds_write_b128 v226, v[24:27] offset:0\n"
    "v_mfma_f32_32x32x16_bf16 v[32:47], v[220:223], v[180:183], v[32:47]\n"
    "ds_write_b128 v227, v[28:31] offset:0\n"
    "v_cmp_u_f32 vcc, v16, v16\n"
    "s_cbranch_vccnz .Lh1slow113_%=\n"
    ".Lh1cont114_%=:\n"
    "s_nop 8\n"
    "ds_write_b128 v224, v[32:35] offset:128\n"
    "ds_write_b128 v225, v[36:39] offset:128\n"
    "ds_write_b128 v226, v[40:43] offset:128\n"
    "ds_write_b128 v227, v[44:47] offset:128\n"
    "s_waitcnt lgkmcnt(0)\n"
    "s_barrier\n"
    "ds_read_b128 v[16:19], v230 offset:0\n"
    "ds_read_b128 v[20:23], v230 offset:8192\n"
    "ds_read_b128 v[24:27], v230 offset:16384\n"
    "ds_read_b128 v[28:31], v230 offset:24576\n"
    "ds_read_b128 v[32:35], v231 offset:0\n"
    "ds_read_b128 v[36:39], v231 offset:8192\n"
    "ds_read_b128 v[40:43], v231 offset:16384\n"
    "ds_read_b128 v[44:47], v231 offset:24576\n"
    "s_add_u32 s58, s32, 32768\n"
    "s_waitcnt lgkmcnt(6)\n"
    "v_add_f32 v16, v16, v20\n"
    "v_add_f32 v17, v17, v21\n"
    "v_add_f32 v18, v18, v22\n"
    "v_add_f32 v19, v19, v23\n"
    "s_waitcnt lgkmcnt(5)\n"
    "v_add_f32 v16, v16, v24\n"
    "v_add_f32 v17, v17, v25\n"
    "v_add_f32 v18, v18, v26\n"
    "v_add_f32 v19, v19, v27\n"
    "s_waitcnt lgkmcnt(4)\n"
    "v_add_f32 v16, v16, v28\n"
    "v_add_f32 v17, v17, v29\n"
    "v_add_f32 v18, v18, v30\n"
    "v_add_f32 v19, v19, v31\n"
    "s_waitcnt lgkmcnt(3)\n"
    "v_add_f32 v16, v16, v32\n"
    "v_add_f32 v17, v17, v33\n"
    "v_add_f32 v18, v18, v34\n"
    "v_add_f32 v19, v19, v35\n"
    "s_waitcnt lgkmcnt(2)\n"
    "v_add_f32 v16, v16, v36\n"
    "v_add_f32 v17, v17, v37\n"
    "v_add_f32 v18, v18, v38\n"
    "v_add_f32 v19, v19, v39\n"
    "s_waitcnt lgkmcnt(1)\n"
    ".Lpad19_%=:\n"
    "s_cbranch_execz .Lhopret_%=\n"
    "v_add_f32 v16, v16, v40\n"
    "v_add_f32 v17, v17, v41\n"
    "v_add_f32 v18, v18, v42\n"
    "v_add_f32 v19, v19, v43\n"
    "s_waitcnt lgkmcnt(0)\n"
    "v_add_f32 v16, v16, v44\n"
    "v_add_f32 v17, v17, v45\n"
    "v_add_f32 v18, v18, v46\n"
    "v_add_f32 v19, v19, v47\n"
    "v_fmac_f32 v240, v16, v13\n"
    "v_fmac_f32 v241, v17, v13\n"
    "v_fmac_f32 v242, v18, v13\n"
    "v_fmac_f32 v243, v19, v13\n"
    "v_cvt_pk_bf16_f32 v240, v240, v241\n"
    "v_cvt_pk_bf16_f32 v241, v242, v243\n"
    "buffer_store_dwordx2 v[240:241], v250, s[40:43], s58 offen sc1\n"
    "s_endpgm\n"
    ".Lhop3_%=:\n"
    ".Lpad20_%=:\n"
    "s_cbranch_execz .Lpad21_%=\n"
    ".Lpagain13_%=:\n"
    "s_mov_b32 s62, 0x20000\n"
    "s_nop 2\n"
    ".Lpoll14_%=:\n"
    "buffer_load_dwordx2 v[16:17], v19, s[44:47], s59 offen sc1\n"
    ".Lpchk15_%=:\n"
    "s_waitcnt vmcnt(0)\n"
    "v_cmp_ne_u64 vcc, s[56:57], v[16:17]\n"
    "s_cbranch_vccz .Lpdone16_%=\n"
    "s_sleep 8\n"
    "s_sub_u32 s62, s62, 1\n"
    "s_cbranch_scc0 .Lpoll14_%=\n"
    ".Lpdone16_%=:\n"
    "s_branch .Lpok12_%=\n"
    ".Lpagain24_%=:\n"
    "s_mov_b32 s62, 0x20000\n"
    "s_nop 2\n"
    ".Lpoll25_%=:\n"
    "buffer_load_dwordx2 v[16:17], v19, s[44:47], s59 offen sc1\n"
    ".Lpchk26_%=:\n"
    "s_waitcnt vmcnt(0)\n"
    "v_cmp_ne_u64 vcc, s[56:57], v[16:17]\n"
    "s_cbranch_vccz .Lpdone27_%=\n"
    "s_sleep 8\n"
    "s_sub_u32 s62, s62, 1\n"
    "s_cbranch_scc0 .Lpoll25_%=\n"
    ".Lpdone27_%=:\n"
    "s_branch .Lpok23_%=\n"
    ".Lpagain31_%=:\n"
    "s_mov_b32 s62, 0x20000\n"
    "s_nop 2\n"
    ".Lpoll32_%=:\n"
    "buffer_load_dwordx2 v[48:49], v13, s[44:47], s16 offen sc1\n"
    ".Lpchk33_%=:\n"
    "s_waitcnt vmcnt(0)\n"
    "v_cmp_ne_u64 vcc, s[56:57], v[48:49]\n"
    "s_cbranch_vccz .Lpdone34_%=\n"
    "s_sleep 1\n"
    "s_sub_u32 s62, s62, 1\n"
    "s_cbranch_scc0 .Lpoll32_%=\n"
    ".Lpdone34_%=:\n"
    "s_add_u32 s58, s22, 2129920\n"
    "s_add_u32 s61, s58, 4096\n"
    "s_lshl_b32 m0, s53, 13\n"
    "s_add_u32 m0, m0, 114688\n"
    "s_nop 0\n"
    "buffer_load_dwordx4 v1, s[44:47], s58 offen sc1 lds\n"
    "buffer_load_dwordx4 v1, s[44:47], s58 offen offset:1024 sc1 lds\n"
    "buffer_load_dwordx4 v1, s[44:47], s58 offen offset:2048 sc1 lds\n"
    "buffer_load_dwordx4 v1, s[44:47], s58 offen offset:3072 sc1 lds\n"
    "s_add_u32 m0, m0, 4096\n"
    "s_nop 0\n"
    "buffer_load_dwordx4 v1, s[44:47], s61 offen sc1 lds\n"
    "buffer_load_dwordx4 v1, s[44:47], s61 offen offset:1024 sc1 lds\n"
    "buffer_load_dwordx4 v1, s[44:47], s61 offen offset:2048 sc1 lds\n"
    "buffer_load_dwordx4 v1, s[44:47], s61 offen offset:3072 sc1 lds\n"
    "s_waitcnt vmcnt(0)\n"
    "s_branch .Lpok30_%=\n"
    ".Loslow35_%=:\n"
    "s_waitcnt vmcnt(0) lgkmcnt(0)\n"
    "s_nop 2\n"
    "s_lshl2_add_u32 s58, s51, s53\n"
    "s_lshl_b32 s58, s58, 14\n"
    "s_add_u32 s58, s58, 17301504\n"
    "v_add_u32 v16, s58, v1\n"
    "s_lshl_b32 s61, s98, 12\n"
    "s_add_u32 s61, s61, 4096\n"
    "s_and_b32 s61, s61, 0x3fff\n"
    "buffer_load_dwordx4 v[80:83], v16, s[44:47], s61 offen offset:0 sc1\n"
    "buffer_load_dwordx4 v[84:87], v16, s[44:47], s61 offen offset:1024 sc1\n"
    "buffer_load_dwordx4 v[88:91], v16, s[44:47], s61 offen offset:2048 sc1\n"
    "buffer_load_dwordx4 v[92:95], v16, s[44:47], s61 offen offset:3072 sc1\n"
    "s_add_u32 s61, s61, 4096\n"
    "s_and_b32 s61, s61, 0x3fff\n"
    "buffer_load_dwordx4 v[96:99], v16, s[44:47], s61 offen offset:0 sc1\n"
    "buffer_load_dwordx4 v[100:103], v16, s[44:47], s61 offen offset:1024 sc1\n"
    "buffer_load_dwordx4 v[104:107], v16, s[44:47], s61 offen offset:2048 sc1\n"
    "buffer_load_dwordx4 v[108:111], v16, s[44:47], s61 offen offset:3072 sc1\n"
    "s_add_u32 s61, s61, 4096\n"
    "s_and_b32 s61, s61, 0x3fff\n"
    "buffer_load_dwordx4 v[112:115], v16, s[44:47], s61 offen offset:0 sc1\n"
    "buffer_load_dwordx4 v[116:119], v16, s[44:47], s61 offen offset:1024 sc1\n"
    "buffer_load_dwordx4 v[120:123], v16, s[44:47], s61 offen offset:2048 sc1\n"
    "buffer_load_dwordx4 v[124:127], v16, s[44:47], s61 offen offset:3072 sc1\n"
    "s_add_u32 s61, s61, 4096\n"
    "s_and_b32 s61, s61, 0x3fff\n"
    "buffer_load_dwordx4 v[64:67], v16, s[44:47], s61 offen offset:0 sc1\n"
    "buffer_load_dwordx4 v[68:71], v16, s[44:47], s61 offen offset:1024 sc1\n"
    "buffer_load_dwordx4 v[72:75], v16, s[44:47], s61 offen offset:2048 sc1\n"
    "buffer_load_dwordx4 v[76:79], v16, s[44:47], s61 offen offset:3072 sc1\n"
    "v_lshlrev_b32 v24, 16, v10\n"
    "v_and_b32 v25, 0xffff0000, v10\n"
    "v_lshlrev_b32 v26, 16, v11\n"
    "v_and_b32 v27, 0xffff0000, v11\n"
    "v_lshlrev_b32 v28, 16, v14\n"
    "v_and_b32 v29, 0xffff0000, v14\n"
    "v_lshlrev_b32 v30, 16, v15\n"
    "v_and_b32 v31, 0xffff0000, v15\n"
    "s_lshl_b32 s58, s67, 15\n"
    "s_add_u32 s58, s58, 2129920\n"
    "s_add_u32 s59, s23, 2304\n"
    "v_bfe_u32 v20, v0, 4, 1\n"
    "v_lshlrev_b32 v20, 10, v20\n"
    "v_lshrrev_b32 v21, 5, v0\n"
    "v_lshl_add_u32 v20, v21, 9, v20\n"
    "v_and_b32 v21, 0xf0, v1\n"
    "v_add3_u32 v20, v20, v21, s58\n"
    "v_bfe_u32 v17, v0, 2, 3\n"
    "v_mul_u32_u24 v17, 2560, v17\n"
    "v_and_b32 v18, 3, v0\n"
    "v_lshl_add_u32 v17, v18, 3, v17\n"
    "s_lshl_b32 s58, s98, 13\n"
    "s_mov_b32 s62, 0x20000\n"
    "s_nop 0\n"
    ".Lpoll37_%=:\n"
    "buffer_load_dwordx2 v[18:19], v17, s[44:47], s59 offen sc1\n"
    ".Lpchk38_%=:\n"
    "s_waitcnt vmcnt(0)\n"
    "v_cmp_ne_u64 vcc, s[56:57], v[18:19]\n"
    "s_cbranch_vccz .Lpdone39_%=\n"
    "s_sleep 8\n"
    "s_sub_u32 s62, s62, 1\n"
    "s_cbranch_scc0 .Lpoll37_%=\n"
    ".Lpdone39_%=:\n"
    "buffer_load_dwordx4 v[32:35], v20, s[44:47], s58 offen offset:0 sc1\n"
    "buffer_load_dwordx4 v[36:39], v20, s[44:47], s58 offen offset:256 sc1\n"
    "buffer_load_dwordx4 v[40:43], v20, s[44:47], s58 offen offset:2048 sc1\n"
    "buffer_load_dwordx4 v[44:47], v20, s[44:47], s58 offen offset:2304 sc1\n"
    "s_add_u32 s58, s58, 4096\n"
    "buffer_load_dwordx4 v[48:51], v20, s[44:47], s58 offen offset:0 sc1\n"
    "buffer_load_dwordx4 v[52:55], v20, s[44:47], s58 offen offset:256 sc1\n"
    "s_waitcnt vmcnt(5)\n"
    "v_mfma_f32_16x16x32_bf16 v[24:27], v[64:67], v[32:35], v[24:27]\n"
    "buffer_load_dwordx4 v[32:35], v20, s[44:47], s58 offen offset:2048 sc1\n"
    ".Lpad21_%=:\n"
    "s_cbranch_execz .Lpad22_%=\n"
    "s_waitcnt vmcnt(5)\n"
    "v_mfma_f32_16x16x32_bf16 v[28:31], v[64:67], v[36:39], v[28:31]\n"
    "buffer_load_dwordx4 v[36:39], v20, s[44:47], s58 offen offset:2304 sc1\n"
    "s_waitcnt vmcnt(5)\n"
    "v_mfma_f32_16x16x32_bf16 v[24:27], v[68:71], v[40:43], v[24:27]\n"
    "s_add_u32 s58, s58, 4096\n"
    "s_and_b32 s58, s58, 0x7fff\n"
    "buffer_load_dwordx4 v[40:43], v20, s[44:47], s58 offen offset:0 sc1\n"
    "s_waitcnt vmcnt(5)\n"
    "v_mfma_f32_16x16x32_bf16 v[28:31], v[68:71], v[44:47], v[28:31]\n"
    "buffer_load_dwordx4 v[44:47], v20, s[44:47], s58 offen offset:256 sc1\n"
    "s_waitcnt vmcnt(5)\n"
    "v_mfma_f32_16x16x32_bf16 v[24:27], v[72:75], v[48:51], v[24:27]\n"
    "buffer_load_dwordx4 v[48:51], v20, s[44:47], s58 offen offset:2048 sc1\n"
    "s_waitcnt vmcnt(5)\n"
    "v_mfma_f32_16x16x32_bf16 v[28:31], v[72:75], v[52:55], v[28:31]\n"
    "buffer_load_dwordx4 v[52:55], v20, s[44:47], s58 offen offset:2304 sc1\n"
    "s_waitcnt vmcnt(5)\n"
    "v_mfma_f32_16x16x32_bf16 v[24:27], v[76:79], v[32:35], v[24:27]\n"
    "s_add_u32 s58, s58, 4096\n"
    "buffer_load_dwordx4 v[32:35], v20, s[44:47], s58 offen offset:0 sc1\n"
    "s_waitcnt vmcnt(5)\n"
    "v_mfma_f32_16x16x32_bf16 v[28:31], v[76:79], v[36:39], v[28:31]\n"
    "buffer_load_dwordx4 v[36:39], v20, s[44:47], s58 offen offset:256 sc1\n"
    "s_waitcnt vmcnt(5)\n"
    "v_mfma_f32_16x16x32_bf16 v[24:27], v[80:83], v[40:43], v[24:27]\n"
    "buffer_load_dwordx4 v[40:43], v20, s[44:47], s58 offen offset:2048 sc1\n"
    "s_waitcnt vmcnt(5)\n"
    "v_mfma_f32_16x16x32_bf16 v[28:31], v[80:83], v[44:47], v[28:31]\n"
    "buffer_load_dwordx4 v[44:47], v20, s[44:47], s58 offen offset:2304 sc1\n"
    "s_waitcnt vmcnt(5)\n"
    "v_mfma_f32_16x16x32_bf16 v[24:27], v[84:87], v[48:51], v[24:27]\n"
    "s_add_u32 s58, s58, 4096\n"
    "s_and_b32 s58, s58, 0x7fff\n"
    "buffer_load_dwordx4 v[48:51], v20, s[44:47], s58 offen offset:0 sc1\n"
    "s_waitcnt vmcnt(5)\n"
    "v_mfma_f32_16x16x32_bf16 v[28:31], v[84:87], v[52:55], v[28:31]\n"
    "buffer_load_dwordx4 v[52:55], v20, s[44:47], s58 offen offset:256 sc1\n"
    "s_waitcnt vmcnt(5)\n"
    "v_mfma_f32_16x16x32_bf16 v[24:27], v[88:91], v[32:35], v[24:27]\n"
    "buffer_load_dwordx4 v[32:35], v20, s[44:47], s58 offen offset:2048 sc1\n"
    "s_waitcnt vmcnt(5)\n"
    "v_mfma_f32_16x16x32_bf16 v[28:31], v[88:91], v[36:39], v[28:31]\n"
    "buffer_load_dwordx4 v[36:39], v20, s[44:47], s58 offen offset:2304 sc1\n"
    "s_waitcnt vmcnt(5)\n"
    "v_mfma_f32_16x16x32_bf16 v[24:27], v[92:95], v[40:43], v[24:27]\n"
    "s_add_u32 s58, s58, 4096\n"
    "buffer_load_dwordx4 v[40:43], v20, s[44:47], s58 offen offset:0 sc1\n"
    "s_waitcnt vmcnt(5)\n"
    "v_mfma_f32_16x16x32_bf16 v[28:31], v[92:95], v[44:47], v[28:31]\n"
    "buffer_load_dwordx4 v[44:47], v20, s[44:47], s58 offen offset:256 sc1\n"
    "s_waitcnt vmcnt(5)\n"
    "v_mfma_f32_16x16x32_bf16 v[24:27], v[96:99], v[48:51], v[24:27]\n"
    "buffer_load_dwordx4 v[48:51], v20, s[44:47], s58 offen offset:2048 sc1\n"
    "s_waitcnt vmcnt(5)\n"
    "v_mfma_f32_16x16x32_bf16 v[28:31], v[96:99], v[52:55], v[28:31]\n"
    "buffer_load_dwordx4 v[52:55], v20, s[44:47], s58 offen offset:2304 sc1\n"
    "s_waitcnt vmcnt(5)\n"
    "v_mfma_f32_16x16x32_bf16 v[24:27], v[100:103], v[32:35], v[24:27]\n"
    "s_add_u32 s58, s58, 4096\n"
    "s_and_b32 s58, s58, 0x7fff\n"
    "buffer_load_dwordx4 v[32:35], v20, s[44:47], s58 offen offset:0 sc1\n"
    "s_waitcnt vmcnt(5)\n"
    "v_mfma_f32_16x16x32_bf16 v[28:31], v[100:103], v[36:39], v[28:31]\n"
    "buffer_load_dwordx4 v[36:39], v20, s[44:47], s58 offen offset:256 sc1\n"
    "s_waitcnt vmcnt(5)\n"
    "v_mfma_f32_16x16x32_bf16 v[24:27], v[104:107], v[40:43], v[24:27]\n"
    "buffer_load_dwordx4 v[40:43], v20, s[44:47], s58 offen offset:2048 sc1\n"
    "s_waitcnt vmcnt(5)\n"
    "v_mfma_f32_16x16x32_bf16 v[28:31], v[104:107], v[44:47], v[28:31]\n"
    "buffer_load_dwordx4 v[44:47], v20, s[44:47], s58 offen offset:2304 sc1\n"
    "s_waitcnt vmcnt(5)\n"
    "v_mfma_f32_16x16x32_bf16 v[24:27], v[108:111], v[48:51], v[24:27]\n"
    "s_add_u32 s58, s58, 4096\n"
    "buffer_load_dwordx4 v[48:51], v20, s[44:47], s58 offen offset:0 sc1\n"
    "s_waitcnt vmcnt(5)\n"
    "v_mfma_f32_16x16x32_bf16 v[28:31], v[108:111], v[52:55], v[28:31]\n"
    "buffer_load_dwordx4 v[52:55], v20, s[44:47], s58 offen offset:256 sc1\n"
    "s_waitcnt vmcnt(5)\n"
    "v_mfma_f32_16x16x32_bf16 v[24:27], v[112:115], v[32:35], v[24:27]\n"
    "buffer_load_dwordx4 v[32:35], v20, s[44:47], s58 offen offset:2048 sc1\n"
    "s_waitcnt vmcnt(5)\n"
    "v_mfma_f32_16x16x32_bf16 v[28:31], v[112:115], v[36:39], v[28:31]\n"
    "buffer_load_dwordx4 v[36:39], v20, s[44:47], s58 offen offset:2304 sc1\n"
    "s_waitcnt vmcnt(5)\n"
    "v_mfma_f32_16x16x32_bf16 v[24:27], v[116:119], v[40:43], v[24:27]\n"
    "s_waitcnt vmcnt(4)\n"
    "v_mfma_f32_16x16x32_bf16 v[28:31], v[116:119], v[44:47], v[28:31]\n"
    "s_waitcnt vmcnt(3)\n"
    "v_mfma_f32_16x16x32_bf16 v[24:27], v[120:123], v[48:51], v[24:27]\n"
    "s_waitcnt vmcnt(2)\n"
    "v_mfma_f32_16x16x32_bf16 v[28:31], v[120:123], v[52:55], v[28:31]\n"
    "s_waitcnt vmcnt(1)\n"
    "v_mfma_f32_16x16x32_bf16 v[24:27], v[124:127], v[32:35], v[24:27]\n"
    "s_waitcnt vmcnt(0)\n"
    "v_mfma_f32_16x16x32_bf16 v[28:31], v[124:127], v[36:39], v[28:31]\n"
    "s_nop 7\n"
    "s_branch .Locont36_%=\n"
    ".Lpagain42_%=:\n"
    "s_mov_b32 s62, 0x20000\n"
    "s_nop 5\n"
    ".Lpoll43_%=:\n"
    "buffer_load_dwordx2 v[48:49], v10, s[44:47], s16 offen sc1\n"
    ".Lpchk44_%=:\n"
    "s_waitcnt vmcnt(0)\n"
    "v_cmp_ne_u64 vcc, s[56:57], v[48:49]\n"
    "s_cbranch_vccz .Lpdone45_%=\n"
    "s_sleep 1\n"
    "s_sub_u32 s62, s62, 1\n"
    "s_cbranch_scc0 .Lpoll43_%=\n"
    ".Lpdone45_%=:\n"
    "s_branch .Lpok41_%=\n"
    ".Lhslow46_%=:\n"
    "s_cmp_lg_u32 s3, 0\n"
    "s_cbranch_scc1 .Lhcont47_%=\n"
    "s_mov_b32 s3, 1\n"
    "s_waitcnt vmcnt(0) lgkmcnt(0)\n"
    "s_barrier\n"
    "s_mul_i32 s58, s51, 7\n"
    "s_add_u32 s58, s58, s49\n"
    "s_lshl_b32 s58, s58, 15\n"
    "s_add_u32 s58, s58, 13631488\n"
    "s_add_u32 s58, s58, s63\n"
    ".Lpad22_%=:\n"
    "s_cbranch_execz .Lpad23_%=\n"
    "v_add_u32 v20, s58, v1\n"
    "s_lshl_b32 s35, s98, 13\n"
    "buffer_load_dwordx4 v[128:131], v20, s[44:47], s35 offen offset:0 sc1\n"
    "buffer_load_dwordx4 v[132:135], v20, s[44:47], s35 offen offset:1024 sc1\n"
    "buffer_load_dwordx4 v[136:139], v20, s[44:47], s35 offen offset:2048 sc1\n"
    "buffer_load_dwordx4 v[140:143], v20, s[44:47], s35 offen offset:3072 sc1\n"
    "s_add_u32 s35, s35, 4096\n"
    "buffer_load_dwordx4 v[144:147], v20, s[44:47], s35 offen offset:0 sc1\n"
    "buffer_load_dwordx4 v[148:151], v20, s[44:47], s35 offen offset:1024 sc1\n"
    "buffer_load_dwordx4 v[152:155], v20, s[44:47], s35 offen offset:2048 sc1\n"
    "buffer_load_dwordx4 v[156:159], v20, s[44:47], s35 offen offset:3072 sc1\n"
    "s_add_u32 s35, s35, 4096\n"
    "s_and_b32 s35, s35, 0x7fff\n"
    "buffer_load_dwordx4 v[160:163], v20, s[44:47], s35 offen offset:0 sc1\n"
    "buffer_load_dwordx4 v[164:167], v20, s[44:47], s35 offen offset:1024 sc1\n"
    "buffer_load_dwordx4 v[168:171], v20, s[44:47], s35 offen offset:2048 sc1\n"
    "buffer_load_dwordx4 v[172:175], v20, s[44:47], s35 offen offset:3072 sc1\n"
    "s_add_u32 s35, s35, 4096\n"
    "buffer_load_dwordx4 v[176:179], v20, s[44:47], s35 offen offset:0 sc1\n"
    "buffer_load_dwordx4 v[180:183], v20, s[44:47], s35 offen offset:1024 sc1\n"
    "buffer_load_dwordx4 v[184:187], v20, s[44:47], s35 offen offset:2048 sc1\n"
    "buffer_load_dwordx4 v[188:191], v20, s[44:47], s35 offen offset:3072 sc1\n"
    "s_add_u32 s35, s35, 4096\n"
    "s_and_b32 s35, s35, 0x7fff\n"
    "buffer_load_dwordx4 v[192:195], v20, s[44:47], s35 offen offset:0 sc1\n"
    "buffer_load_dwordx4 v[196:199], v20, s[44:47], s35 offen offset:1024 sc1\n"
    "buffer_load_dwordx4 v[200:203], v20, s[44:47], s35 offen offset:2048 sc1\n"
    "buffer_load_dwordx4 v[204:207], v20, s[44:47], s35 offen offset:3072 sc1\n"
    "s_add_u32 s35, s35, 4096\n"
    "buffer_load_dwordx4 v[208:211], v20, s[44:47], s35 offen offset:0 sc1\n"
    "buffer_load_dwordx4 v[212:215], v20, s[44:47], s35 offen offset:1024 sc1\n"
    "buffer_load_dwordx4 v[216:219], v20, s[44:47], s35 offen offset:2048 sc1\n"
    "buffer_load_dwordx4 v[220:223], v20, s[44:47], s35 offen offset:3072 sc1\n"
    "s_add_u32 s35, s35, 4096\n"
    "s_and_b32 s35, s35, 0x7fff\n"
    "buffer_load_dwordx4 v[224:227], v20, s[44:47], s35 offen offset:0 sc1\n"
    "buffer_load_dwordx4 v[228:231], v20, s[44:47], s35 offen offset:1024 sc1\n"
    "buffer_load_dwordx4 v[232:235], v20, s[44:47], s35 offen offset:2048 sc1\n"
    "buffer_load_dwordx4 v[236:239], v20, s[44:47], s35 offen offset:3072 sc1\n"
    "s_add_u32 s35, s35, 4096\n"
    "buffer_load_dwordx4 v[240:243], v20, s[44:47], s35 offen offset:0 sc1\n"
    "buffer_load_dwordx4 v[244:247], v20, s[44:47], s35 offen offset:1024 sc1\n"
    "buffer_load_dwordx4 v[248:251], v20, s[44:47], s35 offen offset:2048 sc1\n"
    "buffer_load_dwordx4 v[252:255], v20, s[44:47], s35 offen offset:3072 sc1\n"
    "s_waitcnt vmcnt(0)\n"
    "s_branch .LretryA40_%=\n"
    ".Lpagain49_%=:\n"
    "s_mov_b32 s62, 0x20000\n"
    "s_nop 2\n"
    ".Lpoll50_%=:\n"
    "buffer_load_dwordx2 v[244:245], v249, s[44:47], s11 offen sc1\n"
    ".Lpchk51_%=:\n"
    "s_waitcnt vmcnt(0)\n"
    "v_cmp_ne_u64 vcc, s[56:57], v[244:245]\n"
    "s_cbranch_vccz .Lpdone52_%=\n"
    "s_sleep 1\n"
    "s_sub_u32 s62, s62, 1\n"
    "s_cbranch_scc0 .Lpoll50_%=\n"
    ".Lpdone52_%=:\n"
    "s_add_u32 s35, s26, 6406144\n"
    "buffer_load_dwordx4 v[128:131], v1, s[44:47], s35 offen offset:0 sc1\n"
    "buffer_load_dwordx4 v[132:135], v1, s[44:47], s35 offen offset:1024 sc1\n"
    "buffer_load_dwordx4 v[136:139], v1, s[44:47], s35 offen offset:2048 sc1\n"
    "buffer_load_dwordx4 v[140:143], v1, s[44:47], s35 offen offset:3072 sc1\n"
    "s_add_u32 s35, s26, 6410240\n"
    "buffer_load_dwordx4 v[144:147], v1, s[44:47], s35 offen offset:0 sc1\n"
    "buffer_load_dwordx4 v[148:151], v1, s[44:47], s35 offen offset:1024 sc1\n"
    "buffer_load_dwordx4 v[152:155], v1, s[44:47], s35 offen offset:2048 sc1\n"
    "buffer_load_dwordx4 v[156:159], v1, s[44:47], s35 offen offset:3072 sc1\n"
    "s_add_u32 s35, s26, 6414336\n"
    "buffer_load_dwordx4 v[160:163], v1, s[44:47], s35 offen offset:0 sc1\n"
    "buffer_load_dwordx4 v[164:167], v1, s[44:47], s35 offen offset:1024 sc1\n"
    "buffer_load_dwordx4 v[168:171], v1, s[44:47], s35 offen offset:2048 sc1\n"
    "buffer_load_dwordx4 v[172:175], v1, s[44:47], s35 offen offset:3072 sc1\n"
    "s_add_u32 s35, s26, 6418432\n"
    "buffer_load_dwordx4 v[176:179], v1, s[44:47], s35 offen offset:0 sc1\n"
    "buffer_load_dwordx4 v[180:183], v1, s[44:47], s35 offen offset:1024 sc1\n"
    "s_waitcnt vmcnt(0)\n"
    "s_branch .Lpok48_%=\n"
    ".Lh1slow53_%=:\n"
    "s_waitcnt vmcnt(0) lgkmcnt(0)\n"
    "s_nop 2\n"
    "v_bfe_u32 v248, v0, 3, 3\n"
    "v_mul_u32_u24 v248, 2560, v248\n"
    "v_and_b32 v246, 7, v0\n"
    "v_lshl_add_u32 v248, v246, 3, v248\n"
    "s_add_u32 s59, s23, 2368\n"
    "s_mov_b32 s62, 0x20000\n"
    "s_nop 0\n"
    ".Lpoll55_%=:\n"
    "buffer_load_dwordx2 v[246:247], v248, s[44:47], s59 offen sc1\n"
    ".Lpchk56_%=:\n"
    "s_waitcnt vmcnt(0)\n"
    "v_cmp_ne_u64 vcc, s[56:57], v[246:247]\n"
    "s_cbranch_vccz .Lpdone57_%=\n"
    "s_sleep 4\n"
    "s_sub_u32 s62, s62, 1\n"
    "s_cbranch_scc0 .Lpoll55_%=\n"
    ".Lpdone57_%=:\n"
    "s_add_u32 s35, s26, 6406144\n"
    "buffer_load_dwordx4 v[128:131], v1, s[44:47], s35 offen offset:0 sc1\n"
    "buffer_load_dwordx4 v[132:135], v1, s[44:47], s35 offen offset:1024 sc1\n"
    "buffer_load_dwordx4 v[136:139], v1, s[44:47], s35 offen offset:2048 sc1\n"
    "buffer_load_dwordx4 v[140:143], v1, s[44:47], s35 offen offset:3072 sc1\n"
    "s_add_u32 s35, s26, 6410240\n"
    "buffer_load_dwordx4 v[144:147], v1, s[44:47], s35 offen offset:0 sc1\n"
    "buffer_load_dwordx4 v[148:151], v1, s[44:47], s35 offen offset:1024 sc1\n"
    "buffer_load_dwordx4 v[152:155], v1, s[44:47], s35 offen offset:2048 sc1\n"
    "buffer_load_dwordx4 v[156:159], v1, s[44:47], s35 offen offset:3072 sc1\n"
    "s_add_u32 s35, s26, 6414336\n"
    "buffer_load_dwordx4 v[160:163], v1, s[44:47], s35 offen offset:0 sc1\n"
    "buffer_load_dwordx4 v[164:167], v1, s[44:47], s35 offen offset:1024 sc1\n"
    "buffer_load_dwordx4 v[168:171], v1, s[44:47], s35 offen offset:2048 sc1\n"
    "buffer_load_dwordx4 v[172:175], v1, s[44:47], s35 offen offset:3072 sc1\n"
    "s_add_u32 s35, s26, 6418432\n"
    "buffer_load_dwordx4 v[176:179], v1, s[44:47], s35 offen offset:0 sc1\n"
    "buffer_load_dwordx4 v[180:183], v1, s[44:47], s35 offen offset:1024 sc1\n"
    "s_waitcnt vmcnt(13)\n"
    "v_mfma_f32_32x32x16_bf16 v[16:31], v[56:59], v[128:131], 0\n"
    "v_mfma_f32_32x32x16_bf16 v[32:47], v[60:63], v[128:131], 0\n"
    "s_waitcnt vmcnt(12)\n"
    "v_mfma_f32_32x32x16_bf16 v[16:31], v[64:67], v[132:135], v[16:31]\n"
    "v_mfma_f32_32x32x16_bf16 v[32:47], v[68:71], v[132:135], v[32:47]\n"
    "s_waitcnt vmcnt(11)\n"
    "v_mfma_f32_32x32x16_bf16 v[16:31], v[72:75], v[136:139], v[16:31]\n"
    "v_mfma_f32_32x32x16_bf16 v[32:47], v[76:79], v[136:139], v[32:47]\n"
    ".Lpad23_%=:\n"
    "s_cbranch_execz .Lpad24_%=\n"
    "s_waitcnt vmcnt(10)\n"
    "v_mfma_f32_32x32x16_bf16 v[16:31], v[80:83], v[140:143], v[16:31]\n"
    "v_mfma_f32_32x32x16_bf16 v[32:47], v[84:87], v[140:143], v[32:47]\n"
    "s_waitcnt vmcnt(9)\n"
    "v_mfma_f32_32x32x16_bf16 v[16:31], v[88:91], v[144:147], v[16:31]\n"
    "v_mfma_f32_32x32x16_bf16 v[32:47], v[92:95], v[144:147], v[32:47]\n"
    "s_waitcnt vmcnt(8)\n"
    "v_mfma_f32_32x32x16_bf16 v[16:31], v[96:99], v[148:151], v[16:31]\n"
    "v_mfma_f32_32x32x16_bf16 v[32:47], v[100:103], v[148:151], v[32:47]\n"
    "s_waitcnt vmcnt(7)\n"
    "v_mfma_f32_32x32x16_bf16 v[16:31], v[104:107], v[152:155], v[16:31]\n"
    "v_mfma_f32_32x32x16_bf16 v[32:47], v[108:111], v[152:155], v[32:47]\n"
    "s_waitcnt vmcnt(6)\n"
    "v_mfma_f32_32x32x16_bf16 v[16:31], v[112:115], v[156:159], v[16:31]\n"
    "v_mfma_f32_32x32x16_bf16 v[32:47], v[116:119], v[156:159], v[32:47]\n"
    "s_waitcnt vmcnt(5)\n"
    "v_mfma_f32_32x32x16_bf16 v[16:31], v[120:123], v[160:163], v[16:31]\n"
    "v_mfma_f32_32x32x16_bf16 v[32:47], v[124:127], v[160:163], v[32:47]\n"
    "s_waitcnt vmcnt(4)\n"
    "v_mfma_f32_32x32x16_bf16 v[16:31], v[184:187], v[164:167], v[16:31]\n"
    "v_mfma_f32_32x32x16_bf16 v[32:47], v[188:191], v[164:167], v[32:47]\n"
    "s_waitcnt vmcnt(3)\n"
    "v_mfma_f32_32x32x16_bf16 v[16:31], v[192:195], v[168:171], v[16:31]\n"
    "v_mfma_f32_32x32x16_bf16 v[32:47], v[196:199], v[168:171], v[32:47]\n"
    "s_waitcnt vmcnt(2)\n"
    "v_mfma_f32_32x32x16_bf16 v[16:31], v[200:203], v[172:175], v[16:31]\n"
    "v_mfma_f32_32x32x16_bf16 v[32:47], v[204:207], v[172:175], v[32:47]\n"
    "s_waitcnt vmcnt(1)\n"
    "v_mfma_f32_32x32x16_bf16 v[16:31], v[208:211], v[176:179], v[16:31]\n"
    "v_mfma_f32_32x32x16_bf16 v[32:47], v[212:215], v[176:179], v[32:47]\n"
    "s_waitcnt vmcnt(0)\n"
    "v_mfma_f32_32x32x16_bf16 v[16:31], v[216:219], v[180:183], v[16:31]\n"
    "v_mfma_f32_32x32x16_bf16 v[32:47], v[220:223], v[180:183], v[32:47]\n"
    "s_nop 11\n"
    "s_branch .Lh1cont54_%=\n"
    ".Llate81_%=:\n"
    "s_waitcnt vmcnt(0)\n"
    "s_barrier\n"
    "s_mov_b32 s62, 0x20000\n"
    "s_nop 0\n"
    ".Lpoll83_%=:\n"
    "buffer_load_dwordx2 v[8:9], v104, s[44:47], s1 offen sc1\n"
    ".Lpchk84_%=:\n"
    "s_waitcnt vmcnt(0)\n"
    "v_cmp_ne_u64 vcc, s[56:57], v[8:9]\n"
    "s_cbranch_vccz .Lpdone85_%=\n"
    "s_sleep 8\n"
    "s_sub_u32 s62, s62, 1\n"
    "s_cbranch_scc0 .Lpoll83_%=\n"
    ".Lpdone85_%=:\n"
    "s_add_u32 s58, s50, 1\n"
    "s_and_b32 s58, s58, 7\n"
    "s_lshl2_add_u32 s58, s58, s53\n"
    "s_lshl_b32 s58, s58, 12\n"
    "s_mul_i32 m0, s53, 4160\n"
    "s_add_u32 m0, m0, 16640\n"
    "s_add_u32 s58, s58, 327680\n"
    "buffer_load_dwordx4 v1, s[44:47], s58 offen sc1 lds\n"
    "s_add_u32 m0, m0, 16\n"
    "s_nop 0\n"
    "buffer_load_dwordx4 v1, s[44:47], s58 offen offset:1024 sc1 lds\n"
    "s_add_u32 m0, m0, 16\n"
    "s_nop 0\n"
    "buffer_load_dwordx4 v1, s[44:47], s58 offen offset:2048 sc1 lds\n"
    "s_add_u32 m0, m0, 16\n"
    "s_nop 0\n"
    "buffer_load_dwordx4 v1, s[44:47], s58 offen offset:3072 sc1 lds\n"
    "s_add_u32 s58, s50, 2\n"
    "s_and_b32 s58, s58, 7\n"
    "s_lshl2_add_u32 s58, s58, s53\n"
    "s_lshl_b32 s58, s58, 12\n"
    "s_mul_i32 m0, s53, 4160\n"
    "s_add_u32 m0, m0, 33280\n"
    "s_add_u32 s58, s58, 327680\n"
    "buffer_load_dwordx4 v1, s[44:47], s58 offen sc1 lds\n"
    "s_add_u32 m0, m0, 16\n"
    "s_nop 0\n"
    "buffer_load_dwordx4 v1, s[44:47], s58 offen offset:1024 sc1 lds\n"
    "s_add_u32 m0, m0, 16\n"
    "s_nop 0\n"
    "buffer_load_dwordx4 v1, s[44:47], s58 offen offset:2048 sc1 lds\n"
    "s_add_u32 m0, m0, 16\n"
    "s_nop 0\n"
    "buffer_load_dwordx4 v1, s[44:47], s58 offen offset:3072 sc1 lds\n"
    "s_branch .Lmerge82_%=\n"
    ".Lpagain93_%=:\n"
    "s_mov_b32 s62, 0x20000\n"
    "s_nop 2\n"
    ".Lpoll94_%=:\n"
    "buffer_load_dwordx2 v[14:15], v13, s[44:47], s16 offen sc1\n"
    ".Lpchk95_%=:\n"
    "s_waitcnt vmcnt(0)\n"
    "v_cmp_ne_u64 vcc, s[56:57], v[14:15]\n"
    "s_cbranch_vccz .Lpdone96_%=\n"
    "s_sleep 1\n"
    "s_sub_u32 s62, s62, 1\n"
    "s_cbranch_scc0 .Lpoll94_%=\n"
    ".Lpdone96_%=:\n"
    "v_and_b32 v11, 0x300, v1\n"
    "v_lshlrev_b32 v11, 2, v11\n"
    "v_and_b32 v12, 0xf0, v1\n"
    "s_lshl_b32 s58, s67, 13\n"
    "s_lshl_b32 s61, s53, 8\n"
    "s_add_u32 s58, s58, s61\n"
    "s_add_u32 s61, s58, 532480\n"
    "v_add3_u32 v12, v11, v12, s61\n"
    "s_lshl_b32 s58, s58, 2\n"
    "s_add_u32 s58, s58, 4227072\n"
    "v_add_u32 v11, s58, v1\n"
    "s_lshl_b32 m0, s53, 11\n"
    "s_add_u32 m0, m0, 150784\n"
    "s_lshl_b32 s58, s53, 10\n"
    "s_add_u32 s58, s58, 114688\n"
    "s_lshl_b32 s61, s98, 13\n"
    "s_movk_i32 s60, 3072\n"
    "s_mov_b32 s18, m0\n"
    "s_add_u32 m0, s58, 0\n"
    "s_nop 0\n"
    "buffer_load_dwordx4 v11, s[44:47], s61 offen sc1 lds\n"
    "s_add_u32 s61, s61, 4096\n"
    "s_add_u32 m0, s58, 4096\n"
    "s_nop 0\n"
    "buffer_load_dwordx4 v11, s[44:47], s61 offen sc1 lds\n"
    "s_add_u32 s61, s61, 4096\n"
    "s_and_b32 s61, s61, 0x7fff\n"
    "s_add_u32 m0, s58, 8192\n"
    ".Lpad24_%=:\n"
    "s_cbranch_execz .Lpad25_%=\n"
    "s_nop 0\n"
    "buffer_load_dwordx4 v11, s[44:47], s61 offen sc1 lds\n"
    "s_add_u32 s61, s61, 4096\n"
    "s_add_u32 m0, s58, 12288\n"
    "s_nop 0\n"
    "buffer_load_dwordx4 v11, s[44:47], s61 offen sc1 lds\n"
    "s_mov_b32 m0, s18\n"
    "s_nop 0\n"
    "buffer_load_dwordx4 v12, s[44:47], 0 offen sc1 lds\n"
    "buffer_load_dwordx4 v12, s[44:47], s60 offen offset:1024 sc1 lds\n"
    "s_add_u32 s61, s61, 4096\n"
    "s_and_b32 s61, s61, 0x7fff\n"
    "s_add_u32 m0, s58, 16384\n"
    "s_nop 0\n"
    "buffer_load_dwordx4 v11, s[44:47], s61 offen sc1 lds\n"
    "s_add_u32 s61, s61, 4096\n"
    "s_add_u32 m0, s58, 20480\n"
    "s_nop 0\n"
    "buffer_load_dwordx4 v11, s[44:47], s61 offen sc1 lds\n"
    "s_add_u32 s61, s61, 4096\n"
    "s_and_b32 s61, s61, 0x7fff\n"
    "s_add_u32 m0, s58, 24576\n"
    "s_nop 0\n"
    "buffer_load_dwordx4 v11, s[44:47], s61 offen sc1 lds\n"
    "s_add_u32 s61, s61, 4096\n"
    "s_add_u32 m0, s58, 28672\n"
    "s_nop 0\n"
    "buffer_load_dwordx4 v11, s[44:47], s61 offen sc1 lds\n"
    "s_waitcnt vmcnt(0)\n"
    "s_branch .Lpok92_%=\n"
    ".Lpagain99_%=:\n"
    "s_mov_b32 s62, 0x20000\n"
    "s_nop 2\n"
    ".Lpoll100_%=:\n"
    "buffer_load_dwordx2 v[8:9], v10, s[44:47], s17 offen sc1\n"
    ".Lpchk101_%=:\n"
    "s_waitcnt vmcnt(0)\n"
    "v_cmp_ne_u64 vcc, s[56:57], v[8:9]\n"
    "s_cbranch_vccz .Lpdone102_%=\n"
    "s_sleep 1\n"
    "s_sub_u32 s62, s62, 1\n"
    "s_cbranch_scc0 .Lpoll100_%=\n"
    ".Lpdone102_%=:\n"
    "s_branch .Lpok98_%=\n"
    ".Lhslow103_%=:\n"
    "s_cmp_lg_u32 s3, 0\n"
    "s_cbranch_scc1 .Lhcont104_%=\n"
    "s_mov_b32 s3, 1\n"
    "s_waitcnt vmcnt(0) lgkmcnt(0)\n"
    "s_barrier\n"
    "s_mul_i32 s58, s51, 7\n"
    "s_add_u32 s58, s58, s49\n"
    "s_lshl_b32 s58, s58, 15\n"
    "s_add_u32 s58, s58, 13631488\n"
    "s_add_u32 s58, s58, s63\n"
    "v_add_u32 v20, s58, v1\n"
    "s_lshl_b32 s35, s98, 13\n"
    "buffer_load_dwordx4 v[128:131], v20, s[44:47], s35 offen offset:0 sc1\n"
    "buffer_load_dwordx4 v[132:135], v20, s[44:47], s35 offen offset:1024 sc1\n"
    "buffer_load_dwordx4 v[136:139], v20, s[44:47], s35 offen offset:2048 sc1\n"
    "buffer_load_dwordx4 v[140:143], v20, s[44:47], s35 offen offset:3072 sc1\n"
    "s_add_u32 s35, s35, 4096\n"
    "buffer_load_dwordx4 v[144:147], v20, s[44:47], s35 offen offset:0 sc1\n"
    "buffer_load_dwordx4 v[148:151], v20, s[44:47], s35 offen offset:1024 sc1\n"
    "buffer_load_dwordx4 v[152:155], v20, s[44:47], s35 offen offset:2048 sc1\n"
    "buffer_load_dwordx4 v[156:159], v20, s[44:47], s35 offen offset:3072 sc1\n"
    "s_add_u32 s35, s35, 4096\n"
    "s_and_b32 s35, s35, 0x7fff\n"
    "buffer_load_dwordx4 v[160:163], v20, s[44:47], s35 offen offset:0 sc1\n"
    "buffer_load_dwordx4 v[164:167], v20, s[44:47], s35 offen offset:1024 sc1\n"
    "buffer_load_dwordx4 v[168:171], v20, s[44:47], s35 offen offset:2048 sc1\n"
    "buffer_load_dwordx4 v[172:175], v20, s[44:47], s35 offen offset:3072 sc1\n"
    "s_add_u32 s35, s35, 4096\n"
    "buffer_load_dwordx4 v[176:179], v20, s[44:47], s35 offen offset:0 sc1\n"
    "buffer_load_dwordx4 v[180:183], v20, s[44:47], s35 offen offset:1024 sc1\n"
    "buffer_load_dwordx4 v[184:187], v20, s[44:47], s35 offen offset:2048 sc1\n"
    "buffer_load_dwordx4 v[188:191], v20, s[44:47], s35 offen offset:3072 sc1\n"
    "s_add_u32 s35, s35, 4096\n"
    "s_and_b32 s35, s35, 0x7fff\n"
    "buffer_load_dwordx4 v[192:195], v20, s[44:47], s35 offen offset:0 sc1\n"
    "buffer_load_dwordx4 v[196:199], v20, s[44:47], s35 offen offset:1024 sc1\n"
    "buffer_load_dwordx4 v[200:203], v20, s[44:47], s35 offen offset:2048 sc1\n"
    "buffer_load_dwordx4 v[204:207], v20, s[44:47], s35 offen offset:3072 sc1\n"
    "s_add_u32 s35, s35, 4096\n"
    "buffer_load_dwordx4 v[208:211], v20, s[44:47], s35 offen offset:0 sc1\n"
    "buffer_load_dwordx4 v[212:215], v20, s[44:47], s35 offen offset:1024 sc1\n"
    "buffer_load_dwordx4 v[216:219], v20, s[44:47], s35 offen offset:2048 sc1\n"
    "buffer_load_dwordx4 v[220:223], v20, s[44:47], s35 offen offset:3072 sc1\n"
    "s_add_u32 s35, s35, 4096\n"
    "s_and_b32 s35, s35, 0x7fff\n"
    "buffer_load_dwordx4 v[224:227], v20, s[44:47], s35 offen offset:0 sc1\n"
    "buffer_load_dwordx4 v[228:231], v20, s[44:47], s35 offen offset:1024 sc1\n"
    "buffer_load_dwordx4 v[232:235], v20, s[44:47], s35 offen offset:2048 sc1\n"
    "buffer_load_dwordx4 v[236:239], v20, s[44:47], s35 offen offset:3072 sc1\n"
    "s_add_u32 s35, s35, 4096\n"
    "buffer_load_dwordx4 v[240:243], v20, s[44:47], s35 offen offset:0 sc1\n"
    "buffer_load_dwordx4 v[244:247], v20, s[44:47], s35 offen offset:1024 sc1\n"
    "buffer_load_dwordx4 v[248:251], v20, s[44:47], s35 offen offset:2048 sc1\n"
    "buffer_load_dwordx4 v[252:255], v20, s[44:47], s35 offen offset:3072 sc1\n"
    "s_add_u32 s59, s23, 2336\n"
    "v_bfe_u32 v18, v0, 2, 3\n"
    "v_mul_u32_u24 v18, 2560, v18\n"
    "v_and_b32 v19, 3, v0\n"
    "v_lshl_add_u32 v18, v19, 3, v18\n"
    "s_mov_b32 s62, 0x20000\n"
    "s_nop 1\n"
    ".Lpoll105_%=:\n"
    "buffer_load_dwordx2 v[16:17], v18, s[44:47], s59 offen sc1\n"
    ".Lpchk106_%=:\n"
    "s_waitcnt vmcnt(0)\n"
    "v_cmp_ne_u64 vcc, s[56:57], v[16:17]\n"
    "s_cbranch_vccz .Lpdone107_%=\n"
    "s_sleep 4\n"
    "s_sub_u32 s62, s62, 1\n"
    "s_cbranch_scc0 .Lpoll105_%=\n"
    ".Lpdone107_%=:\n"
    "v_and_b32 v11, 0x300, v1\n"
    "v_lshlrev_b32 v11, 2, v11\n"
    "v_and_b32 v12, 0xf0, v1\n"
    "s_lshl_b32 s58, s67, 13\n"
    "s_lshl_b32 s61, s53, 8\n"
    "s_add_u32 s58, s58, s61\n"
    "s_add_u32 s61, s58, 532480\n"
    "v_add3_u32 v12, v11, v12, s61\n"
    "s_lshl_b32 s58, s58, 2\n"
    "s_add_u32 s58, s58, 4227072\n"
    ".Lpad25_%=:\n"
    "s_cbranch_execz .Lpad26_%=\n"
    "v_add_u32 v11, s58, v1\n"
    "s_lshl_b32 m0, s53, 11\n"
    "s_add_u32 m0, m0, 150784\n"
    "s_lshl_b32 s58, s53, 10\n"
    "s_add_u32 s58, s58, 114688\n"
    "s_lshl_b32 s61, s98, 13\n"
    "s_movk_i32 s60, 3072\n"
    "s_mov_b32 s18, m0\n"
    "s_add_u32 m0, s58, 0\n"
    "s_nop 0\n"
    "buffer_load_dwordx4 v11, s[44:47], s61 offen sc1 lds\n"
    "s_add_u32 s61, s61, 4096\n"
    "s_add_u32 m0, s58, 4096\n"
    "s_nop 0\n"
    "buffer_load_dwordx4 v11, s[44:47], s61 offen sc1 lds\n"
    "s_add_u32 s61, s61, 4096\n"
    "s_and_b32 s61, s61, 0x7fff\n"
    "s_add_u32 m0, s58, 8192\n"
    "s_nop 0\n"
    "buffer_load_dwordx4 v11, s[44:47], s61 offen sc1 lds\n"
    "s_add_u32 s61, s61, 4096\n"
    "s_add_u32 m0, s58, 12288\n"
    "s_nop 0\n"
    "buffer_load_dwordx4 v11, s[44:47], s61 offen sc1 lds\n"
    "s_mov_b32 m0, s18\n"
    "s_nop 0\n"
    "buffer_load_dwordx4 v12, s[44:47], 0 offen sc1 lds\n"
    "buffer_load_dwordx4 v12, s[44:47], s60 offen offset:1024 sc1 lds\n"
    "s_add_u32 s61, s61, 4096\n"
    "s_and_b32 s61, s61, 0x7fff\n"
    "s_add_u32 m0, s58, 16384\n"
    "s_nop 0\n"
    "buffer_load_dwordx4 v11, s[44:47], s61 offen sc1 lds\n"
    "s_add_u32 s61, s61, 4096\n"
    "s_add_u32 m0, s58, 20480\n"
    "s_nop 0\n"
    "buffer_load_dwordx4 v11, s[44:47], s61 offen sc1 lds\n"
    "s_add_u32 s61, s61, 4096\n"
    "s_and_b32 s61, s61, 0x7fff\n"
    "s_add_u32 m0, s58, 24576\n"
    "s_nop 0\n"
    "buffer_load_dwordx4 v11, s[44:47], s61 offen sc1 lds\n"
    "s_add_u32 s61, s61, 4096\n"
    "s_add_u32 m0, s58, 28672\n"
    "s_nop 0\n"
    "buffer_load_dwordx4 v11, s[44:47], s61 offen sc1 lds\n"
    "s_waitcnt vmcnt(0)\n"
    "s_branch .LretryB97_%=\n"
    ".Lpagain109_%=:\n"
    "s_mov_b32 s62, 0x20000\n"
    "s_nop 2\n"
    ".Lpoll110_%=:\n"
    "buffer_load_dwordx2 v[244:245], v249, s[44:47], s11 offen sc1\n"
    ".Lpchk111_%=:\n"
    "s_waitcnt vmcnt(0)\n"
    "v_cmp_ne_u64 vcc, s[56:57], v[244:245]\n"
    "s_cbranch_vccz .Lpdone112_%=\n"
    "s_sleep 1\n"
    "s_sub_u32 s62, s62, 1\n"
    "s_cbranch_scc0 .Lpoll110_%=\n"
    ".Lpdone112_%=:\n"
    "s_add_u32 s35, s26, 6406144\n"
    "buffer_load_dwordx4 v[128:131], v1, s[44:47], s35 offen offset:0 sc1\n"
    "buffer_load_dwordx4 v[132:135], v1, s[44:47], s35 offen offset:1024 sc1\n"
    "buffer_load_dwordx4 v[136:139], v1, s[44:47], s35 offen offset:2048 sc1\n"
    "buffer_load_dwordx4 v[140:143], v1, s[44:47], s35 offen offset:3072 sc1\n"
    "s_add_u32 s35, s26, 6410240\n"
    "buffer_load_dwordx4 v[144:147], v1, s[44:47], s35 offen offset:0 sc1\n"
    "buffer_load_dwordx4 v[148:151], v1, s[44:47], s35 offen offset:1024 sc1\n"
    "buffer_load_dwordx4 v[152:155], v1, s[44:47], s35 offen offset:2048 sc1\n"
    "buffer_load_dwordx4 v[156:159], v1, s[44:47], s35 offen offset:3072 sc1\n"
    "s_add_u32 s35, s26, 6414336\n"
    "buffer_load_dwordx4 v[160:163], v1, s[44:47], s35 offen offset:0 sc1\n"
    "buffer_load_dwordx4 v[164:167], v1, s[44:47], s35 offen offset:1024 sc1\n"
    "s_waitcnt vmcnt(0)\n"
    "s_branch .Lpok108_%=\n"
    ".Lh1slow113_%=:\n"
    "s_waitcnt vmcnt(0) lgkmcnt(0)\n"
    "s_nop 7\n"
    "v_bfe_u32 v248, v0, 3, 3\n"
    "v_mul_u32_u24 v248, 2560, v248\n"
    "v_and_b32 v246, 7, v0\n"
    "v_lshl_add_u32 v248, v246, 3, v248\n"
    "s_add_u32 s59, s23, 2368\n"
    "s_mov_b32 s62, 0x20000\n"
    "s_nop 0\n"
    ".Lpoll115_%=:\n"
    "buffer_load_dwordx2 v[246:247], v248, s[44:47], s59 offen sc1\n"
    ".Lpchk116_%=:\n"
    "s_waitcnt vmcnt(0)\n"
    "v_cmp_ne_u64 vcc, s[56:57], v[246:247]\n"
    "s_cbranch_vccz .Lpdone117_%=\n"
    "s_sleep 4\n"
    "s_sub_u32 s62, s62, 1\n"
    "s_cbranch_scc0 .Lpoll115_%=\n"
    ".Lpdone117_%=:\n"
    "s_add_u32 s35, s26, 6406144\n"
    "buffer_load_dwordx4 v[128:131], v1, s[44:47], s35 offen offset:0 sc1\n"
    "buffer_load_dwordx4 v[132:135], v1, s[44:47], s35 offen offset:1024 sc1\n"
    "buffer_load_dwordx4 v[136:139], v1, s[44:47], s35 offen offset:2048 sc1\n"
    "buffer_load_dwordx4 v[140:143], v1, s[44:47], s35 offen offset:3072 sc1\n"
    "s_add_u32 s35, s26, 6410240\n"
    "buffer_load_dwordx4 v[144:147], v1, s[44:47], s35 offen offset:0 sc1\n"
    "buffer_load_dwordx4 v[148:151], v1, s[44:47], s35 offen offset:1024 sc1\n"
    "buffer_load_dwordx4 v[152:155], v1, s[44:47], s35 offen offset:2048 sc1\n"
    "buffer_load_dwordx4 v[156:159], v1, s[44:47], s35 offen offset:3072 sc1\n"
    "s_add_u32 s35, s26, 6414336\n"
    "buffer_load_dwordx4 v[160:163], v1, s[44:47], s35 offen offset:0 sc1\n"
    "buffer_load_dwordx4 v[164:167], v1, s[44:47], s35 offen offset:1024 sc1\n"
    "buffer_load_dwordx4 v[168:171], v1, s[44:47], s35 offen offset:2048 sc1\n"
    "buffer_load_dwordx4 v[172:175], v1, s[44:47], s35 offen offset:3072 sc1\n"
    "s_add_u32 s35, s26, 6418432\n"
    "buffer_load_dwordx4 v[176:179], v1, s[44:47], s35 offen offset:0 sc1\n"
    "buffer_load_dwordx4 v[180:183], v1, s[44:47], s35 offen offset:1024 sc1\n"
    "s_waitcnt vmcnt(13)\n"
    "v_mfma_f32_32x32x16_bf16 v[16:31], v[56:59], v[128:131], 0\n"
    "v_mfma_f32_32x32x16_bf16 v[32:47], v[60:63], v[128:131], 0\n"
    "s_waitcnt vmcnt(12)\n"
    "v_mfma_f32_32x32x16_bf16 v[16:31], v[64:67], v[132:135], v[16:31]\n"
    "v_mfma_f32_32x32x16_bf16 v[32:47], v[68:71], v[132:135], v[32:47]\n"
    "s_waitcnt vmcnt(11)\n"
    "v_mfma_f32_32x32x16_bf16 v[16:31], v[72:75], v[136:139], v[16:31]\n"
    "v_mfma_f32_32x32x16_bf16 v[32:47], v[76:79], v[136:139], v[32:47]\n"
    "s_waitcnt vmcnt(10)\n"
    "v_mfma_f32_32x32x16_bf16 v[16:31], v[80:83], v[140:143], v[16:31]\n"
    "v_mfma_f32_32x32x16_bf16 v[32:47], v[84:87], v[140:143], v[32:47]\n"
    ".Lpad26_%=:\n"
    "s_cbranch_execz .Lhopret_%=\n"
    "s_waitcnt vmcnt(9)\n"
    "v_mfma_f32_32x32x16_bf16 v[16:31], v[88:91], v[144:147], v[16:31]\n"
    "v_mfma_f32_32x32x16_bf16 v[32:47], v[92:95], v[144:147], v[32:47]\n"
    "s_waitcnt vmcnt(8)\n"
    "v_mfma_f32_32x32x16_bf16 v[16:31], v[96:99], v[148:151], v[16:31]\n"
    "v_mfma_f32_32x32x16_bf16 v[32:47], v[100:103], v[148:151], v[32:47]\n"
    "s_waitcnt vmcnt(7)\n"
    "v_mfma_f32_32x32x16_bf16 v[16:31], v[104:107], v[152:155], v[16:31]\n"
    "v_mfma_f32_32x32x16_bf16 v[32:47], v[108:111], v[152:155], v[32:47]\n"
    "s_waitcnt vmcnt(6)\n"
    "v_mfma_f32_32x32x16_bf16 v[16:31], v[112:115], v[156:159], v[16:31]\n"
    "v_mfma_f32_32x32x16_bf16 v[32:47], v[116:119], v[156:159], v[32:47]\n"
    "s_waitcnt vmcnt(5)\n"
    "v_mfma_f32_32x32x16_bf16 v[16:31], v[120:123], v[160:163], v[16:31]\n"
    "v_mfma_f32_32x32x16_bf16 v[32:47], v[124:127], v[160:163], v[32:47]\n"
    "s_waitcnt vmcnt(4)\n"
    "v_mfma_f32_32x32x16_bf16 v[16:31], v[184:187], v[164:167], v[16:31]\n"
    "v_mfma_f32_32x32x16_bf16 v[32:47], v[188:191], v[164:167], v[32:47]\n"
    "s_waitcnt vmcnt(3)\n"
    "v_mfma_f32_32x32x16_bf16 v[16:31], v[192:195], v[168:171], v[16:31]\n"
    "v_mfma_f32_32x32x16_bf16 v[32:47], v[196:199], v[168:171], v[32:47]\n"
    "s_waitcnt vmcnt(2)\n"
    "v_mfma_f32_32x32x16_bf16 v[16:31], v[200:203], v[172:175], v[16:31]\n"
    "v_mfma_f32_32x32x16_bf16 v[32:47], v[204:207], v[172:175], v[32:47]\n"
    "s_waitcnt vmcnt(1)\n"
    "v_mfma_f32_32x32x16_bf16 v[16:31], v[208:211], v[176:179], v[16:31]\n"
    "v_mfma_f32_32x32x16_bf16 v[32:47], v[212:215], v[176:179], v[32:47]\n"
    "s_waitcnt vmcnt(0)\n"
    "v_mfma_f32_32x32x16_bf16 v[16:31], v[216:219], v[180:183], v[16:31]\n"
    "v_mfma_f32_32x32x16_bf16 v[32:47], v[220:223], v[180:183], v[32:47]\n"
    "s_nop 10\n"
    "ds_write_b128 v224, v[16:19] offset:0\n"
    "ds_write_b128 v225, v[20:23] offset:0\n"
    "ds_write_b128 v226, v[24:27] offset:0\n"
    "ds_write_b128 v227, v[28:31] offset:0\n"
    "s_waitcnt lgkmcnt(0)\n"
    "s_branch .Lh1cont114_%=\n"
    ".Lend_%=:\n"
    ".Lfb2_%=:\n"
    "s_waitcnt vmcnt(0) lgkmcnt(0)\n"
    "v_mov_b32 v12, 0xff800000\n"
    "s_mov_b32 s58, 327680\n"
    ".Lfb1118_%=:\n"
    "buffer_load_dwordx4 v[64:67], v1, s[44:47], s58 offen sc1\n"
    "s_waitcnt vmcnt(0)\n"
    "v_mfma_f32_32x32x16_bf16 v[16:31], v[64:67], v[8:11], 0\n"
    "s_nop 11\n"
    "v_max3_f32 v12, v12, v16, v17\n"
    "v_max3_f32 v12, v12, v18, v19\n"
    "v_max3_f32 v12, v12, v20, v21\n"
    "v_max3_f32 v12, v12, v22, v23\n"
    "v_max3_f32 v12, v12, v24, v25\n"
    "v_max3_f32 v12, v12, v26, v27\n"
    "v_max3_f32 v12, v12, v28, v29\n"
    "v_max3_f32 v12, v12, v30, v31\n"
    "s_add_u32 s58, s58, 2048\n"
    "s_cmp_lt_u32 s58, 458752\n"
    "s_cbranch_scc1 .Lfb1118_%=\n"
    "v_mov_b32 v13, v12\n"
    "s_nop 1\n"
    "v_permlane32_swap_b32 v12, v13\n"
    "v_max_f32 v12, v12, v13\n"
    "v_sub_f32 v32, 0, v12\n"
    "v_mov_b32 v48, 0\n"
    "v_sub_f32 v33, 0, v12\n"
    "v_mov_b32 v49, 0\n"
    "v_sub_f32 v34, 0, v12\n"
    "v_mov_b32 v50, 0\n"
    "v_sub_f32 v35, 0, v12\n"
    "v_mov_b32 v51, 0\n"
    "v_sub_f32 v36, 0, v12\n"
    "v_mov_b32 v52, 0\n"
    "v_sub_f32 v37, 0, v12\n"
    "v_mov_b32 v53, 0\n"
    "v_sub_f32 v38, 0, v12\n"
    "v_mov_b32 v54, 0\n"
    "v_sub_f32 v39, 0, v12\n"
    "v_mov_b32 v55, 0\n"
    "v_sub_f32 v40, 0, v12\n"
    "v_mov_b32 v56, 0\n"
    "v_sub_f32 v41, 0, v12\n"
    "v_mov_b32 v57, 0\n"
    "v_sub_f32 v42, 0, v12\n"
    "v_mov_b32 v58, 0\n"
    "v_sub_f32 v43, 0, v12\n"
    "v_mov_b32 v59, 0\n"
    "v_sub_f32 v44, 0, v12\n"
    "v_mov_b32 v60, 0\n"
    "v_sub_f32 v45, 0, v12\n"
    "v_mov_b32 v61, 0\n"
    "v_sub_f32 v46, 0, v12\n"
    "v_mov_b32 v62, 0\n"
    "v_sub_f32 v47, 0, v12\n"
    "v_mov_b32 v63, 0\n"
    "v_and_b32 v13, 15, v0\n"
    "v_lshrrev_b32 v14, 5, v0\n"
    "v_lshlrev_b32 v14, 8, v14\n"
    "v_lshl_add_u32 v13, v13, 4, v14\n"
    "v_and_b32 v14, 16, v0\n"
    "v_cmp_eq_u32 vcc, 0, v14\n"
    "v_mov_b32 v14, 0x3f803f80\n"
    "s_mov_b32 s58, 327680\n"
    "s_nop 2\n"
    ".Lfb2119_%=:\n"
    "buffer_load_dwordx4 v[64:67], v1, s[44:47], s58 offen sc1\n"
    "buffer_load_dwordx4 v[68:71], v13, s[44:47], s58 offen offset:1024 sc1\n"
    "buffer_load_dwordx4 v[76:79], v13, s[44:47], s58 offen offset:1536 sc1\n"
    "s_waitcnt vmcnt(2)\n"
    "v_mfma_f32_32x32x16_bf16 v[16:31], v[64:67], v[8:11], v[32:47]\n"
    "s_waitcnt vmcnt(1)\n"
    "v_cndmask_b32 v68, v14, v68, vcc\n"
    "s_waitcnt vmcnt(0)\n"
    "v_cndmask_b32 v76, v14, v76, vcc\n"
    "v_cndmask_b32 v69, v14, v69, vcc\n"
    "v_cndmask_b32 v77, v14, v77, vcc\n"
    "v_cndmask_b32 v70, v14, v70, vcc\n"
    "v_cndmask_b32 v78, v14, v78, vcc\n"
    "v_cndmask_b32 v71, v14, v71, vcc\n"
    "v_cndmask_b32 v79, v14, v79, vcc\n"
    "s_nop 1\n"
    "v_exp_f32 v16, v16\n"
    "v_exp_f32 v17, v17\n"
    "v_exp_f32 v18, v18\n"
    "v_exp_f32 v19, v19\n"
    "v_exp_f32 v20, v20\n"
    "v_exp_f32 v21, v21\n"
    "v_exp_f32 v22, v22\n"
    "v_exp_f32 v23, v23\n"
    "v_exp_f32 v24, v24\n"
    "v_exp_f32 v25, v25\n"
    "v_exp_f32 v26, v26\n"
    "v_exp_f32 v27, v27\n"
    "v_exp_f32 v28, v28\n"
    "v_exp_f32 v29, v29\n"
    "v_exp_f32 v30, v30\n"
    "v_exp_f32 v31, v31\n"
    "v_smfmac_f32_32x32x32_bf16 v[48:63], v[68:71], v[16:23], v15\n"
    "s_nop 0\n"
    "v_smfmac_f32_32x32x32_bf16 v[48:63], v[76:79], v[24:31], v15\n"
    "s_add_u32 s58, s58, 2048\n"
    "s_cmp_lt_u32 s58, 458752\n"
    "s_cbranch_scc1 .Lfb2119_%=\n"
    "s_nop 8\n"
    "v_mov_b32 v16, v56\n"
    "v_mov_b32 v17, v56\n"
    "s_nop 1\n"
    "v_permlane32_swap_b32 v16, v17\n"
    "s_nop 2\n"
    "s_cmp_eq_u32 s54, 0\n"
    "s_cbranch_scc1 .LretA3_%=\n"
    "s_branch .LretB4_%=\n"
    : "+v"(tid)
    : "s"(__builtin_amdgcn_kernarg_segment_ptr()), "s"(blockIdx.x)
    : "v1", "v2", "v3", "v4", "v5", "v6", "v7", "v8", "v9", "v10", "v11", "v12", "v13", "v14", "v15", "v16", "v17", "v18", "v19", "v20", "v21", "v22", "v23", "v24", "v25", "v26", "v27", "v28", "v29", "v30", "v31", "v32", "v33", "v34", "v35", "v36", "v37", "v38", "v39", "v40", "v41", "v42", "v43", "v44", "v45", "v46", "v47", "v48", "v49", "v50", "v51", "v52", "v53", "v54", "v55", "v56", "v57", "v58", "v59", "v60", "v61", "v62", "v63", "v64", "v65", "v66", "v67", "v68", "v69", "v70", "v71", "v72", "v73", "v74", "v75", "v76", "v77", "v78", "v79", "v80", "v81", "v82", "v83", "v84", "v85", "v86", "v87", "v88", "v89", "v90", "v91", "v92", "v93", "v94", "v95", "v96", "v97", "v98", "v99", "v100", "v101", "v102", "v103", "v104", "v105", "v106", "v107", "v108", "v109", "v110", "v111", "v112", "v113", "v114", "v115", "v116", "v117", "v118", "v119", "v120", "v121", "v122", "v123", "v124", "v125", "v126", "v127", "v128", "v129", "v130", "v131", "v132", "v133", "v134", "v135", "v136", "v137", "v138", "v139", "v140", "v141", "v142", "v143", "v144", "v145", "v146", "v147", "v148", "v149", "v150", "v151", "v152", "v153", "v154", "v155", "v156", "v157", "v158", "v159", "v160", "v161", "v162", "v163", "v164", "v165", "v166", "v167", "v168", "v169", "v170", "v171", "v172", "v173", "v174", "v175", "v176", "v177", "v178", "v179", "v180", "v181", "v182", "v183", "v184", "v185", "v186", "v187", "v188", "v189", "v190", "v191", "v192", "v193", "v194", "v195", "v196", "v197", "v198", "v199", "v200", "v201", "v202", "v203", "v204", "v205", "v206", "v207", "v208", "v209", "v210", "v211", "v212", "v213", "v214", "v215", "v216", "v217", "v218", "v219", "v220", "v221", "v222", "v223", "v224", "v225", "v226", "v227", "v228", "v229", "v230", "v231", "v232", "v233", "v234", "v235", "v236", "v237", "v238", "v239", "v240", "v241", "v242", "v243", "v244", "v245", "v246", "v247", "v248", "v249", "v250", "v251", "v252", "v253", "v254", "v255", "s3", "s4", "s5", "s6", "s7", "s8", "s9", "s10", "s11", "s12", "s13", "s14", "s15", "s16", "s17", "s18", "s19", "s20", "s21", "s22", "s23", "s24", "s25", "s26", "s27", "s28", "s29", "s30", "s31", "s32", "s33", "s34", "s35", "s36", "s37", "s38", "s39", "s40", "s41", "s42", "s43", "s44", "s45", "s46", "s47", "s48", "s49", "s50", "s51", "s52", "s53", "s54", "s55", "s56", "s57", "s58", "s59", "s60", "s61", "s62", "s63", "s64", "s65", "s66", "s67", "s68", "s69", "s70", "s71", "s72", "s73", "s74", "s75", "s76", "s77", "s78", "s79", "s80", "s81", "s82", "s83", "s84", "s85", "s86", "s87", "s88", "s89", "s90", "s91", "s92", "s93", "s94", "s95", "s96", "s97", "s98", "s99", "vcc", "scc", "memory");
}

extern "C" void kernel_launch(void* const* d_in, const int* in_sizes, int n_in,
                              void* d_out, int out_size, void* d_ws, size_t ws_size,
                              hipStream_t stream) {
  Args a;
  const int idx[9] = {0, 1, 2, 3, 4, 6, 7, 8, 9};
  for (int i = 0; i < 9; ++i) { a.d[i].p = d_in[idx[i]]; a.d[i].n = (unsigned)in_sizes[idx[i]] * 2u; a.d[i].f = 0x00020000u; }
  a.d[9].p = d_out; a.d[9].n = (unsigned)out_size * 2u; a.d[9].f = 0x00020000u;
  a.d[10].p = (char*)d_ws + ((size_t)0 << 20); a.d[10].n = 0x1400000u; a.d[10].f = 0x00020000u;
  block_kernel<<<256, 512, 0, stream>>>(a);
}
```
